```python
import torch

def _softmax(x, axis=-1):
    return torch.softmax(x, dim=axis)
def _gelu(x):
    return torch.nn.functional.gelu(x, approximate="tanh")
def _silu(x):
    return torch.nn.functional.silu(x)
def _sigmoid(x):
    return torch.sigmoid(x)
def _layernorm(x, axis=-1, eps=1e-5):
    m = x.mean(dim=axis, keepdim=True)
    v = x.var(dim=axis, keepdim=True, unbiased=False)
    return (x - m) / torch.sqrt(v + eps)
def _scatter_add(tgt, idx, src, axis=0):
    return tgt.clone().index_add_(axis, idx, src)
def _segment_sum(src, seg, n):
    out = torch.zeros((n,) + src.shape[1:], dtype=src.dtype)
    return out.index_add_(0, seg, src)
def _pad(x, pw):
    flat = tuple(v for pair in reversed(pw) for v in pair)
    return torch.nn.functional.pad(x, flat)

kernel_name = 'recurrent_scan__mha_block_cached__swiglu_ffn__prompt'

def setup_inputs():

    _w = (lambda a: (lambda t: t.to(torch.bfloat16) if t.is_floating_point() else t)(torch.as_tensor(a)))
    def _A_setup():
        B, Sq, D = 1, 2048, 512
        H, Sk, Dh = 32, 2048, 128
        return {
            "x": _w((lambda *s: torch.randn(*s, dtype=torch.bfloat16) * 0.05)(B, Sq, D)),
            "k_cache": _w((lambda *s: torch.randn(*s, dtype=torch.bfloat16) * 0.8)(B, H, Sk, Dh)),
            "v_cache": _w((lambda *s: torch.randn(*s, dtype=torch.bfloat16) * 0.2)(B, H, Sk, Dh)),
            "wq": _w((lambda *s: torch.randn(*s, dtype=torch.bfloat16) * 0.8)(D, H * Dh) * (D ** -0.5)),
            "wo": _w((lambda *s: torch.randn(*s, dtype=torch.bfloat16) * 0.2)(H * Dh, D) * ((H * Dh) ** -0.5)),
        }
    _A_in = _A_setup()

    def _B_setup():
        B, S, D, F = 1, 2048, 512, 1792
        return {
            "x": _w((lambda *s: torch.randn(*s, dtype=torch.bfloat16) * 0.05)(B, S, D)),
            "w_gate": _w((lambda *s: torch.randn(*s, dtype=torch.bfloat16) * 0.1)(D, F)),
            "w_up": _w((lambda *s: torch.randn(*s, dtype=torch.bfloat16) * 0.1)(D, F)),
            "w_down": _w((lambda *s: torch.randn(*s, dtype=torch.bfloat16) * 0.1)(F, D)),
        }
    _B_in = _B_setup()

    return {
        "A_x": _A_in["x"],
        "A_k_cache": _A_in["k_cache"],
        "A_v_cache": _A_in["v_cache"],
        "A_wq": _A_in["wq"],
        "A_wo": _A_in["wo"],
        "B_x": _B_in["x"],
        "B_w_gate": _B_in["w_gate"],
        "B_w_up": _B_in["w_up"],
        "B_w_down": _B_in["w_down"],
    }

def reference(A_x, A_k_cache, A_v_cache, A_wq, A_wo, B_x, B_w_gate, B_w_up, B_w_down):
    def _A_ref(x, k_cache, v_cache, wq, wo):
        B, Sq, D = x.shape
        H, Dh = 32, 128
        q = (lambda x, *d: x.permute(*d))((x @ wq).reshape(B, Sq, H, Dh), 0, 2, 1, 3)
        scores = torch.einsum("bhqd,bhkd->bhqk", q, k_cache) * (Dh ** -0.5)
        p = _softmax(scores, axis=-1)
        o = torch.einsum("bhqk,bhkd->bhqd", p, v_cache)
        return (lambda x, *d: x.permute(*d))(o, 0, 2, 1, 3).reshape(B, Sq, H * Dh) @ wo

    def _B_ref(x, w_gate, w_up, w_down):
        g = _silu(x @ w_gate)
        u = x @ w_up
        return (g * u) @ w_down

    x = A_x
    xn = _layernorm(x, axis=-1)
    s = _A_ref(xn, A_k_cache, A_v_cache, A_wq, A_wo)
    g = _silu(_B_ref(xn, B_w_gate, B_w_up, B_w_down))
    return x + s * g
```

```cpp
#include <hip/hip_runtime.h>
#include <hip/hip_bf16.h>
typedef __attribute__((ext_vector_type(8))) short bf16x8;
typedef __attribute__((ext_vector_type(16))) float f32x16;
typedef unsigned short u16;
__device__ inline float bf2f(u16 x){ return __uint_as_float(((unsigned)x)<<16); }
__device__ inline u16 f2bf(float f){ __hip_bfloat16 b = __float2bfloat16(f); return *(u16*)&b; }
__global__ void ln_k(const u16* x, u16* xn){
  int row = blockIdx.x; int t = threadIdx.x;
  float v[8]; float s=0;
  for(int i=0;i<8;i++){ v[i]=bf2f(x[row*512+t*8+i]); s+=v[i]; }
  for(int o=32;o;o>>=1) s+=__shfl_xor(s,o);
  float m=s/512.f; float q=0;
  for(int i=0;i<8;i++){ float d=v[i]-m; q+=d*d; }
  for(int o=32;o;o>>=1) q+=__shfl_xor(q,o);
  float rs=1.f/sqrtf(q/512.f+1e-5f);
  for(int i=0;i<8;i++) xn[row*512+t*8+i]=f2bf((v[i]-m)*rs);
}
__global__ void gemm_k(const u16* A, const u16* B, float* C, int M,int N,int K){
  int lane=threadIdx.x; int r=lane&31,h=lane>>5;
  int m0=blockIdx.y*32,n0=blockIdx.x*32;
  f32x16 acc={};
  for(int k=0;k<K;k+=16){
    bf16x8 a=*(const bf16x8*)(A+(size_t)(m0+r)*K+k+8*h);
    bf16x8 b; for(int j=0;j<8;j++) b[j]=B[(size_t)(k+8*h+j)*N+n0+r];
    acc=__builtin_amdgcn_mfma_f32_32x32x16_bf16(a,b,acc,0,0,0);
  }
  for(int i=0;i<16;i++){ int row=(i&3)+8*(i>>2)+4*h; C[(size_t)(m0+row)*N+n0+r]=acc[i]; }
}
__global__ void cvt_k(const float* a,u16* b,int n){ int i=blockIdx.x*256+threadIdx.x; if(i<n) b[i]=f2bf(a[i]); }
__global__ void swiglu_k(const float* g,const float* u,u16* h,int n){ int i=blockIdx.x*256+threadIdx.x; if(i<n){ float x=g[i]; h[i]=f2bf(x/(1.f+__expf(-x))*u[i]); } }
__global__ void attn_k(const u16* q,const u16* K,const u16* V,u16* O){
  int row=blockIdx.x, hd=blockIdx.y, lane=threadIdx.x;
  __shared__ float qs[128]; __shared__ float p[2048];
  qs[lane]=bf2f(q[row*4096+hd*128+lane]); qs[lane+64]=bf2f(q[row*4096+hd*128+lane+64]);
  __syncthreads();
  float mx=-1e30f;
  for(int k=lane;k<2048;k+=64){ float s=0; const u16* kr=K+((size_t)hd*2048+k)*128; for(int d=0;d<128;d++) s+=qs[d]*bf2f(kr[d]); s*=0.08838834764f; p[k]=s; mx=fmaxf(mx,s);}
  for(int o=32;o;o>>=1) mx=fmaxf(mx,__shfl_xor(mx,o));
  float l=0; for(int k=lane;k<2048;k+=64){ float e=__expf(p[k]-mx); p[k]=e; l+=e; }
  for(int o=32;o;o>>=1) l+=__shfl_xor(l,o);
  __syncthreads();
  float a0=0,a1=0;
  for(int k=0;k<2048;k++){ const u16* vr=V+((size_t)hd*2048+k)*128; a0+=p[k]*bf2f(vr[lane]); a1+=p[k]*bf2f(vr[lane+64]); }
  O[row*4096+hd*128+lane]=f2bf(a0/l); O[row*4096+hd*128+lane+64]=f2bf(a1/l);
}
__global__ void fin_k(const u16* x,const float* s,const float* f,u16* out,int n){ int i=blockIdx.x*256+threadIdx.x; if(i<n){ float y=f[i]; out[i]=f2bf(bf2f(x[i])+s[i]*(y/(1.f+__expf(-y)))); } }
extern "C" void kernel_launch(void* const* d_in, const int* in_sizes, int n_in, void* d_out, int out_size, void* d_ws, size_t ws_size, hipStream_t stream) {
  const u16* x=(const u16*)d_in[0]; const u16* K=(const u16*)d_in[1]; const u16* V=(const u16*)d_in[2];
  const u16* wq=(const u16*)d_in[3]; const u16* wo=(const u16*)d_in[4];
  const u16* wg=(const u16*)d_in[6]; const u16* wu=(const u16*)d_in[7]; const u16* wd=(const u16*)d_in[8];
  char* ws=(char*)d_ws;
  u16* xn=(u16*)ws; float* t0=(float*)(ws+(4<<20)); float* t1=(float*)(ws+(40<<20)); u16* q=(u16*)(ws+(80<<20)); u16* O=(u16*)(ws+(100<<20)); u16* h=(u16*)(ws+(120<<20));
  float* s=(float*)(ws+(130<<20)); float* f=(float*)(ws+(140<<20));
  ln_k<<<2048,64,0,stream>>>(x,xn);
  gemm_k<<<dim3(128,64),64,0,stream>>>(xn,wq,t0,2048,4096,512);
  cvt_k<<<2048*4096/256,256,0,stream>>>(t0,q,2048*4096);
  attn_k<<<dim3(2048,32),64,0,stream>>>(q,K,V,O);
  gemm_k<<<dim3(16,64),64,0,stream>>>(O,wo,s,2048,512,4096);
  gemm_k<<<dim3(56,64),64,0,stream>>>(xn,wg,t0,2048,1792,512);
  gemm_k<<<dim3(56,64),64,0,stream>>>(xn,wu,t1,2048,1792,512);
  swiglu_k<<<2048*1792/256,256,0,stream>>>(t0,t1,h,2048*1792);
  gemm_k<<<dim3(16,64),64,0,stream>>>(h,wd,f,2048,512,1792);
  fin_k<<<2048*512/256,256,0,stream>>>(x,s,f,(u16*)d_out,2048*512);
}
```

```cpp
#include <hip/hip_runtime.h>
extern "C" __global__ __launch_bounds__(256,1) void fused_block(const void* x, const void* K, const void* V, const void* wq, const void* wo, const void* wg, const void* wu, const void* wd, void* out, void* ws){
  __shared__ __attribute__((aligned(16))) char lds[163840];
  asm volatile(
    "s_load_dwordx16 s[36:51], %0, 0\n"
    "s_load_dwordx4 s[52:55], %0, 64\n"
    "s_mov_b32 s24, %1\n"
    "s_mov_b32 s25, %2\n"
    "v_mbcnt_lo_u32_b32 v181, -1, 0\n"
    "v_mbcnt_hi_u32_b32 v181, -1, v181\n"
    "v_lshlrev_b32 v180, 4, v181\n"
    "s_and_b32 s26, s24, 7\n"
    "s_lshl_b32 s26, s26, 2\n"
    "s_bfe_u32 s28, s24, 0x20003\n"
    "s_add_u32 s26, s26, s28\n"
    "s_lshr_b32 s27, s24, 5\n"
    "s_lshl_b32 s22, s25, 12\n"
    "s_lshl_b32 s23, s25, 10\n"
    "s_lshl_b32 s85, s25, 16\n"
    "s_mov_b32 s21, 0\n"
    "s_lshl_b32 s90, s27, 4\n"
    "s_lshl_b32 s28, s25, 2\n"
    "s_add_u32 s90, s90, s28\n"
    "s_lshl_b32 s91, s90, 14\n"
    "s_lshl_b32 s28, s25, 14\n"
    "s_add_u32 s18, s28, 65536\n"
    "s_add_u32 s19, s28, 32768\n"
    "s_add_u32 s29, s28, 98304\n"
    "s_cmp_ge_u32 s25, 2\n"
    "s_cselect_b32 s19, s29, s19\n"
    "s_lshl_b32 s92, s27, 13\n"
    "s_lshl_b32 s28, s26, 5\n"
    "s_add_u32 s92, s92, s28\n"
    "v_bfe_u32 v184, v181, 4, 2\n"
    "v_xor_b32 v184, v184, v181\n"
    "v_and_b32 v184, 7, v184\n"
    "v_lshlrev_b32 v184, 4, v184\n"
    "v_lshrrev_b32 v185, 3, v181\n"
    "v_lshl_add_u32 v184, v185, 10, v184\n"
    "v_xor_b32 v185, 64, v184\n"
    "v_add_u32 v167, 3072, v184\n"
    "v_add_u32 v168, 10240, v185\n"
    "v_add_u32 v169, 1152, v184\n"
    "v_add_u32 v170, 8320, v185\n"
    "s_mov_b32 s33, 8192\n"
    "s_cmp_lt_u32 s26, 28\n"
    "s_cselect_b32 s33, 3584, s33\n"
    "s_cselect_b32 s30, 7, 8\n"
    "s_lshl_b32 s30, s26, s30\n"
    "v_bfe_u32 v184, v181, 2, 3\n"
    "v_mul_u32_u24 v184, s33, v184\n"
    "v_lshrrev_b32 v185, 5, v181\n"
    "v_lshl_add_u32 v184, v185, 6, v184\n"
    "v_lshrrev_b32 v185, 1, v181\n"
    "v_xor_b32 v185, s25, v185\n"
    "v_and_b32 v185, 1, v185\n"
    "v_lshl_add_u32 v184, v185, 5, v184\n"
    "v_and_b32 v185, 1, v181\n"
    "v_lshl_add_u32 v178, v185, 4, v184\n"
    "s_lshl_b32 s28, s27, 13\n"
    "v_mov_b32 v130, s28\n"
    "s_mul_i32 s16, s33, s25\n"
    "s_lshl_b32 s16, s16, 3\n"
    "v_lshlrev_b32 v128, 5, v181\n"
    "s_lshl_b32 s12, s27, 18\n"
    "s_lshl_b32 s13, s26, 13\n"
    "s_add_u32 s12, s12, s13\n"
    "s_lshl_b32 s13, s25, 11\n"
    "s_add_u32 s12, s12, s13\n"
    "s_add_u32 s12, s12, 3072\n"
    "s_waitcnt lgkmcnt(0)\n"
    "s_add_u32 s68, s36, 0\n"
    "s_addc_u32 s69, s37, 0\n"
    "s_sub_u32 s68, s68, 3072\n"
    "s_subb_u32 s69, s69, 0\n"
    "s_mov_b32 s70, 2100224\n"
    "s_mov_b32 s71, 0x20000\n"
    "s_add_u32 s82, s54, 33556480\n"
    "s_addc_u32 s83, s55, 0\n"
    "buffer_load_dwordx4 v[32:35], v128, s[68:71], s12 offen\n"
    "buffer_load_dwordx4 v[36:39], v128, s[68:71], s12 offen offset:16\n"
    "global_load_dwordx2 v[132:133], v130, s[82:83] offset:-2048 sc1\n"
    "s_add_u32 m0, s18, 0\n"
    "s_add_u32 s84, s91, 0\n"
    "buffer_load_dwordx4 v167, s[68:71], s84 offen offset:0 lds\n"
    "buffer_load_dwordx4 v168, s[68:71], s84 offen offset:1024 lds\n"
    "buffer_load_dwordx4 v169, s[68:71], s84 offen offset:2048 lds\n"
    "buffer_load_dwordx4 v170, s[68:71], s84 offen offset:3072 lds\n"
    "s_add_u32 m0, s18, 4096\n"
    "s_add_u32 s84, s91, 16384\n"
    "buffer_load_dwordx4 v167, s[68:71], s84 offen offset:0 lds\n"
    "buffer_load_dwordx4 v168, s[68:71], s84 offen offset:1024 lds\n"
    "buffer_load_dwordx4 v169, s[68:71], s84 offen offset:2048 lds\n"
    "buffer_load_dwordx4 v170, s[68:71], s84 offen offset:3072 lds\n"
    "s_add_u32 m0, s18, 8192\n"
    "s_add_u32 s84, s91, 32768\n"
    "buffer_load_dwordx4 v167, s[68:71], s84 offen offset:0 lds\n"
    "buffer_load_dwordx4 v168, s[68:71], s84 offen offset:1024 lds\n"
    "buffer_load_dwordx4 v169, s[68:71], s84 offen offset:2048 lds\n"
    "buffer_load_dwordx4 v170, s[68:71], s84 offen offset:3072 lds\n"
    "s_add_u32 m0, s18, 12288\n"
    "s_add_u32 s84, s91, 49152\n"
    "buffer_load_dwordx4 v167, s[68:71], s84 offen offset:0 lds\n"
    "buffer_load_dwordx4 v168, s[68:71], s84 offen offset:1024 lds\n"
    "buffer_load_dwordx4 v169, s[68:71], s84 offen offset:2048 lds\n"
    "buffer_load_dwordx4 v170, s[68:71], s84 offen offset:3072 lds\n"
    "s_cmp_lt_u32 s26, 28\n"
    "s_cselect_b32 s60, s46, s42\n"
    "s_cselect_b32 s61, s47, s43\n"
    "s_cselect_b32 s64, s48, s42\n"
    "s_cselect_b32 s65, s49, s43\n"
    "s_cselect_b32 s31, 0, 1048576\n"
    "s_add_u32 s60, s60, s30\n"
    "s_addc_u32 s61, s61, 0\n"
    "s_add_u32 s31, s31, s30\n"
    "s_add_u32 s64, s64, s31\n"
    "s_addc_u32 s65, s65, 0\n"
    "s_lshl_b32 s28, s33, 9\n"
    "s_sub_u32 s62, s28, s30\n"
    "s_sub_u32 s66, s28, s31\n"
    "s_mov_b32 s63, 0x20000\n"
    "s_mov_b32 s67, 0x20000\n"
    "s_add_u32 m0, s23, 0\n"
    "s_mul_i32 s28, s33, 0\n"
    "s_add_u32 s84, s16, s28\n"
    "buffer_load_dwordx4 v178, s[60:63], s84 offen lds\n"
    "s_add_u32 m0, s23, 4096\n"
    "s_mul_i32 s28, s33, 32\n"
    "s_add_u32 s84, s16, s28\n"
    "buffer_load_dwordx4 v178, s[60:63], s84 offen lds\n"
    "s_add_u32 m0, s23, 8192\n"
    "s_mul_i32 s28, s33, 64\n"
    "s_add_u32 s84, s16, s28\n"
    "buffer_load_dwordx4 v178, s[60:63], s84 offen lds\n"
    "s_add_u32 m0, s23, 12288\n"
    "s_mul_i32 s28, s33, 96\n"
    "s_add_u32 s84, s16, s28\n"
    "buffer_load_dwordx4 v178, s[60:63], s84 offen lds\n"
    "s_add_u32 m0, s23, 16384\n"
    "s_mul_i32 s28, s33, 0\n"
    "s_add_u32 s84, s16, s28\n"
    "buffer_load_dwordx4 v178, s[64:67], s84 offen lds\n"
    "s_add_u32 m0, s23, 20480\n"
    "s_mul_i32 s28, s33, 32\n"
    "s_add_u32 s84, s16, s28\n"
    "buffer_load_dwordx4 v178, s[64:67], s84 offen lds\n"
    "s_add_u32 m0, s23, 24576\n"
    "s_mul_i32 s28, s33, 64\n"
    "s_add_u32 s84, s16, s28\n"
    "buffer_load_dwordx4 v178, s[64:67], s84 offen lds\n"
    "s_add_u32 m0, s23, 28672\n"
    "s_mul_i32 s28, s33, 96\n"
    "s_add_u32 s84, s16, s28\n"
    "buffer_load_dwordx4 v178, s[64:67], s84 offen lds\n"
    "s_getpc_b64 s[30:31]\n"
    "Lwpc%=:\n"
    "s_lshr_b32 s28, s24, 3\n"
    "s_and_b32 s28, s28, 7\n"
    "s_lshl_b32 s28, s28, 2\n"
    "s_add_u32 s28, s28, s25\n"
    "s_lshl_b32 s28, s28, 11\n"
    "s_min_u32 s28, s28, Lend%=-Lwpc%=-2048\n"
    "s_add_u32 s30, s30, s28\n"
    "s_addc_u32 s31, s31, 0\n"
    "s_and_b32 s30, s30, 0xfffffffc\n"
    "v_and_b32 v184, 15, v181\n"
    "v_lshlrev_b32 v184, 7, v184\n"
    "global_load_dword v191, v184, s[30:31]\n"
    "s_lshl_b32 s28, s26, 8\n"
    "s_add_u32 s56, s42, s28\n"
    "s_addc_u32 s57, s43, 0\n"
    "s_mov_b32 s58, 0\n"
    "s_mov_b32 s59, 0x20000\n"
    "s_sub_u32 s58, 4194304, s28\n"
    "v_bfe_u32 v184, v181, 1, 4\n"
    "v_mul_u32_u24 v185, s33, v184\n"
    "v_lshlrev_b32 v184, 13, v184\n"
    "v_and_b32 v186, 1, v181\n"
    "v_lshl_add_u32 v184, v186, 7, v184\n"
    "s_lshl_b32 s30, s90, 2\n"
    "s_mul_i32 s31, s30, s33\n"
    "s_lshl_b32 s30, s30, 13\n"
    "buffer_load_dword v152, v185, s[60:63], s31 offen\n"
    "buffer_load_dword v153, v185, s[64:67], s31 offen\n"
    "v_and_b32 v184, 15, v181\n"
    "v_lshrrev_b32 v185, 1, v184\n"
    "v_lshrrev_b32 v186, 4, v181\n"
    "v_xor_b32 v185, v185, v186\n"
    "v_lshlrev_b32 v185, 4, v185\n"
    "v_lshl_add_u32 v184, v184, 7, v185\n"
    "v_add_u32 v164, s18, v184\n"
    "v_xor_b32 v175, 64, v164\n"
    "v_lshrrev_b32 v184, 4, v181\n"
    "v_lshlrev_b32 v184, 10, v184\n"
    "v_bfe_u32 v185, v181, 2, 2\n"
    "v_lshl_add_u32 v184, v185, 6, v184\n"
    "v_bfe_u32 v185, v181, 4, 1\n"
    "v_lshlrev_b32 v185, 5, v185\n"
    "v_and_b32 v186, 3, v181\n"
    "v_lshl_add_u32 v173, v186, 3, v184\n"
    "v_add_u32 v173, v173, v185\n"
    "v_xor_b32 v174, 32, v173\n"
    "v_lshlrev_b32 v186, 4, v186\n"
    "v_xor_b32 v186, v186, v185\n"
    "v_add_u32 v184, v184, v186\n"
    "v_add_u32 v176, 0x10000, v184\n"
    "s_and_b32 s28, s25, 1\n"
    "s_lshl_b32 s28, s28, 5\n"
    "v_xor_b32 v179, s28, v173\n"
    "s_lshr_b32 s28, s25, 1\n"
    "s_lshl_b32 s28, s28, 9\n"
    "v_add_u32 v179, s28, v179\n"
    "s_and_b32 s29, s25, 1\n"
    "s_lshl_b32 s29, s29, 3\n"
    "s_add_u32 s17, s28, s29\n"
    "v_mov_b32 v160, 0x3f803f80\n"
    "v_mov_b32 v161, 0x3f803f80\n"
    "v_mov_b32 v162, 0x3f803f80\n"
    "v_mov_b32 v163, 0x3f803f80\n"
    "v_and_b32 v183, 1, v181\n"
    "v_mul_u32_u24 v183, 0xaaaa, v183\n"
    "v_add_u32 v183, 0x4444, v183\n"
    "v_lshl_or_b32 v183, v183, 16, v183\n"
    "v_mov_b32 v144, 0\n"
    "v_mov_b32 v145, 0\n"
    "v_mov_b32 v146, 0\n"
    "v_mov_b32 v147, 0\n"
    "v_mov_b32 v148, 0\n"
    "v_mov_b32 v149, 0\n"
    "v_mov_b32 v150, 0\n"
    "v_mov_b32 v151, 0\n"
    "s_lshl_b32 s93, s25, 6\n"
    "s_add_u32 s93, s93, 0x20000\n"
    "s_lshl_b32 s28, s26, 19\n"
    "s_add_u32 s8, s38, s28\n"
    "s_addc_u32 s9, s39, 0\n"
    "s_sub_u32 s8, s8, 3072\n"
    "s_subb_u32 s9, s9, 0\n"
    "s_mov_b32 s10, 527360\n"
    "s_mov_b32 s11, 0x20000\n"
    "s_add_u32 s12, s40, s28\n"
    "s_addc_u32 s13, s41, 0\n"
    "s_sub_u32 s12, s12, 1024\n"
    "s_subb_u32 s13, s13, 0\n"
    "s_mov_b32 s14, 525312\n"
    "s_mov_b32 s15, 0x20000\n"
    "s_lshl_b32 s28, s26, 8\n"
    "s_add_u32 s56, s42, s28\n"
    "s_addc_u32 s57, s43, 0\n"
    "s_mov_b32 s58, 0\n"
    "s_mov_b32 s59, 0x20000\n"
    "s_sub_u32 s58, 4194304, s28\n"
    "s_add_u32 s72, s54, 2097152\n"
    "s_addc_u32 s73, s55, 0\n"
    "s_mov_b32 s74, 7340032\n"
    "s_mov_b32 s75, 0x20000\n"
    "s_add_u32 s76, s54, 16777216\n"
    "s_addc_u32 s77, s55, 0\n"
    "s_mov_b32 s78, 16777216\n"
    "s_mov_b32 s79, 0x20000\n"
    "s_cmp_lt_u32 s26, 28\n"
    "s_cselect_b32 s74, s74, 0\n"
    "s_mul_i32 s28, s90, 57344\n"
    "s_lshl_b32 s29, s26, 11\n"
    "s_add_u32 s86, s28, s29\n"
    "s_add_u32 s87, s86, 57344\n"
    "s_add_u32 s88, s86, 114688\n"
    "s_add_u32 s89, s86, 172032\n"
    "s_mov_b32 s28, 8192\n"
    "v_bfe_u32 v184, v181, 2, 3\n"
    "v_mul_u32_u24 v184, s28, v184\n"
    "v_lshrrev_b32 v185, 5, v181\n"
    "v_lshl_add_u32 v184, v185, 6, v184\n"
    "v_lshrrev_b32 v185, 1, v181\n"
    "v_xor_b32 v185, s25, v185\n"
    "v_and_b32 v185, 1, v185\n"
    "v_lshl_add_u32 v184, v185, 5, v184\n"
    "v_and_b32 v185, 1, v181\n"
    "v_lshl_add_u32 v177, v185, 4, v184\n"
    "s_lshr_b32 s33, s26, 3\n"
    "s_and_b32 s28, s26, 3\n"
    "s_lshl_b32 s28, s28, 8\n"
    "s_and_b32 s29, s33, 1\n"
    "s_lshl_b32 s29, s29, 7\n"
    "s_add_u32 s35, s28, s29\n"
    "s_lshl_b32 s46, s25, 15\n"
    "s_lshl_b32 s47, s33, 20\n"
    "s_lshl_b32 s28, s25, 14\n"
    "s_add_u32 s47, s47, s28\n"
    "s_bfe_u32 s28, s26, 0x10002\n"
    "s_lshl_b32 s28, s28, 3\n"
    "s_lshl_b32 s29, s27, 4\n"
    "s_add_u32 s34, s28, s29\n"
    "s_lshl_b32 s28, s25, 1\n"
    "s_add_u32 s28, s28, s34\n"
    "s_lshl_b32 s29, s28, 17\n"
    "s_lshl_b32 s30, s33, 15\n"
    "s_add_u32 s49, s29, s30\n"
    "s_lshr_b32 s28, s33, 1\n"
    "s_lshl_b32 s28, s28, 2\n"
    "s_add_u32 s34, s34, s28\n"
    "s_add_u32 s28, s34, s25\n"
    "s_mul_i32 s48, s28, 57344\n"
    "s_lshl_b32 s28, s27, 3\n"
    "s_and_b32 s29, s26, 7\n"
    "s_add_u32 s28, s28, s29\n"
    "s_lshl_b32 s28, s28, 17\n"
    "s_add_u32 s28, s28, 41943040\n"
    "s_add_u32 s40, s54, s28\n"
    "s_addc_u32 s41, s55, 0\n"
    "s_mov_b32 s42, 131072\n"
    "s_mov_b32 s43, 0x20000\n"
    "s_lshl_b32 s28, s25, 15\n"
    "s_lshl_b32 s29, s33, 13\n"
    "s_add_u32 s97, s28, s29\n"
    "s_add_u32 s99, s97, 4096\n"
    "s_lshl_b32 s28, s33, 15\n"
    "s_lshl_b32 s29, s25, 11\n"
    "s_add_u32 s98, s28, s29\n"
    "s_and_b32 s28, s25, 2\n"
    "s_add_u32 s28, s28, s34\n"
    "s_lshl_b32 s28, s28, 14\n"
    "s_add_u32 s28, s28, s35\n"
    "s_and_b32 s29, s25, 1\n"
    "s_lshl_b32 s29, s29, 6\n"
    "s_add_u32 s28, s28, s29\n"
    "s_add_u32 s36, s36, s28\n"
    "s_addc_u32 s37, s37, 0\n"
    "s_add_u32 s52, s52, s28\n"
    "s_addc_u32 s53, s53, 0\n"
    "s_lshl_b32 s28, s27, 11\n"
    "s_add_u32 s34, s54, s28\n"
    "s_addc_u32 s35, s55, 0\n"
    "s_waitcnt vmcnt(27)\n"
    "v_readfirstlane_b32 s80, v132\n"
    "v_readfirstlane_b32 s81, v133\n"
    "s_add_u32 s80, s80, 2654435761\n"
    "s_addc_u32 s81, s81, 2135587861\n"
    "v_lshlrev_b32 v96, 16, v32\n"
    "v_and_b32 v97, 0xffff0000, v32\n"
    "v_lshlrev_b32 v98, 16, v33\n"
    "v_and_b32 v99, 0xffff0000, v33\n"
    "v_lshlrev_b32 v100, 16, v34\n"
    "v_and_b32 v101, 0xffff0000, v34\n"
    "v_lshlrev_b32 v102, 16, v35\n"
    "v_and_b32 v103, 0xffff0000, v35\n"
    "v_lshlrev_b32 v104, 16, v36\n"
    "v_and_b32 v105, 0xffff0000, v36\n"
    "v_lshlrev_b32 v106, 16, v37\n"
    "v_and_b32 v107, 0xffff0000, v37\n"
    "v_lshlrev_b32 v108, 16, v38\n"
    "v_and_b32 v109, 0xffff0000, v38\n"
    "v_lshlrev_b32 v110, 16, v39\n"
    "v_and_b32 v111, 0xffff0000, v39\n"
    "v_pk_add_f32 v[80:81], v[96:97], v[98:99]\n"
    "v_pk_add_f32 v[82:83], v[100:101], v[102:103]\n"
    "v_pk_add_f32 v[80:81], v[80:81], v[104:105]\n"
    "v_pk_add_f32 v[82:83], v[82:83], v[106:107]\n"
    "v_pk_add_f32 v[80:81], v[80:81], v[108:109]\n"
    "v_pk_add_f32 v[82:83], v[82:83], v[110:111]\n"
    "v_pk_add_f32 v[80:81], v[80:81], v[82:83]\n"
    "v_add_f32 v80, v80, v81\n"
    "s_nop 1\n"
    "v_add_f32_dpp v80, v80, v80 quad_perm:[1,0,3,2] row_mask:0xf bank_mask:0xf\n"
    "s_nop 1\n"
    "v_add_f32_dpp v80, v80, v80 quad_perm:[2,3,0,1] row_mask:0xf bank_mask:0xf\n"
    "s_nop 1\n"
    "v_add_f32_dpp v80, v80, v80 row_ror:4 row_mask:0xf bank_mask:0xf\n"
    "s_nop 1\n"
    "v_add_f32_dpp v80, v80, v80 row_ror:8 row_mask:0xf bank_mask:0xf\n"
    "v_mov_b32 v81, v80\n"
    "s_nop 1\n"
    "v_permlane16_swap_b32 v80, v81\n"
    "v_add_f32 v80, v80, v81\n"
    "v_mul_f32 v84, 0xbb000000, v80\n"
    "v_pk_add_f32 v[96:97], v[96:97], v[84:85] op_sel_hi:[1,0]\n"
    "v_pk_add_f32 v[98:99], v[98:99], v[84:85] op_sel_hi:[1,0]\n"
    "v_pk_add_f32 v[100:101], v[100:101], v[84:85] op_sel_hi:[1,0]\n"
    "v_pk_add_f32 v[102:103], v[102:103], v[84:85] op_sel_hi:[1,0]\n"
    "v_pk_add_f32 v[104:105], v[104:105], v[84:85] op_sel_hi:[1,0]\n"
    "v_pk_add_f32 v[106:107], v[106:107], v[84:85] op_sel_hi:[1,0]\n"
    "v_pk_add_f32 v[108:109], v[108:109], v[84:85] op_sel_hi:[1,0]\n"
    "v_pk_add_f32 v[110:111], v[110:111], v[84:85] op_sel_hi:[1,0]\n"
    "v_pk_mul_f32 v[80:81], v[96:97], v[96:97]\n"
    "v_pk_mul_f32 v[82:83], v[98:99], v[98:99]\n"
    "v_pk_fma_f32 v[80:81], v[100:101], v[100:101], v[80:81]\n"
    "v_pk_fma_f32 v[82:83], v[102:103], v[102:103], v[82:83]\n"
    "v_pk_fma_f32 v[80:81], v[104:105], v[104:105], v[80:81]\n"
    "v_pk_fma_f32 v[82:83], v[106:107], v[106:107], v[82:83]\n"
    "v_pk_fma_f32 v[80:81], v[108:109], v[108:109], v[80:81]\n"
    "v_pk_fma_f32 v[82:83], v[110:111], v[110:111], v[82:83]\n"
    "v_pk_add_f32 v[80:81], v[80:81], v[82:83]\n"
    "v_add_f32 v80, v80, v81\n"
    "s_nop 1\n"
    "v_add_f32_dpp v80, v80, v80 quad_perm:[1,0,3,2] row_mask:0xf bank_mask:0xf\n"
    "s_nop 1\n"
    "v_add_f32_dpp v80, v80, v80 quad_perm:[2,3,0,1] row_mask:0xf bank_mask:0xf\n"
    "s_nop 1\n"
    "v_add_f32_dpp v80, v80, v80 row_ror:4 row_mask:0xf bank_mask:0xf\n"
    "s_nop 1\n"
    "v_add_f32_dpp v80, v80, v80 row_ror:8 row_mask:0xf bank_mask:0xf\n"
    "v_mov_b32 v81, v80\n"
    "s_nop 1\n"
    "v_permlane16_swap_b32 v80, v81\n"
    "v_add_f32 v80, v80, v81\n"
    "v_mov_b32 v82, 0x3727c5ac\n"
    "v_fmac_f32 v82, 0x3b000000, v80\n"
    "v_rsq_f32 v85, v82\n"
    "s_lshl_b32 s28, s26, 6\n"
    "s_lshl_b32 s29, s25, 4\n"
    "s_add_u32 s28, s28, s29\n"
    "v_lshrrev_b32 v73, 2, v181\n"
    "v_add_u32 v73, s28, v73\n"
    "s_mov_b32 exec_lo, 1\n"
    "s_mov_b32 exec_hi, 1\n"
    "global_store_dwordx2 v73, v[84:85], s[34:35] sc1\n"
    "s_mov_b64 exec, -1\n"
    "s_waitcnt vmcnt(24)\n"
    "ds_read_b128 a[0:3], v164 offset:0\n"
    "ds_read_b128 a[4:7], v175 offset:0\n"
    "ds_read_b128 a[8:11], v164 offset:2048\n"
    "ds_read_b128 a[12:15], v175 offset:2048\n"
    "s_waitcnt vmcnt(20)\n"
    "ds_read_b128 a[32:35], v164 offset:4096\n"
    "ds_read_b128 a[36:39], v175 offset:4096\n"
    "ds_read_b128 a[40:43], v164 offset:6144\n"
    "ds_read_b128 a[44:47], v175 offset:6144\n"
    "s_waitcnt vmcnt(16)\n"
    "ds_read_b128 a[128:131], v164 offset:8192\n"
    "ds_read_b128 a[132:135], v175 offset:8192\n"
    "ds_read_b128 a[136:139], v164 offset:10240\n"
    "ds_read_b128 a[140:143], v175 offset:10240\n"
    "s_waitcnt vmcnt(12)\n"
    "ds_read_b128 a[160:163], v164 offset:12288\n"
    "ds_read_b128 a[164:167], v175 offset:12288\n"
    "ds_read_b128 a[168:171], v164 offset:14336\n"
    "ds_read_b128 a[172:175], v175 offset:14336\n"
    "s_add_u32 m0, s19, 0\n"
    "s_add_u32 s84, s91, 256\n"
    "buffer_load_dwordx4 v167, s[68:71], s84 offen offset:0 lds\n"
    "buffer_load_dwordx4 v168, s[68:71], s84 offen offset:1024 lds\n"
    "buffer_load_dwordx4 v169, s[68:71], s84 offen offset:2048 lds\n"
    "buffer_load_dwordx4 v170, s[68:71], s84 offen offset:3072 lds\n"
    "s_add_u32 m0, s19, 4096\n"
    "s_add_u32 s84, s91, 16640\n"
    "buffer_load_dwordx4 v167, s[68:71], s84 offen offset:0 lds\n"
    "buffer_load_dwordx4 v168, s[68:71], s84 offen offset:1024 lds\n"
    "buffer_load_dwordx4 v169, s[68:71], s84 offen offset:2048 lds\n"
    "buffer_load_dwordx4 v170, s[68:71], s84 offen offset:3072 lds\n"
    "s_add_u32 m0, s19, 8192\n"
    "s_add_u32 s84, s91, 33024\n"
    "buffer_load_dwordx4 v167, s[68:71], s84 offen offset:0 lds\n"
    "buffer_load_dwordx4 v168, s[68:71], s84 offen offset:1024 lds\n"
    "buffer_load_dwordx4 v169, s[68:71], s84 offen offset:2048 lds\n"
    "buffer_load_dwordx4 v170, s[68:71], s84 offen offset:3072 lds\n"
    "s_add_u32 m0, s19, 12288\n"
    "s_add_u32 s84, s91, 49408\n"
    "buffer_load_dwordx4 v167, s[68:71], s84 offen offset:0 lds\n"
    "buffer_load_dwordx4 v168, s[68:71], s84 offen offset:1024 lds\n"
    "buffer_load_dwordx4 v169, s[68:71], s84 offen offset:2048 lds\n"
    "buffer_load_dwordx4 v170, s[68:71], s84 offen offset:3072 lds\n"
    "s_waitcnt vmcnt(20)\n"
    "s_waitcnt lgkmcnt(0)\n"
    "s_barrier\n"
    "s_sub_u32 s28, s19, s18\n"
    "v_add_u32 v164, s28, v164\n"
    "v_add_u32 v175, s28, v175\n"
    "s_lshl_b32 s28, s25, 9\n"
    "s_add_u32 s34, s34, s28\n"
    "s_addc_u32 s35, s35, 0\n"
    "s_cmp_ge_u32 s26, 28\n"
    "s_cbranch_scc1 Lnoffn%=\n"
    "ds_read_b64_tr_b16 v[192:193], v173 offset:0\n"
    "ds_read_b64_tr_b16 v[194:195], v173 offset:256\n"
    "ds_read_b64_tr_b16 v[196:197], v173 offset:4096\n"
    "ds_read_b64_tr_b16 v[198:199], v173 offset:4352\n"
    "ds_read_b64_tr_b16 v[200:201], v173 offset:8192\n"
    "ds_read_b64_tr_b16 v[202:203], v173 offset:8448\n"
    "ds_read_b64_tr_b16 v[204:205], v173 offset:12288\n"
    "ds_read_b64_tr_b16 v[206:207], v173 offset:12544\n"
    "s_waitcnt lgkmcnt(0)\n"
    "v_mfma_f32_16x16x32_bf16 v[64:67], v[192:195], a[0:3], 0\n"
    "ds_read_b64_tr_b16 v[208:209], v174 offset:0\n"
    "v_mfma_f32_16x16x32_bf16 v[64:67], v[196:199], a[4:7], v[64:67]\n"
    "s_add_u32 m0, s23, 65536\n"
    "ds_read_b64_tr_b16 v[210:211], v174 offset:256\n"
    "v_mfma_f32_16x16x32_bf16 v[64:67], v[200:203], a[8:11], v[64:67]\n"
    "s_add_u32 s84, s16, 458752\n"
    "ds_read_b64_tr_b16 v[212:213], v174 offset:4096\n"
    "v_mfma_f32_16x16x32_bf16 v[64:67], v[204:207], a[12:15], v[64:67]\n"
    "ds_read_b64_tr_b16 v[214:215], v174 offset:4352\n"
    "v_mfma_f32_16x16x32_bf16 v[80:83], v[204:207], a[44:47], 0\n"
    "buffer_load_dwordx4 v178, s[60:63], s84 offen lds\n"
    "v_mfma_f32_16x16x32_bf16 v[80:83], v[200:203], a[40:43], v[80:83]\n"
    "s_add_u32 m0, s23, 69632\n"
    "ds_read_b64_tr_b16 v[216:217], v174 offset:8192\n"
    "v_mfma_f32_16x16x32_bf16 v[80:83], v[196:199], a[36:39], v[80:83]\n"
    "s_add_u32 s84, s16, 573440\n"
    "ds_read_b64_tr_b16 v[218:219], v174 offset:8448\n"
    "v_mfma_f32_16x16x32_bf16 v[80:83], v[192:195], a[32:35], v[80:83]\n"
    "ds_read_b64_tr_b16 v[220:221], v174 offset:12288\n"
    "v_mfma_f32_16x16x32_bf16 v[96:99], v[192:195], a[128:131], 0\n"
    "ds_read_b64_tr_b16 v[222:223], v174 offset:12544\n"
    "v_mfma_f32_16x16x32_bf16 v[96:99], v[196:199], a[132:135], v[96:99]\n"
    "buffer_load_dwordx4 v178, s[60:63], s84 offen lds\n"
    "v_mfma_f32_16x16x32_bf16 v[96:99], v[200:203], a[136:139], v[96:99]\n"
    "s_add_u32 m0, s23, 73728\n"
    "ds_read_b64_tr_b16 v[224:225], v173 offset:512\n"
    "v_mfma_f32_16x16x32_bf16 v[96:99], v[204:207], a[140:143], v[96:99]\n"
    "s_add_u32 s84, s16, 688128\n"
    "ds_read_b64_tr_b16 v[226:227], v173 offset:768\n"
    "v_mfma_f32_16x16x32_bf16 v[112:115], v[204:207], a[172:175], 0\n"
    "buffer_load_dwordx4 v178, s[60:63], s84 offen lds\n"
    "v_mfma_f32_16x16x32_bf16 v[112:115], v[200:203], a[168:171], v[112:115]\n"
    "s_add_u32 m0, s23, 77824\n"
    "ds_read_b64_tr_b16 v[228:229], v173 offset:4608\n"
    "v_mfma_f32_16x16x32_bf16 v[112:115], v[196:199], a[164:167], v[112:115]\n"
    "s_add_u32 s84, s16, 802816\n"
    "ds_read_b64_tr_b16 v[230:231], v173 offset:4864\n"
    "v_mfma_f32_16x16x32_bf16 v[112:115], v[192:195], a[160:163], v[112:115]\n"
    "s_waitcnt lgkmcnt(4)\n"
    "buffer_load_dwordx4 v178, s[60:63], s84 offen lds\n"
    "v_mfma_f32_16x16x32_bf16 v[116:119], v[208:211], a[160:163], 0\n"
    "s_add_u32 m0, s23, 81920\n"
    "ds_read_b64_tr_b16 v[232:233], v173 offset:8704\n"
    "v_mfma_f32_16x16x32_bf16 v[116:119], v[212:215], a[164:167], v[116:119]\n"
    "s_add_u32 s84, s16, 458752\n"
    "ds_read_b64_tr_b16 v[234:235], v173 offset:8960\n"
    "v_mfma_f32_16x16x32_bf16 v[116:119], v[216:219], a[168:171], v[116:119]\n"
    "buffer_load_dwordx4 v178, s[64:67], s84 offen lds\n"
    "v_mfma_f32_16x16x32_bf16 v[116:119], v[220:223], a[172:175], v[116:119]\n"
    "s_add_u32 m0, s23, 86016\n"
    "ds_read_b64_tr_b16 v[236:237], v173 offset:12800\n"
    "v_mfma_f32_16x16x32_bf16 v[100:103], v[220:223], a[140:143], 0\n"
    "s_add_u32 s84, s16, 573440\n"
    "ds_read_b64_tr_b16 v[238:239], v173 offset:13056\n"
    "v_mfma_f32_16x16x32_bf16 v[100:103], v[216:219], a[136:139], v[100:103]\n"
    "buffer_load_dwordx4 v178, s[64:67], s84 offen lds\n"
    "v_mfma_f32_16x16x32_bf16 v[100:103], v[212:215], a[132:135], v[100:103]\n"
    "s_add_u32 m0, s23, 90112\n"
    "ds_read_b64_tr_b16 v[128:129], v179 offset:0\n"
    "v_mfma_f32_16x16x32_bf16 v[100:103], v[208:211], a[128:131], v[100:103]\n"
    "s_add_u32 s84, s16, 688128\n"
    "ds_read_b64_tr_b16 v[130:131], v179 offset:256\n"
    "v_mfma_f32_16x16x32_bf16 v[84:87], v[208:211], a[32:35], 0\n"
    "buffer_load_dwordx4 v178, s[64:67], s84 offen lds\n"
    "v_mfma_f32_16x16x32_bf16 v[84:87], v[212:215], a[36:39], v[84:87]\n"
    "s_add_u32 m0, s23, 94208\n"
    "ds_read_b64_tr_b16 v[132:133], v179 offset:4096\n"
    "v_mfma_f32_16x16x32_bf16 v[84:87], v[216:219], a[40:43], v[84:87]\n"
    "s_add_u32 s84, s16, 802816\n"
    "ds_read_b64_tr_b16 v[134:135], v179 offset:4352\n"
    "v_mfma_f32_16x16x32_bf16 v[84:87], v[220:223], a[44:47], v[84:87]\n"
    "buffer_load_dwordx4 v178, s[64:67], s84 offen lds\n"
    "v_mfma_f32_16x16x32_bf16 v[68:71], v[220:223], a[12:15], 0\n"
    "s_waitcnt vmcnt(20)\n"
    "ds_read_b64_tr_b16 v[136:137], v179 offset:8192\n"
    "v_mfma_f32_16x16x32_bf16 v[68:71], v[216:219], a[8:11], v[68:71]\n"
    "ds_read_b128 a[16:19], v164 offset:0\n"
    "ds_read_b128 a[20:23], v175 offset:0\n"
    "v_mfma_f32_16x16x32_bf16 v[68:71], v[212:215], a[4:7], v[68:71]\n"
    "ds_read_b128 a[24:27], v164 offset:2048\n"
    "v_mfma_f32_16x16x32_bf16 v[68:71], v[208:211], a[0:3], v[68:71]\n"
    "s_waitcnt lgkmcnt(5)\n"
    "ds_read_b128 a[28:31], v175 offset:2048\n"
    "v_mfma_f32_16x16x32_bf16 v[72:75], v[224:227], a[0:3], 0\n"
    "ds_read_b64_tr_b16 v[138:139], v179 offset:8448\n"
    "v_mfma_f32_16x16x32_bf16 v[72:75], v[228:231], a[4:7], v[72:75]\n"
    "ds_read_b64_tr_b16 v[140:141], v179 offset:12288\n"
    "v_mfma_f32_16x16x32_bf16 v[72:75], v[232:235], a[8:11], v[72:75]\n"
    "ds_read_b64_tr_b16 v[142:143], v179 offset:12544\n"
    "v_mfma_f32_16x16x32_bf16 v[72:75], v[236:239], a[12:15], v[72:75]\n"
    "s_waitcnt lgkmcnt(3)\n"
    "ds_read_b64_tr_b16 v[240:241], v174 offset:512\n"
    "v_mfma_f32_16x16x32_bf16 v[88:91], v[236:239], a[44:47], 0\n"
    "s_add_u32 m0, s19, 0\n"
    "ds_read_b64_tr_b16 v[242:243], v174 offset:768\n"
    "v_mfma_f32_16x16x32_bf16 v[88:91], v[232:235], a[40:43], v[88:91]\n"
    "s_add_u32 s84, s91, 512\n"
    "ds_read_b64_tr_b16 v[244:245], v174 offset:4608\n"
    "v_mfma_f32_16x16x32_bf16 v[88:91], v[228:231], a[36:39], v[88:91]\n"
    "ds_read_b64_tr_b16 v[246:247], v174 offset:4864\n"
    "v_mfma_f32_16x16x32_bf16 v[88:91], v[224:227], a[32:35], v[88:91]\n"
    "ds_read_b64_tr_b16 v[248:249], v174 offset:8704\n"
    "v_mfma_f32_16x16x32_bf16 v[104:107], v[224:227], a[128:131], 0\n"
    "ds_read_b64_tr_b16 v[250:251], v174 offset:8960\n"
    "v_mfma_f32_16x16x32_bf16 v[104:107], v[228:231], a[132:135], v[104:107]\n"
    "buffer_load_dwordx4 v167, s[68:71], s84 offen offset:0 lds\n"
    "v_mfma_f32_16x16x32_bf16 v[104:107], v[232:235], a[136:139], v[104:107]\n"
    "ds_read_b64_tr_b16 v[252:253], v174 offset:12800\n"
    "v_mfma_f32_16x16x32_bf16 v[104:107], v[236:239], a[140:143], v[104:107]\n"
    "ds_read_b64_tr_b16 v[254:255], v174 offset:13056\n"
    "v_mfma_f32_16x16x32_bf16 v[120:123], v[236:239], a[172:175], 0\n"
    "buffer_load_dwordx4 v168, s[68:71], s84 offen offset:1024 lds\n"
    "v_mfma_f32_16x16x32_bf16 v[120:123], v[232:235], a[168:171], v[120:123]\n"
    "buffer_load_dwordx4 v169, s[68:71], s84 offen offset:2048 lds\n"
    "v_mfma_f32_16x16x32_bf16 v[120:123], v[228:231], a[164:167], v[120:123]\n"
    "buffer_load_dwordx4 v170, s[68:71], s84 offen offset:3072 lds\n"
    "v_mfma_f32_16x16x32_bf16 v[120:123], v[224:227], a[160:163], v[120:123]\n"
    "s_waitcnt lgkmcnt(2)\n"
    "ds_read_b64_tr_b16 v[192:193], v173 offset:16384\n"
    "v_smfmac_f32_16x16x64_bf16 v[144:147], v[160:163], v[128:135], v183\n"
    "s_waitcnt vmcnt(20)\n"
    "ds_read_b64_tr_b16 v[194:195], v173 offset:16640\n"
    "v_smfmac_f32_16x16x64_bf16 v[144:147], v[160:163], v[136:143], v183\n"
    "ds_read_b128 a[48:51], v164 offset:4096\n"
    "v_mfma_f32_16x16x32_bf16 v[124:127], v[240:243], a[160:163], 0\n"
    "ds_read_b128 a[52:55], v175 offset:4096\n"
    "v_mfma_f32_16x16x32_bf16 v[124:127], v[244:247], a[164:167], v[124:127]\n"
    "ds_read_b128 a[56:59], v164 offset:6144\n"
    "v_mfma_f32_16x16x32_bf16 v[124:127], v[248:251], a[168:171], v[124:127]\n"
    "s_waitcnt lgkmcnt(5)\n"
    "ds_read_b128 a[60:63], v175 offset:6144\n"
    "v_mfma_f32_16x16x32_bf16 v[124:127], v[252:255], a[172:175], v[124:127]\n"
    "ds_read_b64_tr_b16 v[196:197], v173 offset:20480\n"
    "v_mfma_f32_16x16x32_bf16 v[108:111], v[252:255], a[140:143], 0\n"
    "ds_read_b64_tr_b16 v[198:199], v173 offset:20736\n"
    "v_mfma_f32_16x16x32_bf16 v[108:111], v[248:251], a[136:139], v[108:111]\n"
    "ds_read_b64_tr_b16 v[200:201], v173 offset:24576\n"
    "v_mfma_f32_16x16x32_bf16 v[108:111], v[244:247], a[132:135], v[108:111]\n"
    "s_waitcnt lgkmcnt(3)\n"
    "ds_read_b64_tr_b16 v[202:203], v173 offset:24832\n"
    "v_mfma_f32_16x16x32_bf16 v[108:111], v[240:243], a[128:131], v[108:111]\n"
    "ds_read_b64_tr_b16 v[204:205], v173 offset:28672\n"
    "v_mfma_f32_16x16x32_bf16 v[92:95], v[240:243], a[32:35], 0\n"
    "ds_read_b64_tr_b16 v[206:207], v173 offset:28928\n"
    "v_mfma_f32_16x16x32_bf16 v[92:95], v[244:247], a[36:39], v[92:95]\n"
    "ds_read_b64_tr_b16 v[208:209], v174 offset:16384\n"
    "v_mfma_f32_16x16x32_bf16 v[92:95], v[248:251], a[40:43], v[92:95]\n"
    "s_add_u32 m0, s19, 4096\n"
    "ds_read_b64_tr_b16 v[210:211], v174 offset:16640\n"
    "v_mfma_f32_16x16x32_bf16 v[92:95], v[252:255], a[44:47], v[92:95]\n"
    "s_add_u32 s84, s91, 16896\n"
    "ds_read_b64_tr_b16 v[212:213], v174 offset:20480\n"
    "v_mfma_f32_16x16x32_bf16 v[76:79], v[252:255], a[12:15], 0\n"
    "buffer_load_dwordx4 v167, s[68:71], s84 offen offset:0 lds\n"
    "v_mfma_f32_16x16x32_bf16 v[76:79], v[248:251], a[8:11], v[76:79]\n"
    "buffer_load_dwordx4 v168, s[68:71], s84 offen offset:1024 lds\n"
    "v_mfma_f32_16x16x32_bf16 v[76:79], v[244:247], a[4:7], v[76:79]\n"
    "ds_read_b64_tr_b16 v[214:215], v174 offset:20736\n"
    "v_mfma_f32_16x16x32_bf16 v[76:79], v[240:243], a[0:3], v[76:79]\n"
    "s_waitcnt lgkmcnt(3)\n"
    "buffer_load_dwordx4 v169, s[68:71], s84 offen offset:2048 lds\n"
    "v_mfma_f32_16x16x32_bf16 v[0:3], v[192:195], a[0:3], 0\n"
    "ds_read_b64_tr_b16 v[216:217], v174 offset:24576\n"
    "v_mfma_f32_16x16x32_bf16 v[0:3], v[196:199], a[4:7], v[0:3]\n"
    "ds_read_b64_tr_b16 v[218:219], v174 offset:24832\n"
    "v_mfma_f32_16x16x32_bf16 v[0:3], v[200:203], a[8:11], v[0:3]\n"
    "buffer_load_dwordx4 v170, s[68:71], s84 offen offset:3072 lds\n"
    "v_mfma_f32_16x16x32_bf16 v[0:3], v[204:207], a[12:15], v[0:3]\n"
    "ds_read_b64_tr_b16 v[220:221], v174 offset:28672\n"
    "v_mfma_f32_16x16x32_bf16 v[16:19], v[204:207], a[44:47], 0\n"
    "ds_read_b64_tr_b16 v[222:223], v174 offset:28928\n"
    "v_mfma_f32_16x16x32_bf16 v[16:19], v[200:203], a[40:43], v[16:19]\n"
    "ds_read_b64_tr_b16 v[128:129], v179 offset:16384\n"
    "v_mfma_f32_16x16x32_bf16 v[16:19], v[196:199], a[36:39], v[16:19]\n"
    "s_waitcnt vmcnt(20)\n"
    "ds_read_b64_tr_b16 v[130:131], v179 offset:16640\n"
    "v_mfma_f32_16x16x32_bf16 v[16:19], v[192:195], a[32:35], v[16:19]\n"
    "ds_read_b128 a[144:147], v164 offset:8192\n"
    "v_mfma_f32_16x16x32_bf16 v[32:35], v[192:195], a[128:131], 0\n"
    "ds_read_b128 a[148:151], v175 offset:8192\n"
    "v_mfma_f32_16x16x32_bf16 v[32:35], v[196:199], a[132:135], v[32:35]\n"
    "ds_read_b128 a[152:155], v164 offset:10240\n"
    "v_mfma_f32_16x16x32_bf16 v[32:35], v[200:203], a[136:139], v[32:35]\n"
    "ds_read_b128 a[156:159], v175 offset:10240\n"
    "v_mfma_f32_16x16x32_bf16 v[32:35], v[204:207], a[140:143], v[32:35]\n"
    "ds_read_b64_tr_b16 v[132:133], v179 offset:20480\n"
    "v_mfma_f32_16x16x32_bf16 v[48:51], v[204:207], a[172:175], 0\n"
    "ds_read_b64_tr_b16 v[134:135], v179 offset:20736\n"
    "v_mfma_f32_16x16x32_bf16 v[48:51], v[200:203], a[168:171], v[48:51]\n"
    "ds_read_b64_tr_b16 v[136:137], v179 offset:24576\n"
    "v_mfma_f32_16x16x32_bf16 v[48:51], v[196:199], a[164:167], v[48:51]\n"
    "s_waitcnt lgkmcnt(3)\n"
    "ds_read_b64_tr_b16 v[138:139], v179 offset:24832\n"
    "v_mfma_f32_16x16x32_bf16 v[48:51], v[192:195], a[160:163], v[48:51]\n"
    "s_waitcnt lgkmcnt(5)\n"
    "ds_read_b64_tr_b16 v[140:141], v179 offset:28672\n"
    "v_mfma_f32_16x16x32_bf16 v[52:55], v[208:211], a[160:163], 0\n"
    "ds_read_b64_tr_b16 v[142:143], v179 offset:28928\n"
    "v_mfma_f32_16x16x32_bf16 v[52:55], v[212:215], a[164:167], v[52:55]\n"
    "ds_read_b64_tr_b16 v[224:225], v173 offset:16896\n"
    "v_add_u32 v179, 0x10000, v179\n"
    "v_mfma_f32_16x16x32_bf16 v[52:55], v[216:219], a[168:171], v[52:55]\n"
    "s_add_u32 m0, s19, 8192\n"
    "ds_read_b64_tr_b16 v[226:227], v173 offset:17152\n"
    "v_mfma_f32_16x16x32_bf16 v[52:55], v[220:223], a[172:175], v[52:55]\n"
    "s_add_u32 s84, s91, 33280\n"
    "ds_read_b64_tr_b16 v[228:229], v173 offset:20992\n"
    "v_mfma_f32_16x16x32_bf16 v[36:39], v[220:223], a[140:143], 0\n"
    "buffer_load_dwordx4 v167, s[68:71], s84 offen offset:0 lds\n"
    "v_mfma_f32_16x16x32_bf16 v[36:39], v[216:219], a[136:139], v[36:39]\n"
    "ds_read_b64_tr_b16 v[230:231], v173 offset:21248\n"
    "v_mfma_f32_16x16x32_bf16 v[36:39], v[212:215], a[132:135], v[36:39]\n"
    "ds_read_b64_tr_b16 v[232:233], v173 offset:25088\n"
    "v_mfma_f32_16x16x32_bf16 v[36:39], v[208:211], a[128:131], v[36:39]\n"
    "ds_read_b64_tr_b16 v[234:235], v173 offset:25344\n"
    "v_mfma_f32_16x16x32_bf16 v[20:23], v[208:211], a[32:35], 0\n"
    "ds_read_b64_tr_b16 v[236:237], v173 offset:29184\n"
    "v_mfma_f32_16x16x32_bf16 v[20:23], v[212:215], a[36:39], v[20:23]\n"
    "ds_read_b64_tr_b16 v[238:239], v173 offset:29440\n"
    "v_mfma_f32_16x16x32_bf16 v[20:23], v[216:219], a[40:43], v[20:23]\n"
    "buffer_load_dwordx4 v168, s[68:71], s84 offen offset:1024 lds\n"
    "v_mfma_f32_16x16x32_bf16 v[20:23], v[220:223], a[44:47], v[20:23]\n"
    "buffer_load_dwordx4 v169, s[68:71], s84 offen offset:2048 lds\n"
    "v_mfma_f32_16x16x32_bf16 v[4:7], v[220:223], a[12:15], 0\n"
    "buffer_load_dwordx4 v170, s[68:71], s84 offen offset:3072 lds\n"
    "v_mfma_f32_16x16x32_bf16 v[4:7], v[216:219], a[8:11], v[4:7]\n"
    "ds_read_b64_tr_b16 v[240:241], v174 offset:16896\n"
    "v_mfma_f32_16x16x32_bf16 v[4:7], v[212:215], a[4:7], v[4:7]\n"
    "s_waitcnt vmcnt(20)\n"
    "ds_read_b64_tr_b16 v[242:243], v174 offset:17152\n"
    "v_mfma_f32_16x16x32_bf16 v[4:7], v[208:211], a[0:3], v[4:7]\n"
    "s_waitcnt lgkmcnt(2)\n"
    "ds_read_b128 a[176:179], v164 offset:12288\n"
    "v_smfmac_f32_16x16x64_bf16 v[148:151], v[160:163], v[128:135], v183\n"
    "ds_read_b128 a[180:183], v175 offset:12288\n"
    "v_smfmac_f32_16x16x64_bf16 v[148:151], v[160:163], v[136:143], v183\n"
    "ds_read_b128 a[184:187], v164 offset:14336\n"
    "v_mfma_f32_16x16x32_bf16 v[8:11], v[224:227], a[0:3], 0\n"
    "ds_read_b64_tr_b16 v[244:245], v174 offset:20992\n"
    "v_mfma_f32_16x16x32_bf16 v[8:11], v[228:231], a[4:7], v[8:11]\n"
    "ds_read_b64_tr_b16 v[246:247], v174 offset:21248\n"
    "v_mfma_f32_16x16x32_bf16 v[8:11], v[232:235], a[8:11], v[8:11]\n"
    "ds_read_b64_tr_b16 v[248:249], v174 offset:25088\n"
    "v_mfma_f32_16x16x32_bf16 v[8:11], v[236:239], a[12:15], v[8:11]\n"
    "ds_read_b64_tr_b16 v[250:251], v174 offset:25344\n"
    "v_mfma_f32_16x16x32_bf16 v[24:27], v[236:239], a[44:47], 0\n"
    "ds_read_b64_tr_b16 v[252:253], v174 offset:29184\n"
    "ds_read_b64_tr_b16 v[254:255], v174 offset:29440\n"
    "v_mfma_f32_16x16x32_bf16 v[24:27], v[232:235], a[40:43], v[24:27]\n"
    "ds_read_b128 a[188:191], v175 offset:14336\n"
    "v_add_u32 v173, 0x10000, v173\n"
    "v_mfma_f32_16x16x32_bf16 v[24:27], v[228:231], a[36:39], v[24:27]\n"
    "v_add_u32 v174, 0x10000, v174\n"
    "v_mfma_f32_16x16x32_bf16 v[24:27], v[224:227], a[32:35], v[24:27]\n"
    "v_mfma_f32_16x16x32_bf16 v[40:43], v[224:227], a[128:131], 0\n"
    "v_mfma_f32_16x16x32_bf16 v[40:43], v[228:231], a[132:135], v[40:43]\n"
    "s_waitcnt vmcnt(12) lgkmcnt(1)\n"
    "s_waitcnt lgkmcnt(0)\n"
    "v_mfma_f32_16x16x32_bf16 v[40:43], v[232:235], a[136:139], v[40:43]\n"
    "s_barrier\n"
    "s_cmp_lg_u32 s25, 0\n"
    "s_cbranch_scc1 Lpb_z%=\n"
    "v_mov_b32 v184, s92\n"
    "v_mov_b32 v186, s80\n"
    "v_mov_b32 v187, s81\n"
    "s_mov_b64 exec, 1\n"
    "global_store_dwordx2 v184, v[186:187], s[82:83] offset:-1024 sc1\n"
    "s_mov_b64 exec, -1\n"
    "Lpb_z%=:\n"
    "v_mfma_f32_16x16x32_bf16 v[40:43], v[236:239], a[140:143], v[40:43]\n"
    "s_add_u32 m0, s19, 12288\n"
    "ds_read_b64_tr_b16 v[128:129], v179 offset:0\n"
    "v_mfma_f32_16x16x32_bf16 v[56:59], v[236:239], a[172:175], 0\n"
    "s_add_u32 s84, s91, 49664\n"
    "ds_read_b64_tr_b16 v[192:193], v173 offset:0\n"
    "v_mfma_f32_16x16x32_bf16 v[56:59], v[232:235], a[168:171], v[56:59]\n"
    "s_lshl_b32 s30, s27, 13\n"
    "buffer_load_dwordx4 v167, s[68:71], s84 offen offset:0 lds\n"
    "v_mfma_f32_16x16x32_bf16 v[56:59], v[228:231], a[164:167], v[56:59]\n"
    "s_lshl_b32 s31, s25, 8\n"
    "buffer_load_dwordx4 v168, s[68:71], s84 offen offset:1024 lds\n"
    "v_mfma_f32_16x16x32_bf16 v[56:59], v[224:227], a[160:163], v[56:59]\n"
    "s_waitcnt lgkmcnt(2)\n"
    "s_add_u32 s30, s30, s31\n"
    "buffer_load_dwordx4 v169, s[68:71], s84 offen offset:2048 lds\n"
    "v_mfma_f32_16x16x32_bf16 v[60:63], v[240:243], a[160:163], 0\n"
    "ds_read_b64_tr_b16 v[194:195], v173 offset:256\n"
    "v_and_b32 v184, 7, v181\n"
    "v_mfma_f32_16x16x32_bf16 v[60:63], v[244:247], a[164:167], v[60:63]\n"
    "buffer_load_dwordx4 v170, s[68:71], s84 offen offset:3072 lds\n"
    "v_lshl_add_u32 v184, v184, 5, s30\n"
    "v_mfma_f32_16x16x32_bf16 v[60:63], v[248:251], a[168:171], v[60:63]\n"
    "s_add_u32 m0, s23, 98304\n"
    "ds_read_b64_tr_b16 v[196:197], v173 offset:4096\n"
    "v_mfma_f32_16x16x32_bf16 v[60:63], v[252:255], a[172:175], v[60:63]\n"
    "s_add_u32 s84, s16, 917504\n"
    "ds_read_b64_tr_b16 v[198:199], v173 offset:4352\n"
    "v_mfma_f32_16x16x32_bf16 v[44:47], v[252:255], a[140:143], 0\n"
    "ds_read_b64_tr_b16 v[200:201], v173 offset:8192\n"
    "v_mfma_f32_16x16x32_bf16 v[44:47], v[248:251], a[136:139], v[44:47]\n"
    "buffer_load_dwordx4 v178, s[60:63], s84 offen lds\n"
    "v_mfma_f32_16x16x32_bf16 v[44:47], v[244:247], a[132:135], v[44:47]\n"
    "s_add_u32 m0, s23, 102400\n"
    "ds_read_b64_tr_b16 v[202:203], v173 offset:8448\n"
    "v_mfma_f32_16x16x32_bf16 v[44:47], v[240:243], a[128:131], v[44:47]\n"
    "s_add_u32 s84, s16, 1032192\n"
    "ds_read_b64_tr_b16 v[130:131], v179 offset:256\n"
    "v_mfma_f32_16x16x32_bf16 v[28:31], v[240:243], a[32:35], 0\n"
    "ds_read_b64_tr_b16 v[204:205], v173 offset:12288\n"
    "v_mfma_f32_16x16x32_bf16 v[28:31], v[244:247], a[36:39], v[28:31]\n"
    "ds_read_b64_tr_b16 v[206:207], v173 offset:12544\n"
    "v_mfma_f32_16x16x32_bf16 v[28:31], v[248:251], a[40:43], v[28:31]\n"
    "buffer_load_dwordx4 v178, s[60:63], s84 offen lds\n"
    "v_mfma_f32_16x16x32_bf16 v[28:31], v[252:255], a[44:47], v[28:31]\n"
    "s_add_u32 m0, s23, 106496\n"
    "ds_read_b64_tr_b16 v[208:209], v174 offset:0\n"
    "v_mfma_f32_16x16x32_bf16 v[12:15], v[252:255], a[12:15], 0\n"
    "s_add_u32 s84, s16, 1146880\n"
    "ds_read_b64_tr_b16 v[210:211], v174 offset:256\n"
    "v_mfma_f32_16x16x32_bf16 v[12:15], v[248:251], a[8:11], v[12:15]\n"
    "buffer_load_dwordx4 v178, s[60:63], s84 offen lds\n"
    "v_mfma_f32_16x16x32_bf16 v[12:15], v[244:247], a[4:7], v[12:15]\n"
    "s_add_u32 m0, s23, 110592\n"
    "ds_read_b64_tr_b16 v[212:213], v174 offset:4096\n"
    "v_mfma_f32_16x16x32_bf16 v[12:15], v[240:243], a[0:3], v[12:15]\n"
    "s_waitcnt lgkmcnt(3)\n"
    "s_add_u32 s84, s16, 1261568\n"
    "ds_read_b64_tr_b16 v[132:133], v179 offset:4096\n"
    "v_mfma_f32_16x16x32_bf16 v[64:67], v[192:195], a[16:19], v[64:67]\n"
    "buffer_load_dwordx4 v178, s[60:63], s84 offen lds\n"
    "v_mfma_f32_16x16x32_bf16 v[64:67], v[196:199], a[20:23], v[64:67]\n"
    "s_add_u32 m0, s23, 114688\n"
    "ds_read_b64_tr_b16 v[214:215], v174 offset:4352\n"
    "v_mfma_f32_16x16x32_bf16 v[64:67], v[200:203], a[24:27], v[64:67]\n"
    "s_add_u32 s84, s16, 1376256\n"
    "ds_read_b64_tr_b16 v[216:217], v174 offset:8192\n"
    "v_mfma_f32_16x16x32_bf16 v[64:67], v[204:207], a[28:31], v[64:67]\n"
    "buffer_load_dwordx4 v178, s[60:63], s84 offen lds\n"
    "v_mfma_f32_16x16x32_bf16 v[80:83], v[204:207], a[60:63], v[80:83]\n"
    "s_add_u32 m0, s23, 118784\n"
    "ds_read_b64_tr_b16 v[218:219], v174 offset:8448\n"
    "v_mfma_f32_16x16x32_bf16 v[80:83], v[200:203], a[56:59], v[80:83]\n"
    "s_add_u32 s84, s16, 1490944\n"
    "ds_read_b64_tr_b16 v[134:135], v179 offset:4352\n"
    "v_mfma_f32_16x16x32_bf16 v[80:83], v[196:199], a[52:55], v[80:83]\n"
    "buffer_load_dwordx4 v178, s[60:63], s84 offen lds\n"
    "v_mfma_f32_16x16x32_bf16 v[80:83], v[192:195], a[48:51], v[80:83]\n"
    "s_add_u32 m0, s23, 122880\n"
    "ds_read_b64_tr_b16 v[220:221], v174 offset:12288\n"
    "v_mfma_f32_16x16x32_bf16 v[96:99], v[192:195], a[144:147], v[96:99]\n"
    "s_add_u32 s84, s16, 1605632\n"
    "ds_read_b64_tr_b16 v[222:223], v174 offset:12544\n"
    "v_mfma_f32_16x16x32_bf16 v[96:99], v[196:199], a[148:151], v[96:99]\n"
    "buffer_load_dwordx4 v178, s[60:63], s84 offen lds\n"
    "v_mfma_f32_16x16x32_bf16 v[96:99], v[200:203], a[152:155], v[96:99]\n"
    "s_add_u32 m0, s23, 126976\n"
    "ds_read_b64_tr_b16 v[224:225], v173 offset:512\n"
    "v_mfma_f32_16x16x32_bf16 v[96:99], v[204:207], a[156:159], v[96:99]\n"
    "s_add_u32 s84, s16, 1720320\n"
    "ds_read_b64_tr_b16 v[136:137], v179 offset:8192\n"
    "v_mfma_f32_16x16x32_bf16 v[112:115], v[204:207], a[188:191], v[112:115]\n"
    "buffer_load_dwordx4 v178, s[60:63], s84 offen lds\n"
    "v_mfma_f32_16x16x32_bf16 v[112:115], v[200:203], a[184:187], v[112:115]\n"
    "s_waitcnt vmcnt(20)\n"
    "ds_read_b64_tr_b16 v[226:227], v173 offset:768\n"
    "v_mfma_f32_16x16x32_bf16 v[112:115], v[196:199], a[180:183], v[112:115]\n"
    "ds_read_b128 a[64:67], v164 offset:0\n"
    "v_mfma_f32_16x16x32_bf16 v[112:115], v[192:195], a[176:179], v[112:115]\n"
    "s_waitcnt lgkmcnt(4)\n"
    "ds_read_b128 a[68:71], v175 offset:0\n"
    "v_mfma_f32_16x16x32_bf16 v[116:119], v[208:211], a[176:179], v[116:119]\n"
    "ds_read_b128 a[72:75], v164 offset:2048\n"
    "v_mfma_f32_16x16x32_bf16 v[116:119], v[212:215], a[180:183], v[116:119]\n"
    "ds_read_b128 a[76:79], v175 offset:2048\n"
    "v_mfma_f32_16x16x32_bf16 v[116:119], v[216:219], a[184:187], v[116:119]\n"
    "ds_read_b64_tr_b16 v[228:229], v173 offset:4608\n"
    "v_mfma_f32_16x16x32_bf16 v[116:119], v[220:223], a[188:191], v[116:119]\n"
    "ds_read_b64_tr_b16 v[230:231], v173 offset:4864\n"
    "v_mfma_f32_16x16x32_bf16 v[100:103], v[220:223], a[156:159], v[100:103]\n"
    "ds_read_b64_tr_b16 v[232:233], v173 offset:8704\n"
    "v_mfma_f32_16x16x32_bf16 v[100:103], v[216:219], a[152:155], v[100:103]\n"
    "s_waitcnt lgkmcnt(3)\n"
    "ds_read_b64_tr_b16 v[234:235], v173 offset:8960\n"
    "v_mfma_f32_16x16x32_bf16 v[100:103], v[212:215], a[148:151], v[100:103]\n"
    "s_add_u32 m0, s19, 0\n"
    "ds_read_b64_tr_b16 v[236:237], v173 offset:12800\n"
    "v_mfma_f32_16x16x32_bf16 v[100:103], v[208:211], a[144:147], v[100:103]\n"
    "s_add_u32 s84, s91, 768\n"
    "ds_read_b64_tr_b16 v[138:139], v179 offset:8448\n"
    "v_mfma_f32_16x16x32_bf16 v[84:87], v[208:211], a[48:51], v[84:87]\n"
    "ds_read_b64_tr_b16 v[238:239], v173 offset:13056\n"
    "v_mfma_f32_16x16x32_bf16 v[84:87], v[212:215], a[52:55], v[84:87]\n"
    "buffer_load_dwordx4 v167, s[68:71], s84 offen offset:0 lds\n"
    "v_mfma_f32_16x16x32_bf16 v[84:87], v[216:219], a[56:59], v[84:87]\n"
    "buffer_load_dwordx4 v168, s[68:71], s84 offen offset:1024 lds\n"
    "v_mfma_f32_16x16x32_bf16 v[84:87], v[220:223], a[60:63], v[84:87]\n"
    "buffer_load_dwordx4 v169, s[68:71], s84 offen offset:2048 lds\n"
    "v_mfma_f32_16x16x32_bf16 v[68:71], v[220:223], a[28:31], v[68:71]\n"
    "ds_read_b64_tr_b16 v[140:141], v179 offset:12288\n"
    "v_mfma_f32_16x16x32_bf16 v[68:71], v[216:219], a[24:27], v[68:71]\n"
    "buffer_load_dwordx4 v170, s[68:71], s84 offen offset:3072 lds\n"
    "v_mfma_f32_16x16x32_bf16 v[68:71], v[212:215], a[20:23], v[68:71]\n"
    "ds_read_b64_tr_b16 v[142:143], v179 offset:12544\n"
    "v_mfma_f32_16x16x32_bf16 v[68:71], v[208:211], a[16:19], v[68:71]\n"
    "s_waitcnt lgkmcnt(2)\n"
    "ds_read_b64_tr_b16 v[240:241], v174 offset:512\n"
    "v_mfma_f32_16x16x32_bf16 v[72:75], v[224:227], a[16:19], v[72:75]\n"
    "ds_read_b64_tr_b16 v[242:243], v174 offset:768\n"
    "v_mfma_f32_16x16x32_bf16 v[72:75], v[228:231], a[20:23], v[72:75]\n"
    "ds_read_b64_tr_b16 v[244:245], v174 offset:4608\n"
    "v_mfma_f32_16x16x32_bf16 v[72:75], v[232:235], a[24:27], v[72:75]\n"
    "s_waitcnt vmcnt(20)\n"
    "ds_read_b64_tr_b16 v[246:247], v174 offset:4864\n"
    "v_mfma_f32_16x16x32_bf16 v[72:75], v[236:239], a[28:31], v[72:75]\n"
    "ds_read_b128 a[96:99], v164 offset:4096\n"
    "v_mfma_f32_16x16x32_bf16 v[88:91], v[236:239], a[60:63], v[88:91]\n"
    "ds_read_b128 a[100:103], v175 offset:4096\n"
    "v_mfma_f32_16x16x32_bf16 v[88:91], v[232:235], a[56:59], v[88:91]\n"
    "ds_read_b128 a[104:107], v164 offset:6144\n"
    "v_mfma_f32_16x16x32_bf16 v[88:91], v[228:231], a[52:55], v[88:91]\n"
    "ds_read_b64_tr_b16 v[248:249], v174 offset:8704\n"
    "v_mfma_f32_16x16x32_bf16 v[88:91], v[224:227], a[48:51], v[88:91]\n"
    "ds_read_b64_tr_b16 v[250:251], v174 offset:8960\n"
    "v_mfma_f32_16x16x32_bf16 v[104:107], v[224:227], a[144:147], v[104:107]\n"
    "ds_read_b128 a[108:111], v175 offset:6144\n"
    "v_mfma_f32_16x16x32_bf16 v[104:107], v[228:231], a[148:151], v[104:107]\n"
    "ds_read_b64_tr_b16 v[252:253], v174 offset:12800\n"
    "v_mfma_f32_16x16x32_bf16 v[104:107], v[232:235], a[152:155], v[104:107]\n"
    "ds_read_b64_tr_b16 v[254:255], v174 offset:13056\n"
    "v_mfma_f32_16x16x32_bf16 v[104:107], v[236:239], a[156:159], v[104:107]\n"
    "ds_read_b64_tr_b16 v[192:193], v173 offset:16384\n"
    "v_mfma_f32_16x16x32_bf16 v[120:123], v[236:239], a[188:191], v[120:123]\n"
    "s_waitcnt lgkmcnt(3)\n"
    "ds_read_b64_tr_b16 v[194:195], v173 offset:16640\n"
    "v_mfma_f32_16x16x32_bf16 v[120:123], v[232:235], a[184:187], v[120:123]\n"
    "v_mfma_f32_16x16x32_bf16 v[120:123], v[228:231], a[180:183], v[120:123]\n"
    "s_add_u32 m0, s19, 4096\n"
    "ds_read_b64_tr_b16 v[196:197], v173 offset:20480\n"
    "v_mfma_f32_16x16x32_bf16 v[120:123], v[224:227], a[176:179], v[120:123]\n"
    "s_add_u32 s84, s91, 17152\n"
    "v_smfmac_f32_16x16x64_bf16 v[144:147], v[160:163], v[128:135], v183\n"
    "s_waitcnt lgkmcnt(3)\n"
    "buffer_load_dwordx4 v167, s[68:71], s84 offen offset:0 lds\n"
    "v_smfmac_f32_16x16x64_bf16 v[144:147], v[160:163], v[136:143], v183\n"
    "buffer_load_dwordx4 v168, s[68:71], s84 offen offset:1024 lds\n"
    "v_mfma_f32_16x16x32_bf16 v[124:127], v[240:243], a[176:179], v[124:127]\n"
    "ds_read_b64_tr_b16 v[198:199], v173 offset:20736\n"
    "v_mfma_f32_16x16x32_bf16 v[124:127], v[244:247], a[180:183], v[124:127]\n"
    "buffer_load_dwordx4 v169, s[68:71], s84 offen offset:2048 lds\n"
    "v_mfma_f32_16x16x32_bf16 v[124:127], v[248:251], a[184:187], v[124:127]\n"
    "ds_read_b64_tr_b16 v[200:201], v173 offset:24576\n"
    "v_mfma_f32_16x16x32_bf16 v[124:127], v[252:255], a[188:191], v[124:127]\n"
    "ds_read_b64_tr_b16 v[202:203], v173 offset:24832\n"
    "v_mfma_f32_16x16x32_bf16 v[108:111], v[252:255], a[156:159], v[108:111]\n"
    "ds_read_b64_tr_b16 v[204:205], v173 offset:28672\n"
    "v_mfma_f32_16x16x32_bf16 v[108:111], v[248:251], a[152:155], v[108:111]\n"
    "buffer_load_dwordx4 v170, s[68:71], s84 offen offset:3072 lds\n"
    "v_mfma_f32_16x16x32_bf16 v[108:111], v[244:247], a[148:151], v[108:111]\n"
    "global_load_dwordx2 v[186:187], v184, s[82:83] offset:-1024 sc1\n"
    "v_mfma_f32_16x16x32_bf16 v[108:111], v[240:243], a[144:147], v[108:111]\n"
    "ds_read_b64_tr_b16 v[206:207], v173 offset:28928\n"
    "v_mfma_f32_16x16x32_bf16 v[92:95], v[240:243], a[48:51], v[92:95]\n"
    "s_waitcnt vmcnt(21)\n"
    "ds_read_b64_tr_b16 v[208:209], v174 offset:16384\n"
    "v_mfma_f32_16x16x32_bf16 v[92:95], v[244:247], a[52:55], v[92:95]\n"
    "ds_read_b128 a[192:195], v164 offset:8192\n"
    "v_mfma_f32_16x16x32_bf16 v[92:95], v[248:251], a[56:59], v[92:95]\n"
    "ds_read_b128 a[196:199], v175 offset:8192\n"
    "v_mfma_f32_16x16x32_bf16 v[92:95], v[252:255], a[60:63], v[92:95]\n"
    "ds_read_b128 a[200:203], v164 offset:10240\n"
    "v_mfma_f32_16x16x32_bf16 v[76:79], v[252:255], a[28:31], v[76:79]\n"
    "ds_read_b128 a[204:207], v175 offset:10240\n"
    "v_mfma_f32_16x16x32_bf16 v[76:79], v[248:251], a[24:27], v[76:79]\n"
    "ds_read_b64_tr_b16 v[210:211], v174 offset:16640\n"
    "v_mfma_f32_16x16x32_bf16 v[76:79], v[244:247], a[20:23], v[76:79]\n"
    "ds_read_b64_tr_b16 v[212:213], v174 offset:20480\n"
    "v_mfma_f32_16x16x32_bf16 v[76:79], v[240:243], a[16:19], v[76:79]\n"
    "s_waitcnt lgkmcnt(5)\n"
    "ds_read_b64_tr_b16 v[214:215], v174 offset:20736\n"
    "v_mfma_f32_16x16x32_bf16 v[0:3], v[192:195], a[16:19], v[0:3]\n"
    "s_waitcnt lgkmcnt(3)\n"
    "ds_read_b64_tr_b16 v[216:217], v174 offset:24576\n"
    "v_mfma_f32_16x16x32_bf16 v[0:3], v[196:199], a[20:23], v[0:3]\n"
    "ds_read_b64_tr_b16 v[128:129], v179 offset:16384\n"
    "v_mfma_f32_16x16x32_bf16 v[0:3], v[200:203], a[24:27], v[0:3]\n"
    "ds_read_b64_tr_b16 v[218:219], v174 offset:24832\n"
    "v_mfma_f32_16x16x32_bf16 v[0:3], v[204:207], a[28:31], v[0:3]\n"
    "ds_read_b64_tr_b16 v[130:131], v179 offset:16640\n"
    "v_mfma_f32_16x16x32_bf16 v[16:19], v[204:207], a[60:63], v[16:19]\n"
    "s_add_u32 m0, s19, 8192\n"
    "ds_read_b64_tr_b16 v[220:221], v174 offset:28672\n"
    "v_mfma_f32_16x16x32_bf16 v[16:19], v[200:203], a[56:59], v[16:19]\n"
    "s_add_u32 s84, s91, 33536\n"
    "ds_read_b64_tr_b16 v[132:133], v179 offset:20480\n"
    "v_mfma_f32_16x16x32_bf16 v[16:19], v[196:199], a[52:55], v[16:19]\n"
    "ds_read_b64_tr_b16 v[222:223], v174 offset:28928\n"
    "v_mfma_f32_16x16x32_bf16 v[16:19], v[192:195], a[48:51], v[16:19]\n"
    "buffer_load_dwordx4 v167, s[68:71], s84 offen offset:0 lds\n"
    "v_mfma_f32_16x16x32_bf16 v[32:35], v[192:195], a[144:147], v[32:35]\n"
    "buffer_load_dwordx4 v168, s[68:71], s84 offen offset:1024 lds\n"
    "v_mfma_f32_16x16x32_bf16 v[32:35], v[196:199], a[148:151], v[32:35]\n"
    "buffer_load_dwordx4 v169, s[68:71], s84 offen offset:2048 lds\n"
    "v_mfma_f32_16x16x32_bf16 v[32:35], v[200:203], a[152:155], v[32:35]\n"
    "ds_read_b64_tr_b16 v[134:135], v179 offset:20736\n"
    "v_mfma_f32_16x16x32_bf16 v[32:35], v[204:207], a[156:159], v[32:35]\n"
    "buffer_load_dwordx4 v170, s[68:71], s84 offen offset:3072 lds\n"
    "v_mfma_f32_16x16x32_bf16 v[48:51], v[204:207], a[188:191], v[48:51]\n"
    "ds_read_b64_tr_b16 v[136:137], v179 offset:24576\n"
    "v_mfma_f32_16x16x32_bf16 v[48:51], v[200:203], a[184:187], v[48:51]\n"
    "ds_read_b64_tr_b16 v[138:139], v179 offset:24832\n"
    "v_mfma_f32_16x16x32_bf16 v[48:51], v[196:199], a[180:183], v[48:51]\n"
    "ds_read_b64_tr_b16 v[140:141], v179 offset:28672\n"
    "v_mfma_f32_16x16x32_bf16 v[48:51], v[192:195], a[176:179], v[48:51]\n"
    "s_waitcnt lgkmcnt(4)\n"
    "ds_read_b64_tr_b16 v[142:143], v179 offset:28928\n"
    "v_mfma_f32_16x16x32_bf16 v[52:55], v[208:211], a[176:179], v[52:55]\n"
    "s_waitcnt vmcnt(21)\n"
    "ds_read_b64_tr_b16 v[224:225], v173 offset:16896\n"
    "v_mfma_f32_16x16x32_bf16 v[52:55], v[212:215], a[180:183], v[52:55]\n"
    "ds_read_b128 a[224:227], v164 offset:12288\n"
    "v_mfma_f32_16x16x32_bf16 v[52:55], v[216:219], a[184:187], v[52:55]\n"
    "ds_read_b128 a[228:231], v175 offset:12288\n"
    "v_mfma_f32_16x16x32_bf16 v[52:55], v[220:223], a[188:191], v[52:55]\n"
    "ds_read_b128 a[232:235], v164 offset:14336\n"
    "v_mfma_f32_16x16x32_bf16 v[36:39], v[220:223], a[156:159], v[36:39]\n"
    "ds_read_b64_tr_b16 v[226:227], v173 offset:17152\n"
    "v_mfma_f32_16x16x32_bf16 v[36:39], v[216:219], a[152:155], v[36:39]\n"
    "ds_read_b128 a[236:239], v175 offset:14336\n"
    "v_mfma_f32_16x16x32_bf16 v[36:39], v[212:215], a[148:151], v[36:39]\n"
    "ds_read_b64_tr_b16 v[228:229], v173 offset:20992\n"
    "v_mfma_f32_16x16x32_bf16 v[36:39], v[208:211], a[144:147], v[36:39]\n"
    "ds_read_b64_tr_b16 v[230:231], v173 offset:21248\n"
    "v_mfma_f32_16x16x32_bf16 v[20:23], v[208:211], a[48:51], v[20:23]\n"
    "ds_read_b64_tr_b16 v[232:233], v173 offset:25088\n"
    "v_mfma_f32_16x16x32_bf16 v[20:23], v[212:215], a[52:55], v[20:23]\n"
    "s_waitcnt lgkmcnt(3)\n"
    "ds_read_b64_tr_b16 v[234:235], v173 offset:25344\n"
    "v_mfma_f32_16x16x32_bf16 v[20:23], v[216:219], a[56:59], v[20:23]\n"
    "ds_read_b64_tr_b16 v[236:237], v173 offset:29184\n"
    "v_mfma_f32_16x16x32_bf16 v[20:23], v[220:223], a[60:63], v[20:23]\n"
    "ds_read_b64_tr_b16 v[238:239], v173 offset:29440\n"
    "v_mfma_f32_16x16x32_bf16 v[4:7], v[220:223], a[28:31], v[4:7]\n"
    "s_add_u32 m0, s19, 12288\n"
    "ds_read_b64_tr_b16 v[240:241], v174 offset:16896\n"
    "v_mfma_f32_16x16x32_bf16 v[4:7], v[216:219], a[24:27], v[4:7]\n"
    "s_add_u32 s84, s91, 49920\n"
    "ds_read_b64_tr_b16 v[242:243], v174 offset:17152\n"
    "v_mfma_f32_16x16x32_bf16 v[4:7], v[212:215], a[20:23], v[4:7]\n"
    "buffer_load_dwordx4 v167, s[68:71], s84 offen offset:0 lds\n"
    "v_mfma_f32_16x16x32_bf16 v[4:7], v[208:211], a[16:19], v[4:7]\n"
    "s_waitcnt lgkmcnt(4)\n"
    "ds_read_b64_tr_b16 v[244:245], v174 offset:20992\n"
    "v_smfmac_f32_16x16x64_bf16 v[148:151], v[160:163], v[128:135], v183\n"
    "ds_read_b64_tr_b16 v[246:247], v174 offset:21248\n"
    "v_smfmac_f32_16x16x64_bf16 v[148:151], v[160:163], v[136:143], v183\n"
    "ds_read_b64_tr_b16 v[248:249], v174 offset:25088\n"
    "v_mfma_f32_16x16x32_bf16 v[8:11], v[224:227], a[16:19], v[8:11]\n"
    "ds_read_b64_tr_b16 v[250:251], v174 offset:25344\n"
    "v_mfma_f32_16x16x32_bf16 v[8:11], v[228:231], a[20:23], v[8:11]\n"
    "buffer_load_dwordx4 v168, s[68:71], s84 offen offset:1024 lds\n"
    "v_mfma_f32_16x16x32_bf16 v[8:11], v[232:235], a[24:27], v[8:11]\n"
    "s_waitcnt lgkmcnt(4)\n"
    "ds_read_b64_tr_b16 v[252:253], v174 offset:29184\n"
    "v_mfma_f32_16x16x32_bf16 v[8:11], v[236:239], a[28:31], v[8:11]\n"
    "ds_read_b64_tr_b16 v[254:255], v174 offset:29440\n"
    "v_mfma_f32_16x16x32_bf16 v[24:27], v[236:239], a[60:63], v[24:27]\n"
    "buffer_load_dwordx4 v169, s[68:71], s84 offen offset:2048 lds\n"
    "v_mfma_f32_16x16x32_bf16 v[24:27], v[232:235], a[56:59], v[24:27]\n"
    "buffer_load_dwordx4 v170, s[68:71], s84 offen offset:3072 lds\n"
    "v_mfma_f32_16x16x32_bf16 v[24:27], v[228:231], a[52:55], v[24:27]\n"
    "s_waitcnt vmcnt(8)\n"
    "s_mov_b32 s28, 0x40000\n"
    "Lpk_z%=:\n"
    "v_cmp_ne_u64 vcc, s[80:81], v[186:187]\n"
    "s_cmp_eq_u64 vcc, 0\n"
    "s_cbranch_scc1 Lpkd_z%=\n"
    "global_load_dwordx2 v[186:187], v184, s[82:83] offset:-1024 sc1\n"
    "s_waitcnt vmcnt(0)\n"
    "s_sub_u32 s28, s28, 1\n"
    "s_cbranch_scc0 Lpk_z%=\n"
    "Lpkd_z%=:\n"
    "v_mfma_f32_16x16x32_bf16 v[24:27], v[224:227], a[48:51], v[24:27]\n"
    "v_and_b32 v159, 15, v181\n"
    "v_lshlrev_b32 v159, 3, v159\n"
    "v_mfma_f32_16x16x32_bf16 v[40:43], v[224:227], a[144:147], v[40:43]\n"
    "global_load_dwordx2 v[152:153], v159, s[34:35] offset:0 sc1\n"
    "v_mfma_f32_16x16x32_bf16 v[40:43], v[228:231], a[148:151], v[40:43]\n"
    "s_waitcnt vmcnt(18) lgkmcnt(0)\n"
    "global_load_dwordx2 v[154:155], v159, s[34:35] offset:128 sc1\n"
    "global_load_dwordx2 v[156:157], v159, s[34:35] offset:256 sc1\n"
    "v_mfma_f32_16x16x32_bf16 v[40:43], v[232:235], a[152:155], v[40:43]\n"
    "s_barrier\n"
    "global_load_dwordx2 v[158:159], v159, s[34:35] offset:384 sc1\n"
    "v_mfma_f32_16x16x32_bf16 v[40:43], v[236:239], a[156:159], v[40:43]\n"
    "s_add_u32 m0, s23, 65536\n"
    "ds_read_b64_tr_b16 v[128:129], v179 offset:32768\n"
    "v_mfma_f32_16x16x32_bf16 v[56:59], v[236:239], a[188:191], v[56:59]\n"
    "s_add_u32 s84, s16, 917504\n"
    "ds_read_b64_tr_b16 v[192:193], v173 offset:32768\n"
    "v_bfe_u32 v187, v181, 1, 4\n"
    "v_mfma_f32_16x16x32_bf16 v[56:59], v[232:235], a[184:187], v[56:59]\n"
    "buffer_load_dwordx4 v178, s[64:67], s84 offen lds\n"
    "v_lshlrev_b32 v187, 13, v187\n"
    "v_mfma_f32_16x16x32_bf16 v[56:59], v[228:231], a[180:183], v[56:59]\n"
    "s_add_u32 m0, s23, 69632\n"
    "ds_read_b64_tr_b16 v[194:195], v173 offset:33024\n"
    "v_and_b32 v185, 1, v181\n"
    "v_mfma_f32_16x16x32_bf16 v[56:59], v[224:227], a[176:179], v[56:59]\n"
    "s_add_u32 s84, s16, 1032192\n"
    "ds_read_b64_tr_b16 v[130:131], v179 offset:33024\n"
    "v_lshl_add_u32 v187, v185, 7, v187\n"
    "v_mfma_f32_16x16x32_bf16 v[60:63], v[240:243], a[176:179], v[60:63]\n"
    "s_waitcnt lgkmcnt(4)\n"
    "s_lshl_b32 s30, s90, 15\n"
    "buffer_load_dwordx4 v178, s[64:67], s84 offen lds\n"
    "v_mfma_f32_16x16x32_bf16 v[60:63], v[244:247], a[180:183], v[60:63]\n"
    "ds_read_b64_tr_b16 v[196:197], v173 offset:36864\n"
    "v_mfma_f32_16x16x32_bf16 v[60:63], v[248:251], a[184:187], v[60:63]\n"
    "s_add_u32 m0, s23, 73728\n"
    "ds_read_b64_tr_b16 v[198:199], v173 offset:37120\n"
    "v_mfma_f32_16x16x32_bf16 v[60:63], v[252:255], a[188:191], v[60:63]\n"
    "s_add_u32 s84, s16, 1146880\n"
    "ds_read_b64_tr_b16 v[132:133], v179 offset:36864\n"
    "v_mfma_f32_16x16x32_bf16 v[44:47], v[252:255], a[156:159], v[44:47]\n"
    "ds_read_b64_tr_b16 v[200:201], v173 offset:40960\n"
    "v_mfma_f32_16x16x32_bf16 v[44:47], v[248:251], a[152:155], v[44:47]\n"
    "buffer_load_dwordx4 v178, s[64:67], s84 offen lds\n"
    "v_mfma_f32_16x16x32_bf16 v[44:47], v[244:247], a[148:151], v[44:47]\n"
    "s_add_u32 m0, s23, 77824\n"
    "ds_read_b64_tr_b16 v[202:203], v173 offset:41216\n"
    "v_mfma_f32_16x16x32_bf16 v[44:47], v[240:243], a[144:147], v[44:47]\n"
    "s_add_u32 s84, s16, 1261568\n"
    "ds_read_b64_tr_b16 v[134:135], v179 offset:37120\n"
    "v_mfma_f32_16x16x32_bf16 v[28:31], v[240:243], a[48:51], v[28:31]\n"
    "ds_read_b64_tr_b16 v[204:205], v173 offset:45056\n"
    "v_mfma_f32_16x16x32_bf16 v[28:31], v[244:247], a[52:55], v[28:31]\n"
    "ds_read_b64_tr_b16 v[206:207], v173 offset:45312\n"
    "v_mfma_f32_16x16x32_bf16 v[28:31], v[248:251], a[56:59], v[28:31]\n"
    "buffer_load_dwordx4 v178, s[64:67], s84 offen lds\n"
    "v_mfma_f32_16x16x32_bf16 v[28:31], v[252:255], a[60:63], v[28:31]\n"
    "s_add_u32 m0, s23, 81920\n"
    "ds_read_b64_tr_b16 v[136:137], v179 offset:40960\n"
    "v_mfma_f32_16x16x32_bf16 v[12:15], v[252:255], a[28:31], v[12:15]\n"
    "s_add_u32 s84, s16, 1376256\n"
    "ds_read_b64_tr_b16 v[138:139], v179 offset:41216\n"
    "v_mfma_f32_16x16x32_bf16 v[12:15], v[248:251], a[24:27], v[12:15]\n"
    "buffer_load_dwordx4 v178, s[64:67], s84 offen lds\n"
    "v_mfma_f32_16x16x32_bf16 v[12:15], v[244:247], a[20:23], v[12:15]\n"
    "s_add_u32 m0, s23, 86016\n"
    "ds_read_b64_tr_b16 v[140:141], v179 offset:45056\n"
    "v_mfma_f32_16x16x32_bf16 v[12:15], v[240:243], a[16:19], v[12:15]\n"
    "s_waitcnt lgkmcnt(3)\n"
    "s_add_u32 s84, s16, 1490944\n"
    "ds_read_b64_tr_b16 v[142:143], v179 offset:45312\n"
    "v_mfma_f32_16x16x32_bf16 v[64:67], v[192:195], a[64:67], v[64:67]\n"
    "buffer_load_dwordx4 v178, s[64:67], s84 offen lds\n"
    "v_mfma_f32_16x16x32_bf16 v[64:67], v[196:199], a[68:71], v[64:67]\n"
    "s_add_u32 m0, s23, 90112\n"
    "ds_read_b64_tr_b16 v[208:209], v174 offset:32768\n"
    "v_mfma_f32_16x16x32_bf16 v[64:67], v[200:203], a[72:75], v[64:67]\n"
    "s_add_u32 s84, s16, 1605632\n"
    "ds_read_b64_tr_b16 v[210:211], v174 offset:33024\n"
    "v_mfma_f32_16x16x32_bf16 v[64:67], v[204:207], a[76:79], v[64:67]\n"
    "buffer_load_dwordx4 v178, s[64:67], s84 offen lds\n"
    "v_mfma_f32_16x16x32_bf16 v[80:83], v[204:207], a[108:111], v[80:83]\n"
    "s_add_u32 m0, s23, 94208\n"
    "ds_read_b64_tr_b16 v[212:213], v174 offset:36864\n"
    "v_mfma_f32_16x16x32_bf16 v[80:83], v[200:203], a[104:107], v[80:83]\n"
    "s_add_u32 s84, s16, 1720320\n"
    "ds_read_b64_tr_b16 v[214:215], v174 offset:37120\n"
    "v_mfma_f32_16x16x32_bf16 v[80:83], v[196:199], a[100:103], v[80:83]\n"
    "ds_read_b64_tr_b16 v[216:217], v174 offset:40960\n"
    "v_mfma_f32_16x16x32_bf16 v[80:83], v[192:195], a[96:99], v[80:83]\n"
    "ds_read_b64_tr_b16 v[218:219], v174 offset:41216\n"
    "v_mfma_f32_16x16x32_bf16 v[96:99], v[192:195], a[192:195], v[96:99]\n"
    "ds_read_b64_tr_b16 v[220:221], v174 offset:45056\n"
    "v_mfma_f32_16x16x32_bf16 v[96:99], v[196:199], a[196:199], v[96:99]\n"
    "ds_read_b64_tr_b16 v[222:223], v174 offset:45312\n"
    "v_mfma_f32_16x16x32_bf16 v[96:99], v[200:203], a[200:203], v[96:99]\n"
    "buffer_load_dwordx4 v178, s[64:67], s84 offen lds\n"
    "v_mfma_f32_16x16x32_bf16 v[96:99], v[204:207], a[204:207], v[96:99]\n"
    "s_waitcnt vmcnt(25)\n"
    "ds_read_b64_tr_b16 v[224:225], v173 offset:33280\n"
    "v_lshrrev_b32 v178, 4, v181\n"
    "v_mfma_f32_16x16x32_bf16 v[112:115], v[204:207], a[236:239], v[112:115]\n"
    "ds_read_b128 a[80:83], v164 offset:0\n"
    "v_lshlrev_b32 v178, 4, v178\n"
    "v_mfma_f32_16x16x32_bf16 v[112:115], v[200:203], a[232:235], v[112:115]\n"
    "ds_read_b128 a[84:87], v175 offset:0\n"
    "v_mfma_f32_16x16x32_bf16 v[112:115], v[196:199], a[228:231], v[112:115]\n"
    "ds_read_b128 a[88:91], v164 offset:2048\n"
    "v_mfma_f32_16x16x32_bf16 v[112:115], v[192:195], a[224:227], v[112:115]\n"
    "ds_read_b128 a[92:95], v175 offset:2048\n"
    "v_smfmac_f32_16x16x64_bf16 v[144:147], v[160:163], v[128:135], v183\n"
    "s_waitcnt lgkmcnt(5)\n"
    "ds_read_b64_tr_b16 v[226:227], v173 offset:33536\n"
    "v_smfmac_f32_16x16x64_bf16 v[144:147], v[160:163], v[136:143], v183\n"
    "ds_read_b64_tr_b16 v[128:129], v179 offset:49152\n"
    "v_mfma_f32_16x16x32_bf16 v[116:119], v[208:211], a[224:227], v[116:119]\n"
    "ds_read_b64_tr_b16 v[130:131], v179 offset:49408\n"
    "v_mfma_f32_16x16x32_bf16 v[116:119], v[212:215], a[228:231], v[116:119]\n"
    "ds_read_b64_tr_b16 v[132:133], v179 offset:53248\n"
    "v_mfma_f32_16x16x32_bf16 v[116:119], v[216:219], a[232:235], v[116:119]\n"
    "ds_read_b64_tr_b16 v[134:135], v179 offset:53504\n"
    "v_mfma_f32_16x16x32_bf16 v[116:119], v[220:223], a[236:239], v[116:119]\n"
    "ds_read_b64_tr_b16 v[136:137], v179 offset:57344\n"
    "v_mfma_f32_16x16x32_bf16 v[100:103], v[220:223], a[204:207], v[100:103]\n"
    "ds_read_b64_tr_b16 v[138:139], v179 offset:57600\n"
    "v_mfma_f32_16x16x32_bf16 v[100:103], v[216:219], a[200:203], v[100:103]\n"
    "ds_read_b64_tr_b16 v[140:141], v179 offset:61440\n"
    "v_mfma_f32_16x16x32_bf16 v[100:103], v[212:215], a[196:199], v[100:103]\n"
    "ds_read_b64_tr_b16 v[142:143], v179 offset:61696\n"
    "v_mfma_f32_16x16x32_bf16 v[100:103], v[208:211], a[192:195], v[100:103]\n"
    "ds_read_b64_tr_b16 v[228:229], v173 offset:37376\n"
    "v_mfma_f32_16x16x32_bf16 v[84:87], v[208:211], a[96:99], v[84:87]\n"
    "ds_read_b64_tr_b16 v[230:231], v173 offset:37632\n"
    "v_mfma_f32_16x16x32_bf16 v[84:87], v[212:215], a[100:103], v[84:87]\n"
    "ds_read_b64_tr_b16 v[232:233], v173 offset:41472\n"
    "v_mfma_f32_16x16x32_bf16 v[84:87], v[216:219], a[104:107], v[84:87]\n"
    "ds_read_b64_tr_b16 v[234:235], v173 offset:41728\n"
    "v_mfma_f32_16x16x32_bf16 v[84:87], v[220:223], a[108:111], v[84:87]\n"
    "ds_read_b64_tr_b16 v[236:237], v173 offset:45568\n"
    "v_mfma_f32_16x16x32_bf16 v[68:71], v[220:223], a[76:79], v[68:71]\n"
    "s_waitcnt vmcnt(21)\n"
    "ds_read_b64_tr_b16 v[238:239], v173 offset:45824\n"
    "v_mfma_f32_16x16x32_bf16 v[68:71], v[216:219], a[72:75], v[68:71]\n"
    "ds_read_b128 a[112:115], v164 offset:4096\n"
    "v_mfma_f32_16x16x32_bf16 v[68:71], v[212:215], a[68:71], v[68:71]\n"
    "ds_read_b128 a[116:119], v175 offset:4096\n"
    "v_mfma_f32_16x16x32_bf16 v[68:71], v[208:211], a[64:67], v[68:71]\n"
    "s_waitcnt vmcnt(8) lgkmcnt(5)\n"
    "ds_read_b128 a[120:123], v164 offset:6144\n"
    "v_smfmac_f32_16x16x64_bf16 v[144:147], v[160:163], v[128:135], v183\n"
    "ds_read_b128 a[124:127], v175 offset:6144\n"
    "v_mov_b32 v188, v152\n"
    "v_smfmac_f32_16x16x64_bf16 v[144:147], v[160:163], v[136:143], v183\n"
    "ds_read_b64_tr_b16 v[240:241], v174 offset:33280\n"
    "v_mul_f32 v165, 0xbfb8aa3b, v153\n"
    "v_mfma_f32_16x16x32_bf16 v[72:75], v[224:227], a[64:67], v[72:75]\n"
    "ds_read_b64_tr_b16 v[242:243], v174 offset:33536\n"
    "v_mul_f32 v153, v153, v153\n"
    "v_mfma_f32_16x16x32_bf16 v[72:75], v[228:231], a[68:71], v[72:75]\n"
    "s_waitcnt lgkmcnt(5)\n"
    "ds_read_b64_tr_b16 v[244:245], v174 offset:37376\n"
    "v_mfma_f32_16x16x32_bf16 v[72:75], v[232:235], a[72:75], v[72:75]\n"
    "ds_read_b64_tr_b16 v[246:247], v174 offset:37632\n"
    "v_mfma_f32_16x16x32_bf16 v[72:75], v[236:239], a[76:79], v[72:75]\n"
    "v_rcp_f32 v167, v153\n"
    "v_mfma_f32_16x16x32_bf16 v[88:91], v[236:239], a[108:111], v[88:91]\n"
    "v_mov_b32 v189, v154\n"
    "v_mul_f32 v166, 0xbfb8aa3b, v155\n"
    "v_mfma_f32_16x16x32_bf16 v[88:91], v[232:235], a[104:107], v[88:91]\n"
    "v_add_f32 v144, v144, v145\n"
    "v_lshl_add_u32 v146, v181, 2, s93\n"
    "v_mfma_f32_16x16x32_bf16 v[88:91], v[228:231], a[100:103], v[88:91]\n"
    "v_and_b32 v146, 0xffff, v146\n"
    "v_mul_f32 v155, v155, v155\n"
    "v_mfma_f32_16x16x32_bf16 v[88:91], v[224:227], a[96:99], v[88:91]\n"
    "s_nop 0\n"
    "s_mov_b64 exec, 0xffff\n"
    "ds_write_b32 v146, v144 offset:0\n"
    "s_mov_b64 exec, -1\n"
    "v_rcp_f32 v168, v155\n"
    "v_mfma_f32_16x16x32_bf16 v[104:107], v[224:227], a[192:195], v[104:107]\n"
    "v_mov_b32 v190, v156\n"
    "v_mul_f32 v171, 0xbfb8aa3b, v157\n"
    "v_mul_f32 v157, v157, v157\n"
    "v_rcp_f32 v169, v157\n"
    "v_mfma_f32_16x16x32_bf16 v[104:107], v[228:231], a[196:199], v[104:107]\n"
    "v_mov_b32 v191, v158\n"
    "v_mul_f32 v172, 0xbfb8aa3b, v159\n"
    "v_mul_f32 v159, v159, v159\n"
    "v_rcp_f32 v170, v159\n"
    "v_mfma_f32_16x16x32_bf16 v[104:107], v[232:235], a[200:203], v[104:107]\n"
    "s_waitcnt vmcnt(16)\n"
    "ds_read_b64_tr_b16 v[248:249], v174 offset:41472\n"
    "ds_read_b64_tr_b16 v[250:251], v174 offset:41728\n"
    "v_mfma_f32_16x16x32_bf16 v[104:107], v[236:239], a[204:207], v[104:107]\n"
    "ds_read_b64_tr_b16 v[252:253], v174 offset:45568\n"
    "ds_read_b64_tr_b16 v[254:255], v174 offset:45824\n"
    "v_mfma_f32_16x16x32_bf16 v[120:123], v[236:239], a[236:239], v[120:123]\n"
    "ds_read_b128 a[208:211], v164 offset:8192\n"
    "v_mfma_f32_16x16x32_bf16 v[120:123], v[232:235], a[232:235], v[120:123]\n"
    "s_waitcnt vmcnt(0) lgkmcnt(5)\n"
    "ds_read_b128 a[212:215], v175 offset:8192\n"
    "v_mfma_f32_16x16x32_bf16 v[120:123], v[228:231], a[228:231], v[120:123]\n"
    "s_barrier\n"
    "ds_read_b128 a[216:219], v164 offset:10240\n"
    "v_mfma_f32_16x16x32_bf16 v[120:123], v[224:227], a[224:227], v[120:123]\n"
    "s_waitcnt lgkmcnt(7)\n"
    "ds_read_b128 v[152:155], v178 offset:0\n"
    "v_mfma_f32_16x16x32_bf16 v[124:127], v[240:243], a[224:227], v[124:127]\n"
    "ds_read_b128 a[220:223], v175 offset:10240\n"
    "v_mfma_f32_16x16x32_bf16 v[124:127], v[244:247], a[228:231], v[124:127]\n"
    "s_waitcnt lgkmcnt(5)\n"
    "ds_read_b64_tr_b16 v[192:193], v173 offset:49152\n"
    "v_mfma_f32_16x16x32_bf16 v[124:127], v[248:251], a[232:235], v[124:127]\n"
    "ds_read_b64_tr_b16 v[194:195], v173 offset:49408\n"
    "v_mfma_f32_16x16x32_bf16 v[124:127], v[252:255], a[236:239], v[124:127]\n"
    "ds_read_b64_tr_b16 v[196:197], v173 offset:53248\n"
    "v_mfma_f32_16x16x32_bf16 v[108:111], v[252:255], a[204:207], v[108:111]\n"
    "ds_read_b64_tr_b16 v[198:199], v173 offset:53504\n"
    "v_mfma_f32_16x16x32_bf16 v[108:111], v[248:251], a[200:203], v[108:111]\n"
    "s_waitcnt lgkmcnt(5)\n"
    "ds_read_b64_tr_b16 v[200:201], v173 offset:57344\n"
    "v_fmac_f32 v112, v191, v152\n"
    "v_mfma_f32_16x16x32_bf16 v[108:111], v[244:247], a[196:199], v[108:111]\n"
    "ds_read_b64_tr_b16 v[202:203], v173 offset:57600\n"
    "v_fmac_f32 v113, v191, v153\n"
    "v_mfma_f32_16x16x32_bf16 v[108:111], v[240:243], a[192:195], v[108:111]\n"
    "ds_read_b64_tr_b16 v[204:205], v173 offset:61440\n"
    "v_fmac_f32 v114, v191, v154\n"
    "v_mfma_f32_16x16x32_bf16 v[92:95], v[240:243], a[96:99], v[92:95]\n"
    "s_waitcnt vmcnt(12)\n"
    "ds_read_b64_tr_b16 v[206:207], v173 offset:61696\n"
    "v_fmac_f32 v115, v191, v155\n"
    "v_mfma_f32_16x16x32_bf16 v[92:95], v[244:247], a[100:103], v[92:95]\n"
    "ds_read_b128 a[240:243], v164 offset:12288\n"
    "v_fmac_f32 v96, v190, v152\n"
    "v_mfma_f32_16x16x32_bf16 v[92:95], v[248:251], a[104:107], v[92:95]\n"
    "ds_read_b128 v[156:159], v178 offset:64\n"
    "v_fmac_f32 v97, v190, v153\n"
    "v_mfma_f32_16x16x32_bf16 v[92:95], v[252:255], a[108:111], v[92:95]\n"
    "buffer_load_dword v187, v187, s[56:59], s30 offen\n"
    "v_fmac_f32 v98, v190, v154\n"
    "v_mfma_f32_16x16x32_bf16 v[76:79], v[252:255], a[76:79], v[76:79]\n"
    "ds_read_b128 a[244:247], v175 offset:12288\n"
    "v_fmac_f32 v99, v190, v155\n"
    "v_mfma_f32_16x16x32_bf16 v[76:79], v[248:251], a[72:75], v[76:79]\n"
    "v_fmac_f32 v80, v189, v152\n"
    "v_fmac_f32 v81, v189, v153\n"
    "v_fmac_f32 v82, v189, v154\n"
    "v_fmac_f32 v83, v189, v155\n"
    "v_fmac_f32 v64, v188, v152\n"
    "v_mfma_f32_16x16x32_bf16 v[76:79], v[244:247], a[68:71], v[76:79]\n"
    "v_fmac_f32 v65, v188, v153\n"
    "v_fmac_f32 v66, v188, v154\n"
    "v_fmac_f32 v67, v188, v155\n"
    "v_mfma_f32_16x16x32_bf16 v[76:79], v[240:243], a[64:67], v[76:79]\n"
    "s_waitcnt lgkmcnt(2)\n"
    "ds_read_b128 a[248:251], v164 offset:14336\n"
    "v_mfma_f32_16x16x32_bf16 v[64:67], v[192:195], a[80:83], v[64:67]\n"
    "s_waitcnt lgkmcnt(2)\n"
    "ds_read_b128 a[252:255], v175 offset:14336\n"
    "v_fmac_f32 v116, v191, v156\n"
    "v_mfma_f32_16x16x32_bf16 v[64:67], v[196:199], a[84:87], v[64:67]\n"
    "ds_read_b64_tr_b16 v[128:129], v179 offset:0\n"
    "v_fmac_f32 v117, v191, v157\n"
    "v_mfma_f32_16x16x32_bf16 v[64:67], v[200:203], a[88:91], v[64:67]\n"
    "ds_read_b64_tr_b16 v[130:131], v179 offset:256\n"
    "v_fmac_f32 v118, v191, v158\n"
    "v_mfma_f32_16x16x32_bf16 v[64:67], v[204:207], a[92:95], v[64:67]\n"
    "ds_read_b64_tr_b16 v[132:133], v179 offset:4096\n"
    "v_fmac_f32 v119, v191, v159\n"
    "v_mfma_f32_16x16x32_bf16 v[80:83], v[204:207], a[124:127], v[80:83]\n"
    "ds_read_b64_tr_b16 v[134:135], v179 offset:4352\n"
    "v_fmac_f32 v100, v190, v156\n"
    "v_fmac_f32 v101, v190, v157\n"
    "v_mfma_f32_16x16x32_bf16 v[80:83], v[200:203], a[120:123], v[80:83]\n"
    "ds_read_b64_tr_b16 v[136:137], v179 offset:8192\n"
    "v_fmac_f32 v102, v190, v158\n"
    "v_mfma_f32_16x16x32_bf16 v[80:83], v[196:199], a[116:119], v[80:83]\n"
    "ds_read_b64_tr_b16 v[138:139], v179 offset:8448\n"
    "v_fmac_f32 v103, v190, v159\n"
    "v_mfma_f32_16x16x32_bf16 v[80:83], v[192:195], a[112:115], v[80:83]\n"
    "ds_read_b64_tr_b16 v[140:141], v179 offset:12288\n"
    "v_fmac_f32 v84, v189, v156\n"
    "v_fmac_f32 v85, v189, v157\n"
    "v_mfma_f32_16x16x32_bf16 v[96:99], v[192:195], a[208:211], v[96:99]\n"
    "ds_read_b64_tr_b16 v[142:143], v179 offset:12544\n"
    "ds_read_b128 v[152:155], v178 offset:128\n"
    "v_mfma_f32_16x16x32_bf16 v[96:99], v[196:199], a[212:215], v[96:99]\n"
    "ds_read_b64_tr_b16 v[208:209], v174 offset:49152\n"
    "ds_read_b64_tr_b16 v[210:211], v174 offset:49408\n"
    "v_mfma_f32_16x16x32_bf16 v[96:99], v[200:203], a[216:219], v[96:99]\n"
    "ds_read_b64_tr_b16 v[212:213], v174 offset:53248\n"
    "ds_read_b64_tr_b16 v[214:215], v174 offset:53504\n"
    "v_fmac_f32 v86, v189, v158\n"
    "v_mfma_f32_16x16x32_bf16 v[96:99], v[204:207], a[220:223], v[96:99]\n"
    "s_waitcnt lgkmcnt(8)\n"
    "ds_read_b64_tr_b16 v[216:217], v174 offset:57344\n"
    "ds_read_b64_tr_b16 v[218:219], v174 offset:57600\n"
    "v_mfma_f32_16x16x32_bf16 v[112:115], v[204:207], a[252:255], v[112:115]\n"
    "ds_read_b64_tr_b16 v[220:221], v174 offset:61440\n"
    "ds_read_b64_tr_b16 v[222:223], v174 offset:61696\n"
    "v_mfma_f32_16x16x32_bf16 v[112:115], v[200:203], a[248:251], v[112:115]\n"
    "v_fmac_f32 v87, v189, v159\n"
    "v_fmac_f32 v68, v188, v156\n"
    "v_fmac_f32 v69, v188, v157\n"
    "v_mfma_f32_16x16x32_bf16 v[112:115], v[196:199], a[244:247], v[112:115]\n"
    "v_fmac_f32 v70, v188, v158\n"
    "v_fmac_f32 v71, v188, v159\n"
    "v_mfma_f32_16x16x32_bf16 v[112:115], v[192:195], a[240:243], v[112:115]\n"
    "s_waitcnt lgkmcnt(6)\n"
    "ds_read_b128 v[156:159], v178 offset:192\n"
    "v_fmac_f32 v120, v191, v152\n"
    "v_smfmac_f32_16x16x64_bf16 v[148:151], v[160:163], v[128:135], v183\n"
    "ds_read_b64_tr_b16 v[224:225], v173 offset:49664\n"
    "v_fmac_f32 v121, v191, v153\n"
    "v_fmac_f32 v122, v191, v154\n"
    "v_smfmac_f32_16x16x64_bf16 v[148:151], v[160:163], v[136:143], v183\n"
    "ds_read_b64_tr_b16 v[128:129], v179 offset:16384\n"
    "v_fmac_f32 v123, v191, v155\n"
    "v_mfma_f32_16x16x32_bf16 v[116:119], v[208:211], a[240:243], v[116:119]\n"
    "s_waitcnt lgkmcnt(3)\n"
    "ds_read_b64_tr_b16 v[130:131], v179 offset:16640\n"
    "v_fmac_f32 v104, v190, v152\n"
    "v_mfma_f32_16x16x32_bf16 v[116:119], v[212:215], a[244:247], v[116:119]\n"
    "ds_read_b64_tr_b16 v[132:133], v179 offset:20480\n"
    "v_fmac_f32 v105, v190, v153\n"
    "v_fmac_f32 v106, v190, v154\n"
    "v_mfma_f32_16x16x32_bf16 v[116:119], v[216:219], a[248:251], v[116:119]\n"
    "ds_read_b64_tr_b16 v[134:135], v179 offset:20736\n"
    "v_fmac_f32 v107, v190, v155\n"
    "v_mfma_f32_16x16x32_bf16 v[116:119], v[220:223], a[252:255], v[116:119]\n"
    "ds_read_b64_tr_b16 v[136:137], v179 offset:24576\n"
    "v_fmac_f32 v88, v189, v152\n"
    "v_mfma_f32_16x16x32_bf16 v[100:103], v[220:223], a[220:223], v[100:103]\n"
    "ds_read_b64_tr_b16 v[138:139], v179 offset:24832\n"
    "v_fmac_f32 v89, v189, v153\n"
    "v_fmac_f32 v90, v189, v154\n"
    "v_mfma_f32_16x16x32_bf16 v[100:103], v[216:219], a[216:219], v[100:103]\n"
    "ds_read_b64_tr_b16 v[140:141], v179 offset:28672\n"
    "v_fmac_f32 v91, v189, v155\n"
    "v_mfma_f32_16x16x32_bf16 v[100:103], v[212:215], a[212:215], v[100:103]\n"
    "ds_read_b64_tr_b16 v[142:143], v179 offset:28928\n"
    "v_fmac_f32 v72, v188, v152\n"
    "v_mfma_f32_16x16x32_bf16 v[100:103], v[208:211], a[208:211], v[100:103]\n"
    "ds_read_b64_tr_b16 v[226:227], v173 offset:49920\n"
    "v_fmac_f32 v73, v188, v153\n"
    "v_fmac_f32 v74, v188, v154\n"
    "v_mfma_f32_16x16x32_bf16 v[84:87], v[208:211], a[112:115], v[84:87]\n"
    "ds_read_b64_tr_b16 v[228:229], v173 offset:53760\n"
    "v_fmac_f32 v75, v188, v155\n"
    "v_mfma_f32_16x16x32_bf16 v[84:87], v[212:215], a[116:119], v[84:87]\n"
    "ds_read_b64_tr_b16 v[230:231], v173 offset:54016\n"
    "v_add_u32 v178, 0x20000, v178\n"
    "v_mfma_f32_16x16x32_bf16 v[84:87], v[216:219], a[120:123], v[84:87]\n"
    "s_waitcnt lgkmcnt(5)\n"
    "ds_read_b64_tr_b16 v[232:233], v173 offset:57856\n"
    "v_fmac_f32 v124, v191, v156\n"
    "v_fmac_f32 v125, v191, v157\n"
    "v_mfma_f32_16x16x32_bf16 v[84:87], v[220:223], a[124:127], v[84:87]\n"
    "ds_read_b64_tr_b16 v[234:235], v173 offset:58112\n"
    "v_fmac_f32 v126, v191, v158\n"
    "v_mfma_f32_16x16x32_bf16 v[68:71], v[220:223], a[92:95], v[68:71]\n"
    "ds_read_b64_tr_b16 v[236:237], v173 offset:61952\n"
    "ds_read_b64_tr_b16 v[238:239], v173 offset:62208\n"
    "v_mfma_f32_16x16x32_bf16 v[68:71], v[216:219], a[88:91], v[68:71]\n"
    "ds_read_b64_tr_b16 v[240:241], v174 offset:49664\n"
    "v_fmac_f32 v127, v191, v159\n"
    "v_fmac_f32 v108, v190, v156\n"
    "v_mfma_f32_16x16x32_bf16 v[68:71], v[212:215], a[84:87], v[68:71]\n"
    "ds_read_b64_tr_b16 v[242:243], v174 offset:49920\n"
    "v_fmac_f32 v109, v190, v157\n"
    "v_mfma_f32_16x16x32_bf16 v[68:71], v[208:211], a[80:83], v[68:71]\n"
    "ds_read_b64_tr_b16 v[244:245], v174 offset:53760\n"
    "v_fmac_f32 v110, v190, v158\n"
    "v_smfmac_f32_16x16x64_bf16 v[148:151], v[160:163], v[128:135], v183\n"
    "s_waitcnt lgkmcnt(6)\n"
    "ds_read_b64_tr_b16 v[246:247], v174 offset:54016\n"
    "v_fmac_f32 v111, v190, v159\n"
    "v_fmac_f32 v92, v189, v156\n"
    "v_smfmac_f32_16x16x64_bf16 v[148:151], v[160:163], v[136:143], v183\n"
    "ds_read_b64_tr_b16 v[248:249], v174 offset:57856\n"
    "v_fmac_f32 v93, v189, v157\n"
    "v_mfma_f32_16x16x32_bf16 v[72:75], v[224:227], a[80:83], v[72:75]\n"
    "ds_read_b64_tr_b16 v[250:251], v174 offset:58112\n"
    "v_fmac_f32 v94, v189, v158\n"
    "v_mfma_f32_16x16x32_bf16 v[72:75], v[228:231], a[84:87], v[72:75]\n"
    "s_waitcnt lgkmcnt(5)\n"
    "ds_read_b64_tr_b16 v[252:253], v174 offset:61952\n"
    "v_fmac_f32 v95, v189, v159\n"
    "v_fmac_f32 v76, v188, v156\n"
    "v_mfma_f32_16x16x32_bf16 v[72:75], v[232:235], a[88:91], v[72:75]\n"
    "ds_read_b64_tr_b16 v[254:255], v174 offset:62208\n"
    "v_fmac_f32 v77, v188, v157\n"
    "v_mfma_f32_16x16x32_bf16 v[72:75], v[236:239], a[92:95], v[72:75]\n"
    "v_fmac_f32 v78, v188, v158\n"
    "v_fmac_f32 v79, v188, v159\n"
    "v_mfma_f32_16x16x32_bf16 v[88:91], v[236:239], a[124:127], v[88:91]\n"
    "ds_read_b64_tr_b16 v[192:193], v173 offset:0\n"
    "v_add_u32 v175, 0xffff0000, v176\n"
    "v_mul_f32 v184, v172, v112\n"
    "v_mfma_f32_16x16x32_bf16 v[88:91], v[232:235], a[120:123], v[88:91]\n"
    "v_add_f32 v148, v148, v149\n"
    "v_lshl_add_u32 v150, v181, 2, s93\n"
    "v_mfma_f32_16x16x32_bf16 v[88:91], v[228:231], a[116:119], v[88:91]\n"
    "s_nop 0\n"
    "s_mov_b64 exec, 0xffff\n"
    "ds_write_b32 v150, v148 offset:0\n"
    "s_mov_b64 exec, -1\n"
    "ds_read_b64_tr_b16 v[194:195], v173 offset:256\n"
    "v_mul_f32 v185, v172, v113\n"
    "v_mfma_f32_16x16x32_bf16 v[88:91], v[224:227], a[112:115], v[88:91]\n"
    "ds_read_b64_tr_b16 v[196:197], v173 offset:4096\n"
    "v_mul_f32 v186, v172, v114\n"
    "v_mfma_f32_16x16x32_bf16 v[104:107], v[224:227], a[208:211], v[104:107]\n"
    "ds_read_b64_tr_b16 v[198:199], v173 offset:4352\n"
    "v_mfma_f32_16x16x32_bf16 v[104:107], v[228:231], a[212:215], v[104:107]\n"
    "s_waitcnt lgkmcnt(3)\n"
    "v_exp_f32 v184, v184\n"
    "v_mfma_f32_16x16x32_bf16 v[104:107], v[232:235], a[216:219], v[104:107]\n"
    "s_barrier\n"
    "ds_read_b64_tr_b16 v[200:201], v173 offset:8192\n"
    "v_exp_f32 v185, v185\n"
    "v_mfma_f32_16x16x32_bf16 v[104:107], v[236:239], a[220:223], v[104:107]\n"
    "s_add_u32 m0, s23, 0\n"
    "ds_read_b128 v[152:155], v178 offset:0\n"
    "v_fma_f32 v184, v184, v170, v170\n"
    "v_mfma_f32_16x16x32_bf16 v[120:123], v[236:239], a[252:255], v[120:123]\n"
    "s_add_u32 s84, s85, 0\n"
    "ds_read_b64_tr_b16 v[202:203], v173 offset:8448\n"
    "v_fma_f32 v185, v185, v170, v170\n"
    "v_mfma_f32_16x16x32_bf16 v[120:123], v[232:235], a[248:251], v[120:123]\n"
    "buffer_load_dwordx4 v177, s[56:59], s84 offen lds\n"
    "v_exp_f32 v186, v186\n"
    "v_mfma_f32_16x16x32_bf16 v[120:123], v[228:231], a[244:247], v[120:123]\n"
    "s_add_u32 m0, s23, 4096\n"
    "ds_read_b64_tr_b16 v[204:205], v173 offset:12288\n"
    "v_fma_f32 v186, v186, v170, v170\n"
    "v_mfma_f32_16x16x32_bf16 v[120:123], v[224:227], a[240:243], v[120:123]\n"
    "s_waitcnt lgkmcnt(4)\n"
    "s_add_u32 s84, s85, 262144\n"
    "ds_read_b128 v[156:159], v178 offset:64\n"
    "v_mfma_f32_16x16x32_bf16 v[124:127], v[240:243], a[240:243], v[124:127]\n"
    "buffer_load_dwordx4 v177, s[56:59], s84 offen lds\n"
    "v_rcp_f32 v184, v184\n"
    "v_mfma_f32_16x16x32_bf16 v[124:127], v[244:247], a[244:247], v[124:127]\n"
    "ds_read_b64_tr_b16 v[206:207], v173 offset:12544\n"
    "v_mul_f32 v112, v112, v184\n"
    "v_mfma_f32_16x16x32_bf16 v[124:127], v[248:251], a[248:251], v[124:127]\n"
    "s_add_u32 m0, s23, 8192\n"
    "ds_read_b64_tr_b16 v[208:209], v173 offset:16384\n"
    "v_mul_f32 v184, v172, v115\n"
    "v_mfma_f32_16x16x32_bf16 v[124:127], v[252:255], a[252:255], v[124:127]\n"
    "s_add_u32 s84, s85, 524288\n"
    "v_exp_f32 v184, v184\n"
    "v_mfma_f32_16x16x32_bf16 v[108:111], v[252:255], a[220:223], v[108:111]\n"
    "buffer_load_dwordx4 v177, s[56:59], s84 offen lds\n"
    "v_fma_f32 v184, v184, v170, v170\n"
    "v_mfma_f32_16x16x32_bf16 v[108:111], v[248:251], a[216:219], v[108:111]\n"
    "s_waitcnt lgkmcnt(2)\n"
    "s_add_u32 m0, s23, 12288\n"
    "ds_read_b64_tr_b16 v[210:211], v173 offset:16640\n"
    "v_fmac_f32 v52, v191, v156\n"
    "v_mfma_f32_16x16x32_bf16 v[108:111], v[244:247], a[212:215], v[108:111]\n"
    "s_add_u32 s84, s85, 786432\n"
    "ds_read_b64_tr_b16 v[212:213], v173 offset:20480\n"
    "v_fmac_f32 v53, v191, v157\n"
    "v_fmac_f32 v54, v191, v158\n"
    "v_mfma_f32_16x16x32_bf16 v[108:111], v[240:243], a[208:211], v[108:111]\n"
    "buffer_load_dwordx4 v177, s[56:59], s84 offen lds\n"
    "v_fmac_f32 v55, v191, v159\n"
    "v_mfma_f32_16x16x32_bf16 v[92:95], v[240:243], a[112:115], v[92:95]\n"
    "ds_read_b64_tr_b16 v[214:215], v173 offset:20736\n"
    "v_fmac_f32 v36, v190, v156\n"
    "v_mfma_f32_16x16x32_bf16 v[92:95], v[244:247], a[116:119], v[92:95]\n"
    "s_add_u32 m0, s23, 16384\n"
    "v_fmac_f32 v37, v190, v157\n"
    "v_fmac_f32 v38, v190, v158\n"
    "v_fmac_f32 v39, v190, v159\n"
    "v_mfma_f32_16x16x32_bf16 v[92:95], v[248:251], a[120:123], v[92:95]\n"
    "s_add_u32 s84, s85, 1048576\n"
    "ds_read_b64_tr_b16 v[216:217], v173 offset:24576\n"
    "v_fmac_f32 v20, v189, v156\n"
    "v_mfma_f32_16x16x32_bf16 v[92:95], v[252:255], a[124:127], v[92:95]\n"
    "ds_read_b64_tr_b16 v[218:219], v173 offset:24832\n"
    "v_fmac_f32 v21, v189, v157\n"
    "v_mfma_f32_16x16x32_bf16 v[76:79], v[252:255], a[92:95], v[76:79]\n"
    "ds_read_b64_tr_b16 v[220:221], v173 offset:28672\n"
    "v_fmac_f32 v22, v189, v158\n"
    "v_fmac_f32 v23, v189, v159\n"
    "v_mfma_f32_16x16x32_bf16 v[76:79], v[248:251], a[88:91], v[76:79]\n"
    "ds_read_b64_tr_b16 v[222:223], v173 offset:28928\n"
    "v_fmac_f32 v4, v188, v156\n"
    "v_mfma_f32_16x16x32_bf16 v[76:79], v[244:247], a[84:87], v[76:79]\n"
    "buffer_load_dwordx4 v177, s[56:59], s84 offen lds\n"
    "v_fmac_f32 v5, v188, v157\n"
    "v_mfma_f32_16x16x32_bf16 v[76:79], v[240:243], a[80:83], v[76:79]\n"
    "s_add_u32 m0, s23, 20480\n"
    "ds_read_b64_tr_b16 v[224:225], v174 offset:0\n"
    "v_fmac_f32 v6, v188, v158\n"
    "v_fmac_f32 v7, v188, v159\n"
    "v_mfma_f32_16x16x32_bf16 v[48:51], v[192:195], a[224:227], v[48:51]\n"
    "s_add_u32 s84, s85, 1310720\n"
    "ds_read_b64_tr_b16 v[226:227], v174 offset:256\n"
    "v_mfma_f32_16x16x32_bf16 v[48:51], v[196:199], a[228:231], v[48:51]\n"
    "buffer_load_dwordx4 v177, s[56:59], s84 offen lds\n"
    "v_mfma_f32_16x16x32_bf16 v[48:51], v[200:203], a[232:235], v[48:51]\n"
    "s_waitcnt lgkmcnt(3)\n"
    "s_add_u32 m0, s23, 24576\n"
    "ds_read_b64_tr_b16 v[228:229], v174 offset:4096\n"
    "v_rcp_f32 v185, v185\n"
    "v_mfma_f32_16x16x32_bf16 v[48:51], v[204:207], a[236:239], v[48:51]\n"
    "s_add_u32 s84, s85, 1572864\n"
    "v_rcp_f32 v186, v186\n"
    "v_mfma_f32_16x16x32_bf16 v[48:51], v[208:211], a[240:243], v[48:51]\n"
    "buffer_load_dwordx4 v177, s[56:59], s84 offen lds\n"
    "v_mul_f32 v113, v113, v185\n"
    "v_mfma_f32_16x16x32_bf16 v[48:51], v[212:215], a[244:247], v[48:51]\n"
    "s_add_u32 m0, s23, 28672\n"
    "ds_read_b64_tr_b16 v[230:231], v174 offset:4352\n"
    "v_rcp_f32 v184, v184\n"
    "v_mfma_f32_16x16x32_bf16 v[48:51], v[216:219], a[248:251], v[48:51]\n"
    "s_waitcnt lgkmcnt(2)\n"
    "s_add_u32 s84, s85, 1835008\n"
    "ds_read_b64_tr_b16 v[232:233], v174 offset:8192\n"
    "v_mul_f32 v114, v114, v186\n"
    "v_mfma_f32_16x16x32_bf16 v[48:51], v[220:223], a[252:255], v[48:51]\n"
    "buffer_load_dwordx4 v177, s[56:59], s84 offen lds\n"
    "v_mul_f32 v115, v115, v184\n"
    "v_mfma_f32_16x16x32_bf16 v[32:35], v[220:223], a[220:223], v[32:35]\n"
    "s_add_u32 m0, s23, 32768\n"
    "ds_read_b64_tr_b16 v[234:235], v174 offset:8448\n"
    "v_mul_f32 v185, v171, v96\n"
    "v_mul_f32 v186, v171, v97\n"
    "v_mfma_f32_16x16x32_bf16 v[32:35], v[216:219], a[216:219], v[32:35]\n"
    "s_add_u32 s84, s85, 2097152\n"
    "v_mul_f32 v184, v171, v98\n"
    "v_mfma_f32_16x16x32_bf16 v[32:35], v[212:215], a[212:215], v[32:35]\n"
    "buffer_load_dwordx4 v177, s[56:59], s84 offen lds\n"
    "v_mfma_f32_16x16x32_bf16 v[32:35], v[208:211], a[208:211], v[32:35]\n"
    "s_add_u32 m0, s23, 36864\n"
    "ds_read_b64_tr_b16 v[236:237], v174 offset:12288\n"
    "v_exp_f32 v185, v185\n"
    "v_mfma_f32_16x16x32_bf16 v[32:35], v[204:207], a[204:207], v[32:35]\n"
    "s_add_u32 s84, s85, 2359296\n"
    "ds_read_b64_tr_b16 v[238:239], v174 offset:12544\n"
    "v_fma_f32 v185, v185, v169, v169\n"
    "v_mfma_f32_16x16x32_bf16 v[32:35], v[200:203], a[200:203], v[32:35]\n"
    "v_fmac_f32 v48, v191, v152\n"
    "v_fmac_f32 v49, v191, v153\n"
    "v_mfma_f32_16x16x32_bf16 v[32:35], v[196:199], a[196:199], v[32:35]\n"
    "buffer_load_dwordx4 v177, s[56:59], s84 offen lds\n"
    "v_fmac_f32 v50, v191, v154\n"
    "v_fmac_f32 v51, v191, v155\n"
    "v_mfma_f32_16x16x32_bf16 v[32:35], v[192:195], a[192:195], v[32:35]\n"
    "s_add_u32 m0, s23, 40960\n"
    "ds_read_b64_tr_b16 v[240:241], v174 offset:16384\n"
    "v_mul_f32 v48, v112, v48\n"
    "v_mfma_f32_16x16x32_bf16 v[16:19], v[192:195], a[96:99], v[16:19]\n"
    "s_add_u32 s84, s85, 2621440\n"
    "ds_read_b64_tr_b16 v[242:243], v174 offset:16640\n"
    "v_mul_f32 v49, v113, v49\n"
    "v_mfma_f32_16x16x32_bf16 v[16:19], v[196:199], a[100:103], v[16:19]\n"
    "buffer_load_dwordx4 v177, s[56:59], s84 offen lds\n"
    "v_exp_f32 v186, v186\n"
    "v_mfma_f32_16x16x32_bf16 v[16:19], v[200:203], a[104:107], v[16:19]\n"
    "s_add_u32 m0, s23, 45056\n"
    "ds_read_b64_tr_b16 v[244:245], v174 offset:20480\n"
    "v_fma_f32 v186, v186, v169, v169\n"
    "v_mfma_f32_16x16x32_bf16 v[16:19], v[204:207], a[108:111], v[16:19]\n"
    "s_add_u32 s84, s85, 2883584\n"
    "v_exp_f32 v184, v184\n"
    "v_mfma_f32_16x16x32_bf16 v[16:19], v[208:211], a[112:115], v[16:19]\n"
    "buffer_load_dwordx4 v177, s[56:59], s84 offen lds\n"
    "v_fma_f32 v184, v184, v169, v169\n"
    "v_mul_f32 v50, v114, v50\n"
    "v_mfma_f32_16x16x32_bf16 v[16:19], v[212:215], a[116:119], v[16:19]\n"
    "s_add_u32 m0, s23, 49152\n"
    "v_fmac_f32 v32, v190, v152\n"
    "v_fmac_f32 v33, v190, v153\n"
    "v_mfma_f32_16x16x32_bf16 v[16:19], v[216:219], a[120:123], v[16:19]\n"
    "s_add_u32 s84, s85, 3145728\n"
    "v_fmac_f32 v34, v190, v154\n"
    "v_fmac_f32 v35, v190, v155\n"
    "v_mfma_f32_16x16x32_bf16 v[16:19], v[220:223], a[124:127], v[16:19]\n"
    "ds_read_b64_tr_b16 v[246:247], v174 offset:20736\n"
    "v_rcp_f32 v185, v185\n"
    "v_mfma_f32_16x16x32_bf16 v[0:3], v[220:223], a[92:95], v[0:3]\n"
    "ds_read_b64_tr_b16 v[248:249], v174 offset:24576\n"
    "v_mul_f32 v96, v96, v185\n"
    "v_mfma_f32_16x16x32_bf16 v[0:3], v[216:219], a[88:91], v[0:3]\n"
    "ds_read_b64_tr_b16 v[250:251], v174 offset:24832\n"
    "v_mul_f32 v185, v171, v99\n"
    "v_mfma_f32_16x16x32_bf16 v[0:3], v[212:215], a[84:87], v[0:3]\n"
    "buffer_load_dwordx4 v177, s[56:59], s84 offen lds\n"
    "v_exp_f32 v185, v185\n"
    "v_mfma_f32_16x16x32_bf16 v[0:3], v[208:211], a[80:83], v[0:3]\n"
    "s_add_u32 m0, s23, 53248\n"
    "ds_read_b64_tr_b16 v[252:253], v174 offset:28672\n"
    "v_fma_f32 v185, v185, v169, v169\n"
    "v_mfma_f32_16x16x32_bf16 v[0:3], v[204:207], a[76:79], v[0:3]\n"
    "s_add_u32 s84, s85, 3407872\n"
    "ds_read_b64_tr_b16 v[254:255], v174 offset:28928\n"
    "v_mul_f32 v51, v115, v51\n"
    "v_mfma_f32_16x16x32_bf16 v[0:3], v[200:203], a[72:75], v[0:3]\n"
    "v_fmac_f32 v16, v189, v152\n"
    "v_fmac_f32 v17, v189, v153\n"
    "v_fmac_f32 v18, v189, v154\n"
    "v_mfma_f32_16x16x32_bf16 v[0:3], v[196:199], a[68:71], v[0:3]\n"
    "buffer_load_dwordx4 v177, s[56:59], s84 offen lds\n"
    "v_fmac_f32 v19, v189, v155\n"
    "v_mfma_f32_16x16x32_bf16 v[0:3], v[192:195], a[64:67], v[0:3]\n"
    "s_add_u32 m0, s23, 57344\n"
    "v_rcp_f32 v186, v186\n"
    "v_mfma_f32_16x16x32_bf16 v[4:7], v[224:227], a[64:67], v[4:7]\n"
    "s_waitcnt lgkmcnt(2)\n"
    "s_add_u32 s84, s85, 3670016\n"
    "v_rcp_f32 v184, v184\n"
    "v_mul_f32 v97, v97, v186\n"
    "v_mfma_f32_16x16x32_bf16 v[4:7], v[228:231], a[68:71], v[4:7]\n"
    "buffer_load_dwordx4 v177, s[56:59], s84 offen lds\n"
    "v_mul_f32 v98, v98, v184\n"
    "v_mfma_f32_16x16x32_bf16 v[4:7], v[232:235], a[72:75], v[4:7]\n"
    "s_add_u32 m0, s23, 61440\n"
    "ds_read_b64_tr_b16 v[192:193], v173 offset:512\n"
    "v_mul_f32 v186, v166, v80\n"
    "v_mfma_f32_16x16x32_bf16 v[4:7], v[236:239], a[76:79], v[4:7]\n"
    "s_add_u32 s84, s85, 3932160\n"
    "ds_read_b64_tr_b16 v[194:195], v173 offset:768\n"
    "v_rcp_f32 v185, v185\n"
    "v_mfma_f32_16x16x32_bf16 v[4:7], v[240:243], a[80:83], v[4:7]\n"
    "buffer_load_dwordx4 v177, s[56:59], s84 offen lds\n"
    "v_mul_f32 v99, v99, v185\n"
    "v_mfma_f32_16x16x32_bf16 v[4:7], v[244:247], a[84:87], v[4:7]\n"
    "s_add_u32 m0, s23, 98304\n"
    "v_fmac_f32 v0, v188, v152\n"
    "v_fmac_f32 v1, v188, v153\n"
    "v_mfma_f32_16x16x32_bf16 v[4:7], v[248:251], a[88:91], v[4:7]\n"
    "s_waitcnt lgkmcnt(2)\n"
    "s_add_u32 s84, s85, 128\n"
    "ds_read_b64_tr_b16 v[196:197], v173 offset:4608\n"
    "v_fmac_f32 v2, v188, v154\n"
    "v_fmac_f32 v3, v188, v155\n"
    "v_mfma_f32_16x16x32_bf16 v[4:7], v[252:255], a[92:95], v[4:7]\n"
    "ds_read_b128 v[152:155], v178 offset:128\n"
    "v_mul_f32 v184, v166, v81\n"
    "v_mfma_f32_16x16x32_bf16 v[20:23], v[252:255], a[124:127], v[20:23]\n"
    "buffer_load_dwordx4 v177, s[56:59], s84 offen lds\n"
    "v_mul_f32 v185, v166, v82\n"
    "v_mfma_f32_16x16x32_bf16 v[20:23], v[248:251], a[120:123], v[20:23]\n"
    "ds_read_b64_tr_b16 v[198:199], v173 offset:4864\n"
    "v_exp_f32 v186, v186\n"
    "v_mfma_f32_16x16x32_bf16 v[20:23], v[244:247], a[116:119], v[20:23]\n"
    "ds_read_b64_tr_b16 v[200:201], v173 offset:8704\n"
    "v_fma_f32 v186, v186, v168, v168\n"
    "v_mfma_f32_16x16x32_bf16 v[20:23], v[240:243], a[112:115], v[20:23]\n"
    "ds_read_b64_tr_b16 v[202:203], v173 offset:8960\n"
    "v_cvt_pk_bf16_f32 v112, v48, v49\n"
    "v_mfma_f32_16x16x32_bf16 v[20:23], v[236:239], a[108:111], v[20:23]\n"
    "s_add_u32 m0, s23, 102400\n"
    "ds_read_b128 v[156:159], v178 offset:192\n"
    "v_exp_f32 v184, v184\n"
    "v_mfma_f32_16x16x32_bf16 v[20:23], v[232:235], a[104:107], v[20:23]\n"
    "s_waitcnt lgkmcnt(4)\n"
    "s_add_u32 s84, s85, 262272\n"
    "v_fmac_f32 v56, v191, v152\n"
    "v_fmac_f32 v57, v191, v153\n"
    "v_mfma_f32_16x16x32_bf16 v[20:23], v[228:231], a[100:103], v[20:23]\n"
    "buffer_load_dwordx4 v177, s[56:59], s84 offen lds\n"
    "v_fmac_f32 v58, v191, v154\n"
    "v_mfma_f32_16x16x32_bf16 v[20:23], v[224:227], a[96:99], v[20:23]\n"
    "ds_read_b64_tr_b16 v[204:205], v173 offset:12800\n"
    "v_fmac_f32 v59, v191, v155\n"
    "v_fmac_f32 v40, v190, v152\n"
    "v_mfma_f32_16x16x32_bf16 v[36:39], v[224:227], a[192:195], v[36:39]\n"
    "ds_read_b64_tr_b16 v[206:207], v173 offset:13056\n"
    "v_fmac_f32 v41, v190, v153\n"
    "v_mfma_f32_16x16x32_bf16 v[36:39], v[228:231], a[196:199], v[36:39]\n"
    "ds_read_b64_tr_b16 v[208:209], v173 offset:16896\n"
    "v_fmac_f32 v42, v190, v154\n"
    "v_mfma_f32_16x16x32_bf16 v[36:39], v[232:235], a[200:203], v[36:39]\n"
    "v_fmac_f32 v43, v190, v155\n"
    "v_fmac_f32 v24, v189, v152\n"
    "v_fmac_f32 v25, v189, v153\n"
    "v_mfma_f32_16x16x32_bf16 v[36:39], v[236:239], a[204:207], v[36:39]\n"
    "ds_read_b64_tr_b16 v[210:211], v173 offset:17152\n"
    "v_fmac_f32 v26, v189, v154\n"
    "v_mfma_f32_16x16x32_bf16 v[36:39], v[240:243], a[208:211], v[36:39]\n"
    "s_add_u32 m0, s23, 106496\n"
    "v_fmac_f32 v27, v189, v155\n"
    "v_fmac_f32 v8, v188, v152\n"
    "v_mfma_f32_16x16x32_bf16 v[36:39], v[244:247], a[212:215], v[36:39]\n"
    "s_add_u32 s84, s85, 524416\n"
    "ds_read_b64_tr_b16 v[212:213], v173 offset:20992\n"
    "v_fmac_f32 v9, v188, v153\n"
    "v_fmac_f32 v10, v188, v154\n"
    "v_mfma_f32_16x16x32_bf16 v[36:39], v[248:251], a[216:219], v[36:39]\n"
    "buffer_load_dwordx4 v177, s[56:59], s84 offen lds\n"
    "v_fmac_f32 v11, v188, v155\n"
    "v_mfma_f32_16x16x32_bf16 v[36:39], v[252:255], a[220:223], v[36:39]\n"
    "s_waitcnt lgkmcnt(2)\n"
    "ds_read_b64_tr_b16 v[214:215], v173 offset:21248\n"
    "v_fmac_f32 v60, v191, v156\n"
    "v_mfma_f32_16x16x32_bf16 v[52:55], v[252:255], a[252:255], v[52:55]\n"
    "v_fmac_f32 v61, v191, v157\n"
    "v_fmac_f32 v62, v191, v158\n"
    "v_fmac_f32 v63, v191, v159\n"
    "v_mfma_f32_16x16x32_bf16 v[52:55], v[248:251], a[248:251], v[52:55]\n"
    "ds_read_b64_tr_b16 v[216:217], v173 offset:25088\n"
    "v_fmac_f32 v44, v190, v156\n"
    "v_mfma_f32_16x16x32_bf16 v[52:55], v[244:247], a[244:247], v[52:55]\n"
    "v_fmac_f32 v45, v190, v157\n"
    "v_fmac_f32 v46, v190, v158\n"
    "v_mfma_f32_16x16x32_bf16 v[52:55], v[240:243], a[240:243], v[52:55]\n"
    "s_add_u32 m0, s23, 110592\n"
    "ds_read_b64_tr_b16 v[218:219], v173 offset:25344\n"
    "v_fmac_f32 v47, v190, v159\n"
    "v_fmac_f32 v28, v189, v156\n"
    "v_mfma_f32_16x16x32_bf16 v[52:55], v[236:239], a[236:239], v[52:55]\n"
    "s_add_u32 s84, s85, 786560\n"
    "ds_read_b64_tr_b16 v[220:221], v173 offset:29184\n"
    "v_fmac_f32 v29, v189, v157\n"
    "v_mfma_f32_16x16x32_bf16 v[52:55], v[232:235], a[232:235], v[52:55]\n"
    "ds_read_b64_tr_b16 v[222:223], v173 offset:29440\n"
    "v_fmac_f32 v30, v189, v158\n"
    "v_mfma_f32_16x16x32_bf16 v[52:55], v[228:231], a[228:231], v[52:55]\n"
    "buffer_load_dwordx4 v177, s[56:59], s84 offen lds\n"
    "v_fmac_f32 v31, v189, v159\n"
    "v_fmac_f32 v12, v188, v156\n"
    "v_mfma_f32_16x16x32_bf16 v[52:55], v[224:227], a[224:227], v[52:55]\n"
    "v_fmac_f32 v13, v188, v157\n"
    "v_fmac_f32 v14, v188, v158\n"
    "v_mfma_f32_16x16x32_bf16 v[56:59], v[192:195], a[224:227], v[56:59]\n"
    "v_fmac_f32 v15, v188, v159\n"
    "v_fma_f32 v184, v184, v168, v168\n"
    "v_mfma_f32_16x16x32_bf16 v[56:59], v[196:199], a[228:231], v[56:59]\n"
    "ds_read_b64_tr_b16 v[224:225], v174 offset:512\n"
    "v_exp_f32 v185, v185\n"
    "v_mfma_f32_16x16x32_bf16 v[56:59], v[200:203], a[232:235], v[56:59]\n"
    "v_fma_f32 v185, v185, v168, v168\n"
    "v_cvt_pk_bf16_f32 v113, v50, v51\n"
    "v_mfma_f32_16x16x32_bf16 v[56:59], v[204:207], a[236:239], v[56:59]\n"
    "s_waitcnt lgkmcnt(1)\n"
    "s_add_u32 m0, s23, 114688\n"
    "ds_read_b64_tr_b16 v[226:227], v174 offset:768\n"
    "v_mul_f32 v32, v96, v32\n"
    "v_mfma_f32_16x16x32_bf16 v[56:59], v[208:211], a[240:243], v[56:59]\n"
    "s_add_u32 s84, s85, 1048704\n"
    "v_rcp_f32 v186, v186\n"
    "v_mul_f32 v33, v97, v33\n"
    "v_mfma_f32_16x16x32_bf16 v[56:59], v[212:215], a[244:247], v[56:59]\n"
    "buffer_load_dwordx4 v177, s[56:59], s84 offen lds\n"
    "v_mul_f32 v80, v80, v186\n"
    "v_mfma_f32_16x16x32_bf16 v[56:59], v[216:219], a[248:251], v[56:59]\n"
    "ds_read_b64_tr_b16 v[228:229], v174 offset:4608\n"
    "v_mul_f32 v186, v166, v83\n"
    "v_mfma_f32_16x16x32_bf16 v[56:59], v[220:223], a[252:255], v[56:59]\n"
    "ds_read_b64_tr_b16 v[230:231], v174 offset:4864\n"
    "v_exp_f32 v186, v186\n"
    "v_mfma_f32_16x16x32_bf16 v[40:43], v[220:223], a[220:223], v[40:43]\n"
    "v_fma_f32 v186, v186, v168, v168\n"
    "v_mul_f32 v34, v98, v34\n"
    "v_mfma_f32_16x16x32_bf16 v[40:43], v[216:219], a[216:219], v[40:43]\n"
    "ds_read_b64_tr_b16 v[232:233], v174 offset:8704\n"
    "v_mul_f32 v35, v99, v35\n"
    "v_mfma_f32_16x16x32_bf16 v[40:43], v[212:215], a[212:215], v[40:43]\n"
    "s_add_u32 m0, s23, 118784\n"
    "v_rcp_f32 v184, v184\n"
    "v_cvt_pk_bf16_f32 v96, v32, v33\n"
    "v_mfma_f32_16x16x32_bf16 v[40:43], v[208:211], a[208:211], v[40:43]\n"
    "s_add_u32 s84, s85, 1310848\n"
    "ds_read_b64_tr_b16 v[234:235], v174 offset:8960\n"
    "v_mul_f32 v81, v81, v184\n"
    "v_mfma_f32_16x16x32_bf16 v[40:43], v[204:207], a[204:207], v[40:43]\n"
    "buffer_load_dwordx4 v177, s[56:59], s84 offen lds\n"
    "v_mul_f32 v184, v165, v64\n"
    "v_mfma_f32_16x16x32_bf16 v[40:43], v[200:203], a[200:203], v[40:43]\n"
    "ds_read_b64_tr_b16 v[236:237], v174 offset:12800\n"
    "v_rcp_f32 v185, v185\n"
    "v_mfma_f32_16x16x32_bf16 v[40:43], v[196:199], a[196:199], v[40:43]\n"
    "v_rcp_f32 v186, v186\n"
    "v_mfma_f32_16x16x32_bf16 v[40:43], v[192:195], a[192:195], v[40:43]\n"
    "ds_read_b64_tr_b16 v[238:239], v174 offset:13056\n"
    "v_mul_f32 v82, v82, v185\n"
    "v_mfma_f32_16x16x32_bf16 v[24:27], v[192:195], a[96:99], v[24:27]\n"
    "v_mul_f32 v83, v83, v186\n"
    "v_mul_f32 v185, v165, v65\n"
    "v_cvt_pk_bf16_f32 v97, v34, v35\n"
    "v_mfma_f32_16x16x32_bf16 v[24:27], v[196:199], a[100:103], v[24:27]\n"
    "ds_read_b64_tr_b16 v[240:241], v174 offset:16896\n"
    "v_mul_f32 v186, v165, v66\n"
    "v_mfma_f32_16x16x32_bf16 v[24:27], v[200:203], a[104:107], v[24:27]\n"
    "s_add_u32 m0, s23, 122880\n"
    "v_exp_f32 v184, v184\n"
    "v_mfma_f32_16x16x32_bf16 v[24:27], v[204:207], a[108:111], v[24:27]\n"
    "s_add_u32 s84, s85, 1572992\n"
    "ds_read_b64_tr_b16 v[242:243], v174 offset:17152\n"
    "v_exp_f32 v185, v185\n"
    "v_mfma_f32_16x16x32_bf16 v[24:27], v[208:211], a[112:115], v[24:27]\n"
    "buffer_load_dwordx4 v177, s[56:59], s84 offen lds\n"
    "v_fma_f32 v184, v184, v167, v167\n"
    "v_mfma_f32_16x16x32_bf16 v[24:27], v[212:215], a[116:119], v[24:27]\n"
    "ds_read_b64_tr_b16 v[244:245], v174 offset:20992\n"
    "v_fma_f32 v185, v185, v167, v167\n"
    "v_mfma_f32_16x16x32_bf16 v[24:27], v[216:219], a[120:123], v[24:27]\n"
    "v_exp_f32 v186, v186\n"
    "v_mul_f32 v16, v80, v16\n"
    "v_mfma_f32_16x16x32_bf16 v[24:27], v[220:223], a[124:127], v[24:27]\n"
    "ds_read_b64_tr_b16 v[246:247], v174 offset:21248\n"
    "v_fma_f32 v186, v186, v167, v167\n"
    "v_mfma_f32_16x16x32_bf16 v[8:11], v[220:223], a[92:95], v[8:11]\n"
    "v_rcp_f32 v184, v184\n"
    "v_mfma_f32_16x16x32_bf16 v[8:11], v[216:219], a[88:91], v[8:11]\n"
    "s_add_u32 m0, s23, 126976\n"
    "ds_read_b64_tr_b16 v[248:249], v174 offset:25088\n"
    "v_rcp_f32 v185, v185\n"
    "v_mfma_f32_16x16x32_bf16 v[8:11], v[212:215], a[84:87], v[8:11]\n"
    "s_add_u32 s84, s85, 1835136\n"
    "v_rcp_f32 v186, v186\n"
    "v_mfma_f32_16x16x32_bf16 v[8:11], v[208:211], a[80:83], v[8:11]\n"
    "ds_read_b64_tr_b16 v[250:251], v174 offset:25344\n"
    "v_mul_f32 v64, v64, v184\n"
    "v_mfma_f32_16x16x32_bf16 v[8:11], v[204:207], a[76:79], v[8:11]\n"
    "ds_read_b64_tr_b16 v[252:253], v174 offset:29184\n"
    "v_mul_f32 v184, v165, v67\n"
    "v_mul_f32 v65, v65, v185\n"
    "v_mfma_f32_16x16x32_bf16 v[8:11], v[200:203], a[72:75], v[8:11]\n"
    "ds_read_b64_tr_b16 v[254:255], v174 offset:29440\n"
    "v_mul_f32 v66, v66, v186\n"
    "v_mfma_f32_16x16x32_bf16 v[8:11], v[196:199], a[68:71], v[8:11]\n"
    "buffer_load_dwordx4 v177, s[56:59], s84 offen lds\n"
    "v_mul_f32 v17, v81, v17\n"
    "v_mfma_f32_16x16x32_bf16 v[8:11], v[192:195], a[64:67], v[8:11]\n"
    "s_waitcnt lgkmcnt(3)\n"
    "s_lshl_b32 s30, s90, 17\n"
    "s_lshl_b32 s31, s26, 12\n"
    "s_add_u32 s30, s30, s31\n"
    "v_exp_f32 v184, v184\n"
    "v_mul_f32 v18, v82, v18\n"
    "v_mfma_f32_16x16x32_bf16 v[12:15], v[224:227], a[64:67], v[12:15]\n"
    "s_lshl_b32 s31, s90, 8\n"
    "v_fma_f32 v184, v184, v167, v167\n"
    "v_mul_f32 v19, v83, v19\n"
    "v_mfma_f32_16x16x32_bf16 v[12:15], v[228:231], a[68:71], v[12:15]\n"
    "v_rcp_f32 v184, v184\n"
    "v_mfma_f32_16x16x32_bf16 v[12:15], v[232:235], a[72:75], v[12:15]\n"
    "v_mul_f32 v67, v67, v184\n"
    "v_cvt_pk_bf16_f32 v80, v16, v17\n"
    "v_cvt_pk_bf16_f32 v81, v18, v19\n"
    "v_mfma_f32_16x16x32_bf16 v[12:15], v[236:239], a[76:79], v[12:15]\n"
    "v_mul_f32 v0, v64, v0\n"
    "v_mul_f32 v1, v65, v1\n"
    "v_mfma_f32_16x16x32_bf16 v[12:15], v[240:243], a[80:83], v[12:15]\n"
    "v_mul_f32 v2, v66, v2\n"
    "v_mul_f32 v3, v67, v3\n"
    "v_mfma_f32_16x16x32_bf16 v[12:15], v[244:247], a[84:87], v[12:15]\n"
    "s_waitcnt vmcnt(8) lgkmcnt(0)\n"
    "v_cvt_pk_bf16_f32 v64, v0, v1\n"
    "v_cvt_pk_bf16_f32 v65, v2, v3\n"
    "v_mul_f32 v185, v172, v116\n"
    "v_mfma_f32_16x16x32_bf16 v[12:15], v[248:251], a[88:91], v[12:15]\n"
    "s_barrier\n"
    "v_mul_f32 v186, v172, v117\n"
    "v_mul_f32 v184, v172, v118\n"
    "v_mfma_f32_16x16x32_bf16 v[12:15], v[252:255], a[92:95], v[12:15]\n"
    "s_add_u32 m0, s23, 65536\n"
    "buffer_load_dword v164, v180, s[8:11], s31 offen offset:3072\n"
    "v_mfma_f32_16x16x32_bf16 v[28:31], v[252:255], a[124:127], v[28:31]\n"
    "s_add_u32 s84, s85, 2097280\n"
    "buffer_load_dword v164, v180, s[12:15], s31 offen offset:1024\n"
    "v_exp_f32 v185, v185\n"
    "v_mfma_f32_16x16x32_bf16 v[28:31], v[248:251], a[120:123], v[28:31]\n"
    "ds_read_b64_tr_b16 v[192:193], v175 offset:0\n"
    "v_fma_f32 v185, v185, v170, v170\n"
    "v_mfma_f32_16x16x32_bf16 v[28:31], v[244:247], a[116:119], v[28:31]\n"
    "ds_read_b64_tr_b16 v[194:195], v175 offset:256\n"
    "v_mfma_f32_16x16x32_bf16 v[28:31], v[240:243], a[112:115], v[28:31]\n"
    "buffer_load_dwordx4 v177, s[56:59], s84 offen lds\n"
    "v_exp_f32 v186, v186\n"
    "v_mfma_f32_16x16x32_bf16 v[28:31], v[236:239], a[108:111], v[28:31]\n"
    "ds_read_b64_tr_b16 v[196:197], v175 offset:4096\n"
    "v_fma_f32 v186, v186, v170, v170\n"
    "v_mfma_f32_16x16x32_bf16 v[28:31], v[232:235], a[104:107], v[28:31]\n"
    "v_exp_f32 v184, v184\n"
    "v_mfma_f32_16x16x32_bf16 v[28:31], v[228:231], a[100:103], v[28:31]\n"
    "ds_read_b64_tr_b16 v[198:199], v175 offset:4352\n"
    "v_fma_f32 v184, v184, v170, v170\n"
    "v_mfma_f32_16x16x32_bf16 v[28:31], v[224:227], a[96:99], v[28:31]\n"
    "v_rcp_f32 v185, v185\n"
    "v_mfma_f32_16x16x32_bf16 v[44:47], v[224:227], a[192:195], v[44:47]\n"
    "ds_read_b64_tr_b16 v[200:201], v175 offset:8192\n"
    "v_mul_f32 v116, v116, v185\n"
    "v_mfma_f32_16x16x32_bf16 v[44:47], v[228:231], a[196:199], v[44:47]\n"
    "v_mul_f32 v185, v172, v119\n"
    "v_rcp_f32 v186, v186\n"
    "v_mfma_f32_16x16x32_bf16 v[44:47], v[232:235], a[200:203], v[44:47]\n"
    "ds_read_b64_tr_b16 v[202:203], v175 offset:8448\n"
    "v_mul_f32 v117, v117, v186\n"
    "v_mfma_f32_16x16x32_bf16 v[44:47], v[236:239], a[204:207], v[44:47]\n"
    "v_exp_f32 v185, v185\n"
    "v_mfma_f32_16x16x32_bf16 v[44:47], v[240:243], a[208:211], v[44:47]\n"
    "ds_read_b64_tr_b16 v[204:205], v175 offset:12288\n"
    "v_fma_f32 v185, v185, v170, v170\n"
    "v_mul_f32 v186, v171, v100\n"
    "v_mfma_f32_16x16x32_bf16 v[44:47], v[244:247], a[212:215], v[44:47]\n"
    "s_add_u32 m0, s23, 69632\n"
    "v_rcp_f32 v184, v184\n"
    "v_mfma_f32_16x16x32_bf16 v[44:47], v[248:251], a[216:219], v[44:47]\n"
    "s_add_u32 s84, s85, 2359424\n"
    "ds_read_b64_tr_b16 v[206:207], v175 offset:12544\n"
    "v_mul_f32 v118, v118, v184\n"
    "v_mfma_f32_16x16x32_bf16 v[44:47], v[252:255], a[220:223], v[44:47]\n"
    "buffer_load_dwordx4 v177, s[56:59], s84 offen lds\n"
    "v_rcp_f32 v185, v185\n"
    "v_mfma_f32_16x16x32_bf16 v[60:63], v[252:255], a[252:255], v[60:63]\n"
    "ds_read_b64_tr_b16 v[208:209], v175 offset:16384\n"
    "v_mul_f32 v119, v119, v185\n"
    "v_mfma_f32_16x16x32_bf16 v[60:63], v[248:251], a[248:251], v[60:63]\n"
    "ds_read_b64_tr_b16 v[210:211], v175 offset:16640\n"
    "v_mul_f32 v184, v171, v101\n"
    "v_mfma_f32_16x16x32_bf16 v[60:63], v[244:247], a[244:247], v[60:63]\n"
    "ds_read_b64_tr_b16 v[212:213], v175 offset:20480\n"
    "v_mul_f32 v185, v171, v102\n"
    "v_mul_f32 v52, v116, v52\n"
    "v_mfma_f32_16x16x32_bf16 v[60:63], v[240:243], a[240:243], v[60:63]\n"
    "ds_read_b64_tr_b16 v[214:215], v175 offset:20736\n"
    "v_mul_f32 v53, v117, v53\n"
    "v_mfma_f32_16x16x32_bf16 v[60:63], v[236:239], a[236:239], v[60:63]\n"
    "ds_read_b64_tr_b16 v[216:217], v175 offset:24576\n"
    "v_mul_f32 v54, v118, v54\n"
    "v_mfma_f32_16x16x32_bf16 v[60:63], v[232:235], a[232:235], v[60:63]\n"
    "ds_read_b64_tr_b16 v[218:219], v175 offset:24832\n"
    "v_exp_f32 v186, v186\n"
    "v_mfma_f32_16x16x32_bf16 v[60:63], v[228:231], a[228:231], v[60:63]\n"
    "ds_read_b64_tr_b16 v[220:221], v175 offset:28672\n"
    "ds_read_b64_tr_b16 v[222:223], v175 offset:28928\n"
    "v_mfma_f32_16x16x32_bf16 v[60:63], v[224:227], a[224:227], v[60:63]\n"
    "s_waitcnt lgkmcnt(6)\n"
    "v_exp_f32 v184, v184\n"
    "Lu4%=:\n"
    "v_mfma_f32_16x16x32_bf16 v[48:51], v[192:195], a[160:163], 0\n"
    "v_add_u32 v179, s17, v175\n"
    "v_mov_b32 v144, 0\n"
    "v_mov_b32 v145, 0\n"
    "v_mfma_f32_16x16x32_bf16 v[48:51], v[196:199], a[164:167], v[48:51]\n"
    "ds_read_b64_tr_b16 v[128:129], v179 offset:0\n"
    "v_mov_b32 v146, 0\n"
    "v_mfma_f32_16x16x32_bf16 v[48:51], v[200:203], a[168:171], v[48:51]\n"
    "ds_read_b64_tr_b16 v[130:131], v179 offset:256\n"
    "v_mov_b32 v147, 0\n"
    "v_mfma_f32_16x16x32_bf16 v[48:51], v[204:207], a[172:175], v[48:51]\n"
    "ds_read_b64_tr_b16 v[132:133], v179 offset:4096\n"
    "v_exp_f32 v185, v185\n"
    "v_mfma_f32_16x16x32_bf16 v[48:51], v[208:211], a[176:179], v[48:51]\n"
    "s_waitcnt lgkmcnt(3)\n"
    "s_add_u32 m0, s23, 73728\n"
    "ds_read_b64_tr_b16 v[134:135], v179 offset:4352\n"
    "v_fma_f32 v186, v186, v169, v169\n"
    "v_mfma_f32_16x16x32_bf16 v[48:51], v[212:215], a[180:183], v[48:51]\n"
    "s_add_u32 s84, s85, 2621568\n"
    "ds_read_b64_tr_b16 v[136:137], v179 offset:8192\n"
    "v_fma_f32 v184, v184, v169, v169\n"
    "v_mfma_f32_16x16x32_bf16 v[48:51], v[216:219], a[184:187], v[48:51]\n"
    "ds_read_b64_tr_b16 v[138:139], v179 offset:8448\n"
    "v_fma_f32 v185, v185, v169, v169\n"
    "v_mul_f32 v55, v119, v55\n"
    "v_mfma_f32_16x16x32_bf16 v[48:51], v[220:223], a[188:191], v[48:51]\n"
    "ds_read_b64_tr_b16 v[140:141], v179 offset:12288\n"
    "v_cvt_pk_bf16_f32 v114, v52, v53\n"
    "v_mfma_f32_16x16x32_bf16 v[32:35], v[220:223], a[156:159], 0\n"
    "ds_read_b64_tr_b16 v[142:143], v179 offset:12544\n"
    "v_cvt_pk_bf16_f32 v115, v54, v55\n"
    "v_mfma_f32_16x16x32_bf16 v[32:35], v[216:219], a[152:155], v[32:35]\n"
    "buffer_load_dwordx4 v177, s[56:59], s84 offen lds\n"
    "v_rcp_f32 v186, v186\n"
    "v_mfma_f32_16x16x32_bf16 v[32:35], v[212:215], a[148:151], v[32:35]\n"
    "ds_read_b64_tr_b16 v[224:225], v175 offset:32768\n"
    "v_mul_f32 v100, v100, v186\n"
    "v_mfma_f32_16x16x32_bf16 v[32:35], v[208:211], a[144:147], v[32:35]\n"
    "ds_read_b64_tr_b16 v[226:227], v175 offset:33024\n"
    "v_mul_f32 v186, v171, v103\n"
    "v_mfma_f32_16x16x32_bf16 v[32:35], v[204:207], a[140:143], v[32:35]\n"
    "ds_read_b64_tr_b16 v[228:229], v175 offset:36864\n"
    "v_exp_f32 v186, v186\n"
    "v_mfma_f32_16x16x32_bf16 v[32:35], v[200:203], a[136:139], v[32:35]\n"
    "ds_read_b64_tr_b16 v[230:231], v175 offset:37120\n"
    "v_fma_f32 v186, v186, v169, v169\n"
    "v_mfma_f32_16x16x32_bf16 v[32:35], v[196:199], a[132:135], v[32:35]\n"
    "ds_read_b64_tr_b16 v[232:233], v175 offset:40960\n"
    "v_mul_f32 v36, v100, v36\n"
    "v_mfma_f32_16x16x32_bf16 v[32:35], v[192:195], a[128:131], v[32:35]\n"
    "ds_read_b64_tr_b16 v[234:235], v175 offset:41216\n"
    "v_rcp_f32 v184, v184\n"
    "v_mfma_f32_16x16x32_bf16 v[16:19], v[192:195], a[32:35], 0\n"
    "ds_read_b64_tr_b16 v[236:237], v175 offset:45056\n"
    "v_mul_f32 v101, v101, v184\n"
    "v_mfma_f32_16x16x32_bf16 v[16:19], v[196:199], a[36:39], v[16:19]\n"
    "ds_read_b64_tr_b16 v[238:239], v175 offset:45312\n"
    "v_mul_f32 v184, v166, v84\n"
    "v_mfma_f32_16x16x32_bf16 v[16:19], v[200:203], a[40:43], v[16:19]\n"
    "ds_read_b64_tr_b16 v[240:241], v175 offset:49152\n"
    "v_rcp_f32 v185, v185\n"
    "v_mfma_f32_16x16x32_bf16 v[16:19], v[204:207], a[44:47], v[16:19]\n"
    "s_add_u32 m0, s23, 77824\n"
    "ds_read_b64_tr_b16 v[242:243], v175 offset:49408\n"
    "v_mul_f32 v102, v102, v185\n"
    "v_mfma_f32_16x16x32_bf16 v[16:19], v[208:211], a[48:51], v[16:19]\n"
    "s_add_u32 s84, s85, 2883712\n"
    "v_rcp_f32 v186, v186\n"
    "v_mfma_f32_16x16x32_bf16 v[16:19], v[212:215], a[52:55], v[16:19]\n"
    "ds_read_b64_tr_b16 v[244:245], v175 offset:53248\n"
    "v_mul_f32 v103, v103, v186\n"
    "v_mul_f32 v185, v166, v85\n"
    "v_mfma_f32_16x16x32_bf16 v[16:19], v[216:219], a[56:59], v[16:19]\n"
    "buffer_load_dwordx4 v177, s[56:59], s84 offen lds\n"
    "v_mul_f32 v186, v166, v86\n"
    "v_mfma_f32_16x16x32_bf16 v[16:19], v[220:223], a[60:63], v[16:19]\n"
    "ds_read_b64_tr_b16 v[246:247], v175 offset:53504\n"
    "v_mul_f32 v37, v101, v37\n"
    "v_mfma_f32_16x16x32_bf16 v[0:3], v[220:223], a[28:31], 0\n"
    "v_exp_f32 v184, v184\n"
    "v_mul_f32 v38, v102, v38\n"
    "v_mfma_f32_16x16x32_bf16 v[0:3], v[216:219], a[24:27], v[0:3]\n"
    "ds_read_b64_tr_b16 v[248:249], v175 offset:57344\n"
    "v_fma_f32 v184, v184, v168, v168\n"
    "v_mfma_f32_16x16x32_bf16 v[0:3], v[212:215], a[20:23], v[0:3]\n"
    "v_exp_f32 v185, v185\n"
    "v_mfma_f32_16x16x32_bf16 v[0:3], v[208:211], a[16:19], v[0:3]\n"
    "ds_read_b64_tr_b16 v[250:251], v175 offset:57600\n"
    "v_exp_f32 v186, v186\n"
    "v_mfma_f32_16x16x32_bf16 v[0:3], v[204:207], a[12:15], v[0:3]\n"
    "v_fma_f32 v185, v185, v168, v168\n"
    "v_fma_f32 v186, v186, v168, v168\n"
    "v_mfma_f32_16x16x32_bf16 v[0:3], v[200:203], a[8:11], v[0:3]\n"
    "ds_read_b64_tr_b16 v[252:253], v175 offset:61440\n"
    "v_mul_f32 v39, v103, v39\n"
    "v_mfma_f32_16x16x32_bf16 v[0:3], v[196:199], a[4:7], v[0:3]\n"
    "ds_read_b64_tr_b16 v[254:255], v175 offset:61696\n"
    "v_rcp_f32 v184, v184\n"
    "v_mfma_f32_16x16x32_bf16 v[0:3], v[192:195], a[0:3], v[0:3]\n"
    "s_waitcnt lgkmcnt(3)\n"
    "v_rcp_f32 v185, v185\n"
    "v_smfmac_f32_16x16x64_bf16 v[144:147], v[160:163], v[128:135], v183\n"
    "v_rcp_f32 v186, v186\n"
    "v_smfmac_f32_16x16x64_bf16 v[144:147], v[160:163], v[136:143], v183\n"
    "ds_read_b64_tr_b16 v[128:129], v179 offset:16384\n"
    "v_mul_f32 v84, v84, v184\n"
    "v_mul_f32 v85, v85, v185\n"
    "v_mfma_f32_16x16x32_bf16 v[0:3], v[224:227], a[64:67], v[0:3]\n"
    "s_add_u32 m0, s23, 81920\n"
    "ds_read_b64_tr_b16 v[130:131], v179 offset:16640\n"
    "v_mul_f32 v184, v166, v87\n"
    "v_mfma_f32_16x16x32_bf16 v[0:3], v[228:231], a[68:71], v[0:3]\n"
    "s_add_u32 s84, s85, 3145856\n"
    "ds_read_b64_tr_b16 v[132:133], v179 offset:20480\n"
    "v_mul_f32 v86, v86, v186\n"
    "v_mfma_f32_16x16x32_bf16 v[0:3], v[232:235], a[72:75], v[0:3]\n"
    "ds_read_b64_tr_b16 v[134:135], v179 offset:20736\n"
    "v_exp_f32 v184, v184\n"
    "v_mfma_f32_16x16x32_bf16 v[0:3], v[236:239], a[76:79], v[0:3]\n"
    "ds_read_b64_tr_b16 v[136:137], v179 offset:24576\n"
    "v_fma_f32 v184, v184, v168, v168\n"
    "v_mfma_f32_16x16x32_bf16 v[0:3], v[240:243], a[80:83], v[0:3]\n"
    "ds_read_b64_tr_b16 v[138:139], v179 offset:24832\n"
    "v_mul_f32 v185, v165, v68\n"
    "v_mfma_f32_16x16x32_bf16 v[0:3], v[244:247], a[84:87], v[0:3]\n"
    "s_waitcnt lgkmcnt(5)\n"
    "ds_read_b64_tr_b16 v[140:141], v179 offset:28672\n"
    "v_rcp_f32 v184, v184\n"
    "v_mfma_f32_16x16x32_bf16 v[0:3], v[248:251], a[88:91], v[0:3]\n"
    "ds_read_b64_tr_b16 v[142:143], v179 offset:28928\n"
    "v_mul_f32 v87, v87, v184\n"
    "v_mfma_f32_16x16x32_bf16 v[0:3], v[252:255], a[92:95], v[0:3]\n"
    "buffer_load_dwordx4 v177, s[56:59], s84 offen lds\n"
    "v_mul_f32 v186, v165, v69\n"
    "v_mfma_f32_16x16x32_bf16 v[16:19], v[252:255], a[124:127], v[16:19]\n"
    "ds_read_b64_tr_b16 v[192:193], v175 offset:8\n"
    "v_mul_f32 v184, v165, v70\n"
    "v_cvt_pk_bf16_f32 v98, v36, v37\n"
    "v_mfma_f32_16x16x32_bf16 v[16:19], v[248:251], a[120:123], v[16:19]\n"
    "ds_read_b64_tr_b16 v[194:195], v175 offset:264\n"
    "v_cvt_pk_bf16_f32 v99, v38, v39\n"
    "v_mfma_f32_16x16x32_bf16 v[16:19], v[244:247], a[116:119], v[16:19]\n"
    "ds_read_b64_tr_b16 v[196:197], v175 offset:4104\n"
    "v_mul_f32 v20, v84, v20\n"
    "v_mfma_f32_16x16x32_bf16 v[16:19], v[240:243], a[112:115], v[16:19]\n"
    "ds_read_b64_tr_b16 v[198:199], v175 offset:4360\n"
    "v_exp_f32 v185, v185\n"
    "v_mfma_f32_16x16x32_bf16 v[16:19], v[236:239], a[108:111], v[16:19]\n"
    "ds_read_b64_tr_b16 v[200:201], v175 offset:8200\n"
    "v_fma_f32 v185, v185, v167, v167\n"
    "v_mfma_f32_16x16x32_bf16 v[16:19], v[232:235], a[104:107], v[16:19]\n"
    "ds_read_b64_tr_b16 v[202:203], v175 offset:8456\n"
    "v_mul_f32 v21, v85, v21\n"
    "v_mfma_f32_16x16x32_bf16 v[16:19], v[228:231], a[100:103], v[16:19]\n"
    "s_add_u32 m0, s23, 86016\n"
    "ds_read_b64_tr_b16 v[204:205], v175 offset:12296\n"
    "v_exp_f32 v186, v186\n"
    "v_mfma_f32_16x16x32_bf16 v[16:19], v[224:227], a[96:99], v[16:19]\n"
    "s_waitcnt lgkmcnt(5)\n"
    "s_add_u32 s84, s85, 3408000\n"
    "ds_read_b64_tr_b16 v[206:207], v175 offset:12552\n"
    "v_fma_f32 v186, v186, v167, v167\n"
    "v_smfmac_f32_16x16x64_bf16 v[144:147], v[160:163], v[128:135], v183\n"
    "ds_read_b64_tr_b16 v[208:209], v175 offset:16392\n"
    "v_mul_f32 v22, v86, v22\n"
    "v_smfmac_f32_16x16x64_bf16 v[144:147], v[160:163], v[136:143], v183\n"
    "ds_read_b64_tr_b16 v[128:129], v179 offset:32768\n"
    "v_exp_f32 v184, v184\n"
    "v_mfma_f32_16x16x32_bf16 v[32:35], v[224:227], a[192:195], v[32:35]\n"
    "ds_read_b64_tr_b16 v[130:131], v179 offset:33024\n"
    "v_fma_f32 v184, v184, v167, v167\n"
    "v_mfma_f32_16x16x32_bf16 v[32:35], v[228:231], a[196:199], v[32:35]\n"
    "ds_read_b64_tr_b16 v[132:133], v179 offset:36864\n"
    "v_mul_f32 v23, v87, v23\n"
    "v_mfma_f32_16x16x32_bf16 v[32:35], v[232:235], a[200:203], v[32:35]\n"
    "ds_read_b64_tr_b16 v[134:135], v179 offset:37120\n"
    "v_rcp_f32 v185, v185\n"
    "v_mfma_f32_16x16x32_bf16 v[32:35], v[236:239], a[204:207], v[32:35]\n"
    "ds_read_b64_tr_b16 v[136:137], v179 offset:40960\n"
    "v_mul_f32 v68, v68, v185\n"
    "v_mfma_f32_16x16x32_bf16 v[32:35], v[240:243], a[208:211], v[32:35]\n"
    "ds_read_b64_tr_b16 v[138:139], v179 offset:41216\n"
    "v_mul_f32 v185, v165, v71\n"
    "v_mfma_f32_16x16x32_bf16 v[32:35], v[244:247], a[212:215], v[32:35]\n"
    "ds_read_b64_tr_b16 v[140:141], v179 offset:45056\n"
    "v_exp_f32 v185, v185\n"
    "v_mfma_f32_16x16x32_bf16 v[32:35], v[248:251], a[216:219], v[32:35]\n"
    "ds_read_b64_tr_b16 v[142:143], v179 offset:45312\n"
    "v_fma_f32 v185, v185, v167, v167\n"
    "v_mfma_f32_16x16x32_bf16 v[32:35], v[252:255], a[220:223], v[32:35]\n"
    "ds_read_b64_tr_b16 v[210:211], v175 offset:16648\n"
    "v_cvt_pk_bf16_f32 v82, v20, v21\n"
    "v_mfma_f32_16x16x32_bf16 v[48:51], v[252:255], a[252:255], v[48:51]\n"
    "ds_read_b64_tr_b16 v[212:213], v175 offset:20488\n"
    "v_rcp_f32 v186, v186\n"
    "v_mfma_f32_16x16x32_bf16 v[48:51], v[248:251], a[248:251], v[48:51]\n"
    "ds_read_b64_tr_b16 v[214:215], v175 offset:20744\n"
    "v_mul_f32 v69, v69, v186\n"
    "v_mfma_f32_16x16x32_bf16 v[48:51], v[244:247], a[244:247], v[48:51]\n"
    "ds_read_b64_tr_b16 v[216:217], v175 offset:24584\n"
    "v_cvt_pk_bf16_f32 v83, v22, v23\n"
    "v_mfma_f32_16x16x32_bf16 v[48:51], v[240:243], a[240:243], v[48:51]\n"
    "ds_read_b64_tr_b16 v[218:219], v175 offset:24840\n"
    "v_rcp_f32 v184, v184\n"
    "v_mfma_f32_16x16x32_bf16 v[48:51], v[236:239], a[236:239], v[48:51]\n"
    "ds_read_b64_tr_b16 v[220:221], v175 offset:28680\n"
    "v_mul_f32 v70, v70, v184\n"
    "v_mfma_f32_16x16x32_bf16 v[48:51], v[232:235], a[232:235], v[48:51]\n"
    "ds_read_b64_tr_b16 v[222:223], v175 offset:28936\n"
    "v_mul_f32 v4, v68, v4\n"
    "v_mfma_f32_16x16x32_bf16 v[48:51], v[228:231], a[228:231], v[48:51]\n"
    "buffer_load_dwordx4 v177, s[56:59], s84 offen lds\n"
    "v_rcp_f32 v185, v185\n"
    "v_mfma_f32_16x16x32_bf16 v[48:51], v[224:227], a[224:227], v[48:51]\n"
    "s_waitcnt lgkmcnt(4)\n"
    "s_add_u32 m0, s23, 90112\n"
    "v_mul_f32 v71, v71, v185\n"
    "v_mul_f32 v5, v69, v5\n"
    "v_smfmac_f32_16x16x64_bf16 v[144:147], v[160:163], v[128:135], v183\n"
    "s_add_u32 s84, s85, 3670144\n"
    "v_mul_f32 v6, v70, v6\n"
    "v_mul_f32 v7, v71, v7\n"
    "v_smfmac_f32_16x16x64_bf16 v[144:147], v[160:163], v[136:143], v183\n"
    "ds_read_b64_tr_b16 v[128:129], v179 offset:49152\n"
    "v_cvt_pk_bf16_f32 v66, v4, v5\n"
    "v_cvt_pk_bf16_f32 v67, v6, v7\n"
    "v_mfma_f32_16x16x32_bf16 v[52:55], v[192:195], a[160:163], 0\n"
    "ds_read_b64_tr_b16 v[130:131], v179 offset:49408\n"
    "v_mul_f32 v186, v172, v120\n"
    "v_mfma_f32_16x16x32_bf16 v[52:55], v[196:199], a[164:167], v[52:55]\n"
    "ds_read_b64_tr_b16 v[132:133], v179 offset:53248\n"
    "v_mul_f32 v184, v172, v121\n"
    "v_mfma_f32_16x16x32_bf16 v[52:55], v[200:203], a[168:171], v[52:55]\n"
    "ds_read_b64_tr_b16 v[134:135], v179 offset:53504\n"
    "v_mul_f32 v185, v172, v122\n"
    "v_mfma_f32_16x16x32_bf16 v[52:55], v[204:207], a[172:175], v[52:55]\n"
    "ds_read_b64_tr_b16 v[136:137], v179 offset:57344\n"
    "v_mfma_f32_16x16x32_bf16 v[52:55], v[208:211], a[176:179], v[52:55]\n"
    "ds_read_b64_tr_b16 v[138:139], v179 offset:57600\n"
    "v_mfma_f32_16x16x32_bf16 v[52:55], v[212:215], a[180:183], v[52:55]\n"
    "s_waitcnt lgkmcnt(5)\n"
    "ds_read_b64_tr_b16 v[140:141], v179 offset:61440\n"
    "v_exp_f32 v186, v186\n"
    "v_mfma_f32_16x16x32_bf16 v[52:55], v[216:219], a[184:187], v[52:55]\n"
    "ds_read_b64_tr_b16 v[142:143], v179 offset:61696\n"
    "v_fma_f32 v186, v186, v170, v170\n"
    "v_mfma_f32_16x16x32_bf16 v[52:55], v[220:223], a[188:191], v[52:55]\n"
    "buffer_load_dwordx4 v177, s[56:59], s84 offen lds\n"
    "v_add_u32 v179, 0x10000, v179\n"
    "v_mfma_f32_16x16x32_bf16 v[36:39], v[220:223], a[156:159], 0\n"
    "s_add_u32 m0, s23, 94208\n"
    "buffer_store_dwordx4 v[112:115], v180, s[72:75], s89 offen offset:0 sc1\n"
    "v_exp_f32 v184, v184\n"
    "v_mfma_f32_16x16x32_bf16 v[36:39], v[216:219], a[152:155], v[36:39]\n"
    "s_add_u32 s84, s85, 3932288\n"
    "ds_read_b64_tr_b16 v[224:225], v175 offset:32776\n"
    "v_fma_f32 v184, v184, v170, v170\n"
    "v_mfma_f32_16x16x32_bf16 v[36:39], v[212:215], a[148:151], v[36:39]\n"
    "ds_read_b64_tr_b16 v[226:227], v175 offset:33032\n"
    "v_mfma_f32_16x16x32_bf16 v[36:39], v[208:211], a[144:147], v[36:39]\n"
    "ds_read_b64_tr_b16 v[228:229], v175 offset:36872\n"
    "v_exp_f32 v185, v185\n"
    "v_mfma_f32_16x16x32_bf16 v[36:39], v[204:207], a[140:143], v[36:39]\n"
    "ds_read_b64_tr_b16 v[230:231], v175 offset:37128\n"
    "v_fma_f32 v185, v185, v170, v170\n"
    "v_mfma_f32_16x16x32_bf16 v[36:39], v[200:203], a[136:139], v[36:39]\n"
    "ds_read_b64_tr_b16 v[232:233], v175 offset:40968\n"
    "v_mfma_f32_16x16x32_bf16 v[36:39], v[196:199], a[132:135], v[36:39]\n"
    "ds_read_b64_tr_b16 v[234:235], v175 offset:41224\n"
    "v_rcp_f32 v186, v186\n"
    "v_mfma_f32_16x16x32_bf16 v[36:39], v[192:195], a[128:131], v[36:39]\n"
    "s_waitcnt lgkmcnt(5)\n"
    "ds_read_b64_tr_b16 v[236:237], v175 offset:45064\n"
    "v_mul_f32 v120, v120, v186\n"
    "v_smfmac_f32_16x16x64_bf16 v[144:147], v[160:163], v[128:135], v183\n"
    "ds_read_b64_tr_b16 v[238:239], v175 offset:45320\n"
    "v_mul_f32 v186, v172, v123\n"
    "v_smfmac_f32_16x16x64_bf16 v[144:147], v[160:163], v[136:143], v183\n"
    "buffer_load_dwordx4 v177, s[56:59], s84 offen lds\n"
    "v_exp_f32 v186, v186\n"
    "v_mfma_f32_16x16x32_bf16 v[20:23], v[192:195], a[32:35], 0\n"
    "buffer_store_dwordx4 v[96:99], v180, s[72:75], s88 offen offset:0 sc1\n"
    "v_fma_f32 v186, v186, v170, v170\n"
    "v_mfma_f32_16x16x32_bf16 v[20:23], v[196:199], a[36:39], v[20:23]\n"
    "ds_read_b64_tr_b16 v[240:241], v175 offset:49160\n"
    "v_mul_f32 v56, v120, v56\n"
    "v_mfma_f32_16x16x32_bf16 v[20:23], v[200:203], a[40:43], v[20:23]\n"
    "ds_read_b64_tr_b16 v[242:243], v175 offset:49416\n"
    "v_rcp_f32 v184, v184\n"
    "v_mfma_f32_16x16x32_bf16 v[20:23], v[204:207], a[44:47], v[20:23]\n"
    "ds_read_b64_tr_b16 v[244:245], v175 offset:53256\n"
    "v_mul_f32 v121, v121, v184\n"
    "v_mfma_f32_16x16x32_bf16 v[20:23], v[208:211], a[48:51], v[20:23]\n"
    "ds_read_b64_tr_b16 v[246:247], v175 offset:53512\n"
    "v_mul_f32 v184, v171, v104\n"
    "v_mfma_f32_16x16x32_bf16 v[20:23], v[212:215], a[52:55], v[20:23]\n"
    "ds_read_b64_tr_b16 v[248:249], v175 offset:57352\n"
    "v_add_f32 v144, v144, v145\n"
    "v_lshl_add_u32 v146, v181, 2, s93\n"
    "v_mfma_f32_16x16x32_bf16 v[20:23], v[216:219], a[56:59], v[20:23]\n"
    "s_nop 0\n"
    "s_mov_b64 exec, 0xffff\n"
    "ds_write_b32 v146, v144 offset:256\n"
    "s_mov_b64 exec, -1\n"
    "buffer_store_dwordx4 v[80:83], v180, s[72:75], s87 offen offset:0 sc1\n"
    "v_mul_f32 v57, v121, v57\n"
    "v_mfma_f32_16x16x32_bf16 v[20:23], v[220:223], a[60:63], v[20:23]\n"
    "ds_read_b64_tr_b16 v[250:251], v175 offset:57608\n"
    "v_mfma_f32_16x16x32_bf16 v[4:7], v[220:223], a[28:31], 0\n"
    "buffer_store_dwordx4 v[64:67], v180, s[72:75], s86 offen offset:0 sc1\n"
    "v_rcp_f32 v185, v185\n"
    "v_mfma_f32_16x16x32_bf16 v[4:7], v[216:219], a[24:27], v[4:7]\n"
    "ds_read_b64_tr_b16 v[252:253], v175 offset:61448\n"
    "v_mul_f32 v122, v122, v185\n"
    "v_mfma_f32_16x16x32_bf16 v[4:7], v[212:215], a[20:23], v[4:7]\n"
    "v_rcp_f32 v186, v186\n"
    "v_mfma_f32_16x16x32_bf16 v[4:7], v[208:211], a[16:19], v[4:7]\n"
    "ds_read_b64_tr_b16 v[254:255], v175 offset:61704\n"
    "v_mul_f32 v123, v123, v186\n"
    "v_mul_f32 v185, v171, v105\n"
    "v_mfma_f32_16x16x32_bf16 v[4:7], v[204:207], a[12:15], v[4:7]\n"
    "v_mul_f32 v186, v171, v106\n"
    "v_mul_f32 v58, v122, v58\n"
    "v_mfma_f32_16x16x32_bf16 v[4:7], v[200:203], a[8:11], v[4:7]\n"
    "v_exp_f32 v184, v184\n"
    "v_mfma_f32_16x16x32_bf16 v[4:7], v[196:199], a[4:7], v[4:7]\n"
    "v_exp_f32 v185, v185\n"
    "v_fma_f32 v184, v184, v169, v169\n"
    "v_mfma_f32_16x16x32_bf16 v[4:7], v[192:195], a[0:3], v[4:7]\n"
    "s_waitcnt lgkmcnt(1)\n"
    "v_exp_f32 v186, v186\n"
    "v_mfma_f32_16x16x32_bf16 v[4:7], v[224:227], a[64:67], v[4:7]\n"
    "v_fma_f32 v185, v185, v169, v169\n"
    "v_fma_f32 v186, v186, v169, v169\n"
    "v_mfma_f32_16x16x32_bf16 v[4:7], v[228:231], a[68:71], v[4:7]\n"
    "v_rcp_f32 v184, v184\n"
    "v_mul_f32 v59, v123, v59\n"
    "v_mfma_f32_16x16x32_bf16 v[4:7], v[232:235], a[72:75], v[4:7]\n"
    "v_rcp_f32 v185, v185\n"
    "v_mfma_f32_16x16x32_bf16 v[4:7], v[236:239], a[76:79], v[4:7]\n"
    "v_rcp_f32 v186, v186\n"
    "v_mfma_f32_16x16x32_bf16 v[4:7], v[240:243], a[80:83], v[4:7]\n"
    "v_mul_f32 v104, v104, v184\n"
    "v_mul_f32 v105, v105, v185\n"
    "v_mul_f32 v106, v106, v186\n"
    "v_mfma_f32_16x16x32_bf16 v[4:7], v[244:247], a[84:87], v[4:7]\n"
    "s_waitcnt lgkmcnt(0)\n"
    "v_mul_f32 v184, v171, v107\n"
    "v_mul_f32 v185, v166, v88\n"
    "v_mfma_f32_16x16x32_bf16 v[4:7], v[248:251], a[88:91], v[4:7]\n"
    "s_waitcnt lgkmcnt(0)\n"
    "s_barrier\n"
    "v_exp_f32 v184, v184\n"
    "v_mfma_f32_16x16x32_bf16 v[4:7], v[252:255], a[92:95], v[4:7]\n"
    "v_fma_f32 v184, v184, v169, v169\n"
    "v_mul_f32 v186, v166, v89\n"
    "v_cvt_pk_bf16_f32 v120, v56, v57\n"
    "v_mfma_f32_16x16x32_bf16 v[20:23], v[252:255], a[124:127], v[20:23]\n"
    "v_rcp_f32 v184, v184\n"
    "v_mfma_f32_16x16x32_bf16 v[20:23], v[248:251], a[120:123], v[20:23]\n"
    "ds_read_b128 v[152:155], v178 offset:256\n"
    "v_mul_f32 v107, v107, v184\n"
    "v_mfma_f32_16x16x32_bf16 v[20:23], v[244:247], a[116:119], v[20:23]\n"
    "ds_read_b128 v[156:159], v178 offset:320\n"
    "v_mul_f32 v184, v166, v90\n"
    "v_cvt_pk_bf16_f32 v121, v58, v59\n"
    "v_mfma_f32_16x16x32_bf16 v[20:23], v[240:243], a[112:115], v[20:23]\n"
    "v_exp_f32 v185, v185\n"
    "v_mfma_f32_16x16x32_bf16 v[20:23], v[236:239], a[108:111], v[20:23]\n"
    "v_exp_f32 v186, v186\n"
    "v_mfma_f32_16x16x32_bf16 v[20:23], v[232:235], a[104:107], v[20:23]\n"
    "v_exp_f32 v184, v184\n"
    "v_fma_f32 v185, v185, v168, v168\n"
    "v_mfma_f32_16x16x32_bf16 v[20:23], v[228:231], a[100:103], v[20:23]\n"
    "v_fma_f32 v186, v186, v168, v168\n"
    "v_fma_f32 v184, v184, v168, v168\n"
    "v_mfma_f32_16x16x32_bf16 v[20:23], v[224:227], a[96:99], v[20:23]\n"
    "ds_read_b64_tr_b16 v[192:193], v175 offset:512\n"
    "v_mul_f32 v40, v104, v40\n"
    "v_mfma_f32_16x16x32_bf16 v[36:39], v[224:227], a[192:195], v[36:39]\n"
    "ds_read_b64_tr_b16 v[194:195], v175 offset:768\n"
    "v_rcp_f32 v185, v185\n"
    "v_mfma_f32_16x16x32_bf16 v[36:39], v[228:231], a[196:199], v[36:39]\n"
    "v_rcp_f32 v186, v186\n"
    "v_mfma_f32_16x16x32_bf16 v[36:39], v[232:235], a[200:203], v[36:39]\n"
    "v_rcp_f32 v184, v184\n"
    "v_mfma_f32_16x16x32_bf16 v[36:39], v[236:239], a[204:207], v[36:39]\n"
    "ds_read_b64_tr_b16 v[196:197], v175 offset:4608\n"
    "v_mul_f32 v88, v88, v185\n"
    "v_mul_f32 v89, v89, v186\n"
    "v_mfma_f32_16x16x32_bf16 v[36:39], v[240:243], a[208:211], v[36:39]\n"
    "ds_read_b64_tr_b16 v[198:199], v175 offset:4864\n"
    "v_mul_f32 v185, v166, v91\n"
    "v_mfma_f32_16x16x32_bf16 v[36:39], v[244:247], a[212:215], v[36:39]\n"
    "v_exp_f32 v185, v185\n"
    "v_mfma_f32_16x16x32_bf16 v[36:39], v[248:251], a[216:219], v[36:39]\n"
    "ds_read_b64_tr_b16 v[200:201], v175 offset:8704\n"
    "v_fma_f32 v185, v185, v168, v168\n"
    "v_mul_f32 v90, v90, v184\n"
    "v_mfma_f32_16x16x32_bf16 v[36:39], v[252:255], a[220:223], v[36:39]\n"
    "v_rcp_f32 v185, v185\n"
    "v_mfma_f32_16x16x32_bf16 v[52:55], v[252:255], a[252:255], v[52:55]\n"
    "ds_read_b64_tr_b16 v[202:203], v175 offset:8960\n"
    "v_mul_f32 v91, v91, v185\n"
    "v_mfma_f32_16x16x32_bf16 v[52:55], v[248:251], a[248:251], v[52:55]\n"
    "v_mul_f32 v186, v165, v72\n"
    "v_mul_f32 v184, v165, v73\n"
    "v_mul_f32 v185, v165, v74\n"
    "v_mfma_f32_16x16x32_bf16 v[52:55], v[244:247], a[244:247], v[52:55]\n"
    "ds_read_b64_tr_b16 v[204:205], v175 offset:12800\n"
    "ds_read_b64_tr_b16 v[206:207], v175 offset:13056\n"
    "v_mul_f32 v41, v105, v41\n"
    "v_mul_f32 v42, v106, v42\n"
    "v_mul_f32 v43, v107, v43\n"
    "v_mfma_f32_16x16x32_bf16 v[52:55], v[240:243], a[240:243], v[52:55]\n"
    "ds_read_b64_tr_b16 v[208:209], v175 offset:16896\n"
    "ds_read_b64_tr_b16 v[210:211], v175 offset:17152\n"
    "v_exp_f32 v186, v186\n"
    "v_mfma_f32_16x16x32_bf16 v[52:55], v[236:239], a[236:239], v[52:55]\n"
    "ds_read_b64_tr_b16 v[212:213], v175 offset:20992\n"
    "v_exp_f32 v184, v184\n"
    "v_exp_f32 v185, v185\n"
    "v_mfma_f32_16x16x32_bf16 v[52:55], v[232:235], a[232:235], v[52:55]\n"
    "ds_read_b64_tr_b16 v[214:215], v175 offset:21248\n"
    "ds_read_b64_tr_b16 v[216:217], v175 offset:25088\n"
    "v_fma_f32 v186, v186, v167, v167\n"
    "v_fma_f32 v184, v184, v167, v167\n"
    "v_fma_f32 v185, v185, v167, v167\n"
    "v_mfma_f32_16x16x32_bf16 v[52:55], v[228:231], a[228:231], v[52:55]\n"
    "ds_read_b64_tr_b16 v[218:219], v175 offset:25344\n"
    "ds_read_b64_tr_b16 v[220:221], v175 offset:29184\n"
    "v_cvt_pk_bf16_f32 v104, v40, v41\n"
    "v_cvt_pk_bf16_f32 v105, v42, v43\n"
    "v_mfma_f32_16x16x32_bf16 v[52:55], v[224:227], a[224:227], v[52:55]\n"
    "s_waitcnt lgkmcnt(9)\n"
    "ds_read_b64_tr_b16 v[222:223], v175 offset:29440\n"
    "v_rcp_f32 v186, v186\n"
    "v_rcp_f32 v184, v184\n"
    "v_mfma_f32_16x16x32_bf16 v[56:59], v[192:195], a[160:163], 0\n"
    "s_mov_b32 s18, 32768\n"
    "v_rcp_f32 v185, v185\n"
    "v_mfma_f32_16x16x32_bf16 v[56:59], v[196:199], a[164:167], v[56:59]\n"
    "s_mov_b32 s19, 32768\n"
    "v_mul_f32 v72, v72, v186\n"
    "v_mul_f32 v73, v73, v184\n"
    "v_mul_f32 v74, v74, v185\n"
    "v_mfma_f32_16x16x32_bf16 v[56:59], v[200:203], a[168:171], v[56:59]\n"
    "s_waitcnt lgkmcnt(5)\n"
    "s_mov_b32 s20, 30\n"
    "v_mul_f32 v186, v165, v75\n"
    "v_mul_f32 v24, v88, v24\n"
    "v_mul_f32 v25, v89, v25\n"
    "v_mul_f32 v26, v90, v26\n"
    "v_mul_f32 v27, v91, v27\n"
    "v_mfma_f32_16x16x32_bf16 v[56:59], v[204:207], a[172:175], v[56:59]\n"
    "s_add_u32 s17, s22, 98304\n"
    "v_exp_f32 v186, v186\n"
    "v_mfma_f32_16x16x32_bf16 v[56:59], v[208:211], a[176:179], v[56:59]\n"
    "s_waitcnt lgkmcnt(1)\n"
    "s_and_b32 s30, s26, 3\n"
    "v_fma_f32 v186, v186, v167, v167\n"
    "v_cvt_pk_bf16_f32 v88, v24, v25\n"
    "v_mfma_f32_16x16x32_bf16 v[56:59], v[212:215], a[180:183], v[56:59]\n"
    "s_lshl_b32 s30, s30, 8\n"
    "v_rcp_f32 v186, v186\n"
    "v_cvt_pk_bf16_f32 v89, v26, v27\n"
    "v_mfma_f32_16x16x32_bf16 v[56:59], v[216:219], a[184:187], v[56:59]\n"
    "s_waitcnt lgkmcnt(0)\n"
    "s_bfe_u32 s31, s26, 0x10003\n"
    "v_mul_f32 v75, v75, v186\n"
    "v_mul_f32 v8, v72, v8\n"
    "v_mfma_f32_16x16x32_bf16 v[56:59], v[220:223], a[188:191], v[56:59]\n"
    "s_lshl_b32 s31, s31, 7\n"
    "v_mul_f32 v9, v73, v9\n"
    "v_mul_f32 v10, v74, v10\n"
    "v_mfma_f32_16x16x32_bf16 v[40:43], v[220:223], a[156:159], 0\n"
    "s_add_u32 s31, s31, s30\n"
    "ds_read_b64_tr_b16 v[224:225], v175 offset:33280\n"
    "v_mul_f32 v11, v75, v11\n"
    "v_mul_f32 v184, v172, v124\n"
    "v_mfma_f32_16x16x32_bf16 v[40:43], v[216:219], a[152:155], v[40:43]\n"
    "s_add_u32 s64, s44, s30\n"
    "s_addc_u32 s65, s45, 0\n"
    "v_cvt_pk_bf16_f32 v72, v8, v9\n"
    "v_cvt_pk_bf16_f32 v73, v10, v11\n"
    "v_mfma_f32_16x16x32_bf16 v[40:43], v[212:215], a[148:151], v[40:43]\n"
    "s_sub_u32 s64, s64, 1024\n"
    "s_subb_u32 s65, s65, 0\n"
    "ds_read_b64_tr_b16 v[226:227], v175 offset:33536\n"
    "v_mul_f32 v185, v172, v125\n"
    "v_mfma_f32_16x16x32_bf16 v[40:43], v[208:211], a[144:147], v[40:43]\n"
    "s_sub_u32 s66, 4195328, s30\n"
    "ds_read_b64_tr_b16 v[228:229], v175 offset:37376\n"
    "v_mul_f32 v186, v172, v126\n"
    "v_fmac_f32 v0, v188, v152\n"
    "v_mfma_f32_16x16x32_bf16 v[40:43], v[204:207], a[140:143], v[40:43]\n"
    "s_add_u32 s60, s50, s31\n"
    "s_addc_u32 s61, s51, 0\n"
    "ds_read_b64_tr_b16 v[230:231], v175 offset:37632\n"
    "v_fmac_f32 v1, v188, v153\n"
    "v_mfma_f32_16x16x32_bf16 v[40:43], v[200:203], a[136:139], v[40:43]\n"
    "s_sub_u32 s62, 1835008, s31\n"
    "ds_read_b64_tr_b16 v[232:233], v175 offset:41472\n"
    "v_fmac_f32 v2, v188, v154\n"
    "v_mfma_f32_16x16x32_bf16 v[40:43], v[196:199], a[132:135], v[40:43]\n"
    "ds_read_b64_tr_b16 v[234:235], v175 offset:41728\n"
    "v_exp_f32 v184, v184\n"
    "v_mfma_f32_16x16x32_bf16 v[40:43], v[192:195], a[128:131], v[40:43]\n"
    "ds_read_b64_tr_b16 v[236:237], v175 offset:45568\n"
    "v_fma_f32 v184, v184, v170, v170\n"
    "v_mfma_f32_16x16x32_bf16 v[24:27], v[192:195], a[32:35], 0\n"
    "ds_read_b64_tr_b16 v[238:239], v175 offset:45824\n"
    "v_fmac_f32 v3, v188, v155\n"
    "v_mfma_f32_16x16x32_bf16 v[24:27], v[196:199], a[36:39], v[24:27]\n"
    "ds_read_b64_tr_b16 v[240:241], v175 offset:49664\n"
    "v_exp_f32 v185, v185\n"
    "v_mfma_f32_16x16x32_bf16 v[24:27], v[200:203], a[40:43], v[24:27]\n"
    "v_exp_f32 v186, v186\n"
    "v_mfma_f32_16x16x32_bf16 v[24:27], v[204:207], a[44:47], v[24:27]\n"
    "ds_read_b64_tr_b16 v[242:243], v175 offset:49920\n"
    "v_fma_f32 v185, v185, v170, v170\n"
    "v_mfma_f32_16x16x32_bf16 v[24:27], v[208:211], a[48:51], v[24:27]\n"
    "v_fma_f32 v186, v186, v170, v170\n"
    "v_rcp_f32 v184, v184\n"
    "v_mfma_f32_16x16x32_bf16 v[24:27], v[212:215], a[52:55], v[24:27]\n"
    "ds_read_b64_tr_b16 v[244:245], v175 offset:53760\n"
    "v_mul_f32 v124, v124, v184\n"
    "v_mfma_f32_16x16x32_bf16 v[24:27], v[216:219], a[56:59], v[24:27]\n"
    "v_mul_f32 v184, v172, v127\n"
    "v_mul_f32 v60, v124, v60\n"
    "v_mfma_f32_16x16x32_bf16 v[24:27], v[220:223], a[60:63], v[24:27]\n"
    "ds_read_b64_tr_b16 v[246:247], v175 offset:54016\n"
    "v_exp_f32 v184, v184\n"
    "v_mfma_f32_16x16x32_bf16 v[8:11], v[220:223], a[28:31], 0\n"
    "v_fma_f32 v184, v184, v170, v170\n"
    "v_fmac_f32 v16, v189, v152\n"
    "v_mfma_f32_16x16x32_bf16 v[8:11], v[216:219], a[24:27], v[8:11]\n"
    "ds_read_b64_tr_b16 v[248:249], v175 offset:57856\n"
    "v_fmac_f32 v17, v189, v153\n"
    "v_mfma_f32_16x16x32_bf16 v[8:11], v[212:215], a[20:23], v[8:11]\n"
    "v_rcp_f32 v185, v185\n"
    "v_fmac_f32 v18, v189, v154\n"
    "v_mfma_f32_16x16x32_bf16 v[8:11], v[208:211], a[16:19], v[8:11]\n"
    "ds_read_b64_tr_b16 v[250:251], v175 offset:58112\n"
    "v_mul_f32 v125, v125, v185\n"
    "v_mfma_f32_16x16x32_bf16 v[8:11], v[204:207], a[12:15], v[8:11]\n"
    "ds_read_b64_tr_b16 v[252:253], v175 offset:61952\n"
    "v_mul_f32 v185, v171, v108\n"
    "v_mfma_f32_16x16x32_bf16 v[8:11], v[200:203], a[8:11], v[8:11]\n"
    "ds_read_b64_tr_b16 v[254:255], v175 offset:62208\n"
    "v_rcp_f32 v186, v186\n"
    "v_mfma_f32_16x16x32_bf16 v[8:11], v[196:199], a[4:7], v[8:11]\n"
    "v_rcp_f32 v184, v184\n"
    "v_mfma_f32_16x16x32_bf16 v[8:11], v[192:195], a[0:3], v[8:11]\n"
    "s_waitcnt lgkmcnt(3)\n"
    "v_mul_f32 v126, v126, v186\n"
    "v_mul_f32 v127, v127, v184\n"
    "v_mfma_f32_16x16x32_bf16 v[8:11], v[224:227], a[64:67], v[8:11]\n"
    "v_mul_f32 v186, v171, v109\n"
    "v_mul_f32 v184, v171, v110\n"
    "v_mul_f32 v61, v125, v61\n"
    "v_mfma_f32_16x16x32_bf16 v[8:11], v[228:231], a[68:71], v[8:11]\n"
    "v_exp_f32 v185, v185\n"
    "v_mfma_f32_16x16x32_bf16 v[8:11], v[232:235], a[72:75], v[8:11]\n"
    "v_exp_f32 v186, v186\n"
    "v_mfma_f32_16x16x32_bf16 v[8:11], v[236:239], a[76:79], v[8:11]\n"
    "v_exp_f32 v184, v184\n"
    "v_fma_f32 v185, v185, v169, v169\n"
    "v_mfma_f32_16x16x32_bf16 v[8:11], v[240:243], a[80:83], v[8:11]\n"
    "v_fma_f32 v186, v186, v169, v169\n"
    "v_fma_f32 v184, v184, v169, v169\n"
    "v_mfma_f32_16x16x32_bf16 v[8:11], v[244:247], a[84:87], v[8:11]\n"
    "s_waitcnt lgkmcnt(0)\n"
    "v_rcp_f32 v185, v185\n"
    "v_mfma_f32_16x16x32_bf16 v[8:11], v[248:251], a[88:91], v[8:11]\n"
    "v_rcp_f32 v186, v186\n"
    "v_mul_f32 v108, v108, v185\n"
    "v_mfma_f32_16x16x32_bf16 v[8:11], v[252:255], a[92:95], v[8:11]\n"
    "v_mul_f32 v185, v171, v111\n"
    "v_mul_f32 v109, v109, v186\n"
    "v_mfma_f32_16x16x32_bf16 v[24:27], v[252:255], a[124:127], v[24:27]\n"
    "v_exp_f32 v185, v185\n"
    "v_mfma_f32_16x16x32_bf16 v[24:27], v[248:251], a[120:123], v[24:27]\n"
    "v_fma_f32 v185, v185, v169, v169\n"
    "v_rcp_f32 v184, v184\n"
    "v_mfma_f32_16x16x32_bf16 v[24:27], v[244:247], a[116:119], v[24:27]\n"
    "v_rcp_f32 v185, v185\n"
    "v_mfma_f32_16x16x32_bf16 v[24:27], v[240:243], a[112:115], v[24:27]\n"
    "v_mul_f32 v110, v110, v184\n"
    "v_mul_f32 v111, v111, v185\n"
    "v_mfma_f32_16x16x32_bf16 v[24:27], v[236:239], a[108:111], v[24:27]\n"
    "v_mul_f32 v186, v166, v92\n"
    "v_mul_f32 v184, v166, v93\n"
    "v_mul_f32 v185, v166, v94\n"
    "v_mfma_f32_16x16x32_bf16 v[24:27], v[232:235], a[104:107], v[24:27]\n"
    "v_mul_f32 v62, v126, v62\n"
    "v_mul_f32 v63, v127, v63\n"
    "v_mfma_f32_16x16x32_bf16 v[24:27], v[228:231], a[100:103], v[24:27]\n"
    "v_exp_f32 v186, v186\n"
    "v_mfma_f32_16x16x32_bf16 v[24:27], v[224:227], a[96:99], v[24:27]\n"
    "v_exp_f32 v184, v184\n"
    "v_fma_f32 v186, v186, v168, v168\n"
    "v_mfma_f32_16x16x32_bf16 v[40:43], v[224:227], a[192:195], v[40:43]\n"
    "v_exp_f32 v185, v185\n"
    "v_mfma_f32_16x16x32_bf16 v[40:43], v[228:231], a[196:199], v[40:43]\n"
    "v_fma_f32 v184, v184, v168, v168\n"
    "v_fma_f32 v185, v185, v168, v168\n"
    "v_mfma_f32_16x16x32_bf16 v[40:43], v[232:235], a[200:203], v[40:43]\n"
    "ds_read_b64_tr_b16 v[192:193], v175 offset:520\n"
    "v_cvt_pk_bf16_f32 v122, v60, v61\n"
    "v_cvt_pk_bf16_f32 v123, v62, v63\n"
    "v_mfma_f32_16x16x32_bf16 v[40:43], v[236:239], a[204:207], v[40:43]\n"
    "ds_read_b64_tr_b16 v[194:195], v175 offset:776\n"
    "v_mul_f32 v44, v108, v44\n"
    "v_mfma_f32_16x16x32_bf16 v[40:43], v[240:243], a[208:211], v[40:43]\n"
    "v_rcp_f32 v186, v186\n"
    "v_mfma_f32_16x16x32_bf16 v[40:43], v[244:247], a[212:215], v[40:43]\n"
    "ds_read_b64_tr_b16 v[196:197], v175 offset:4616\n"
    "v_rcp_f32 v184, v184\n"
    "v_mfma_f32_16x16x32_bf16 v[40:43], v[248:251], a[216:219], v[40:43]\n"
    "v_rcp_f32 v185, v185\n"
    "v_mfma_f32_16x16x32_bf16 v[40:43], v[252:255], a[220:223], v[40:43]\n"
    "ds_read_b64_tr_b16 v[198:199], v175 offset:4872\n"
    "v_mul_f32 v92, v92, v186\n"
    "v_mfma_f32_16x16x32_bf16 v[56:59], v[252:255], a[252:255], v[56:59]\n"
    "ds_read_b64_tr_b16 v[200:201], v175 offset:8712\n"
    "v_mul_f32 v186, v166, v95\n"
    "v_mul_f32 v93, v93, v184\n"
    "v_mfma_f32_16x16x32_bf16 v[56:59], v[248:251], a[248:251], v[56:59]\n"
    "ds_read_b64_tr_b16 v[202:203], v175 offset:8968\n"
    "v_exp_f32 v186, v186\n"
    "v_mul_f32 v94, v94, v185\n"
    "v_mul_f32 v184, v165, v76\n"
    "v_mfma_f32_16x16x32_bf16 v[56:59], v[244:247], a[244:247], v[56:59]\n"
    "ds_read_b64_tr_b16 v[204:205], v175 offset:12808\n"
    "ds_read_b64_tr_b16 v[206:207], v175 offset:13064\n"
    "v_fma_f32 v186, v186, v168, v168\n"
    "v_mul_f32 v185, v165, v77\n"
    "v_mul_f32 v45, v109, v45\n"
    "v_mfma_f32_16x16x32_bf16 v[56:59], v[240:243], a[240:243], v[56:59]\n"
    "ds_read_b64_tr_b16 v[208:209], v175 offset:16904\n"
    "v_rcp_f32 v186, v186\n"
    "v_mul_f32 v46, v110, v46\n"
    "v_mul_f32 v47, v111, v47\n"
    "v_mfma_f32_16x16x32_bf16 v[56:59], v[236:239], a[236:239], v[56:59]\n"
    "ds_read_b64_tr_b16 v[210:211], v175 offset:17160\n"
    "ds_read_b64_tr_b16 v[212:213], v175 offset:21000\n"
    "v_mul_f32 v95, v95, v186\n"
    "v_exp_f32 v184, v184\n"
    "v_mfma_f32_16x16x32_bf16 v[56:59], v[232:235], a[232:235], v[56:59]\n"
    "ds_read_b64_tr_b16 v[214:215], v175 offset:21256\n"
    "ds_read_b64_tr_b16 v[216:217], v175 offset:25096\n"
    "v_mul_f32 v186, v165, v78\n"
    "v_exp_f32 v185, v185\n"
    "v_mfma_f32_16x16x32_bf16 v[56:59], v[228:231], a[228:231], v[56:59]\n"
    "ds_read_b64_tr_b16 v[218:219], v175 offset:25352\n"
    "v_exp_f32 v186, v186\n"
    "v_fma_f32 v184, v184, v167, v167\n"
    "v_fma_f32 v185, v185, v167, v167\n"
    "v_mfma_f32_16x16x32_bf16 v[56:59], v[224:227], a[224:227], v[56:59]\n"
    "s_waitcnt lgkmcnt(8)\n"
    "ds_read_b64_tr_b16 v[220:221], v175 offset:29192\n"
    "ds_read_b64_tr_b16 v[222:223], v175 offset:29448\n"
    "v_fma_f32 v186, v186, v167, v167\n"
    "v_cvt_pk_bf16_f32 v106, v44, v45\n"
    "v_cvt_pk_bf16_f32 v107, v46, v47\n"
    "v_mfma_f32_16x16x32_bf16 v[60:63], v[192:195], a[160:163], 0\n"
    "v_rcp_f32 v184, v184\n"
    "v_rcp_f32 v185, v185\n"
    "v_mfma_f32_16x16x32_bf16 v[60:63], v[196:199], a[164:167], v[60:63]\n"
    "v_rcp_f32 v186, v186\n"
    "v_mul_f32 v76, v76, v184\n"
    "v_mul_f32 v77, v77, v185\n"
    "v_mfma_f32_16x16x32_bf16 v[60:63], v[200:203], a[168:171], v[60:63]\n"
    "s_waitcnt lgkmcnt(5)\n"
    "v_mul_f32 v184, v165, v79\n"
    "v_mul_f32 v78, v78, v186\n"
    "v_mul_f32 v28, v92, v28\n"
    "v_mul_f32 v29, v93, v29\n"
    "v_mul_f32 v30, v94, v30\n"
    "v_mfma_f32_16x16x32_bf16 v[60:63], v[204:207], a[172:175], v[60:63]\n"
    "v_exp_f32 v184, v184\n"
    "v_mul_f32 v31, v95, v31\n"
    "v_mfma_f32_16x16x32_bf16 v[60:63], v[208:211], a[176:179], v[60:63]\n"
    "s_waitcnt lgkmcnt(2)\n"
    "v_fma_f32 v184, v184, v167, v167\n"
    "v_cvt_pk_bf16_f32 v90, v28, v29\n"
    "v_mfma_f32_16x16x32_bf16 v[60:63], v[212:215], a[180:183], v[60:63]\n"
    "v_rcp_f32 v184, v184\n"
    "v_mfma_f32_16x16x32_bf16 v[60:63], v[216:219], a[184:187], v[60:63]\n"
    "s_waitcnt lgkmcnt(0)\n"
    "v_mul_f32 v79, v79, v184\n"
    "v_cvt_pk_bf16_f32 v91, v30, v31\n"
    "v_mfma_f32_16x16x32_bf16 v[60:63], v[220:223], a[188:191], v[60:63]\n"
    "v_mul_f32 v184, 0xbdb504f3, v165\n"
    "v_mul_f32 v185, 0xbdb504f3, v166\n"
    "v_mul_f32 v186, 0xbdb504f3, v171\n"
    "v_mul_f32 v187, 0xbdb504f3, v172\n"
    "v_mfma_f32_16x16x32_bf16 v[44:47], v[220:223], a[156:159], 0\n"
    "v_mul_f32 v12, v76, v12\n"
    "v_mul_f32 v13, v77, v13\n"
    "v_mfma_f32_16x16x32_bf16 v[44:47], v[216:219], a[152:155], v[44:47]\n"
    "v_mul_f32 v14, v78, v14\n"
    "v_mul_f32 v15, v79, v15\n"
    "v_mfma_f32_16x16x32_bf16 v[44:47], v[212:215], a[148:151], v[44:47]\n"
    "v_cvt_pk_bf16_f32 v74, v12, v13\n"
    "v_cvt_pk_bf16_f32 v75, v14, v15\n"
    "v_mfma_f32_16x16x32_bf16 v[44:47], v[208:211], a[144:147], v[44:47]\n"
    "buffer_store_dwordx4 v[120:123], v180, s[72:75], s89 offen offset:1024 sc1\n"
    "v_lshrrev_b32 v148, 4, v181\n"
    "v_mfma_f32_16x16x32_bf16 v[44:47], v[204:207], a[140:143], v[44:47]\n"
    "ds_read_b64_tr_b16 v[224:225], v175 offset:33288\n"
    "v_sub_u32 v148, 0, v148\n"
    "v_mfma_f32_16x16x32_bf16 v[44:47], v[200:203], a[136:139], v[44:47]\n"
    "ds_read_b64_tr_b16 v[226:227], v175 offset:33544\n"
    "v_xor_b32 v148, v148, v181\n"
    "v_mfma_f32_16x16x32_bf16 v[44:47], v[196:199], a[132:135], v[44:47]\n"
    "ds_read_b64_tr_b16 v[228:229], v175 offset:37384\n"
    "v_and_b32 v148, 3, v148\n"
    "v_mfma_f32_16x16x32_bf16 v[44:47], v[192:195], a[128:131], v[44:47]\n"
    "ds_read_b64_tr_b16 v[230:231], v175 offset:37640\n"
    "v_lshlrev_b32 v148, 4, v148\n"
    "v_mfma_f32_16x16x32_bf16 v[28:31], v[192:195], a[32:35], 0\n"
    "ds_read_b64_tr_b16 v[232:233], v175 offset:41480\n"
    "v_lshrrev_b32 v149, 2, v181\n"
    "v_mfma_f32_16x16x32_bf16 v[28:31], v[196:199], a[36:39], v[28:31]\n"
    "ds_read_b64_tr_b16 v[234:235], v175 offset:41736\n"
    "v_lshl_add_u32 v148, v149, 8, v148\n"
    "v_mfma_f32_16x16x32_bf16 v[28:31], v[200:203], a[40:43], v[28:31]\n"
    "ds_read_b64_tr_b16 v[236:237], v175 offset:45576\n"
    "v_add_u32 v167, 3072, v148\n"
    "v_mfma_f32_16x16x32_bf16 v[28:31], v[204:207], a[44:47], v[28:31]\n"
    "ds_read_b64_tr_b16 v[238:239], v175 offset:45832\n"
    "v_add_u32 v168, 2112, v148\n"
    "v_mfma_f32_16x16x32_bf16 v[28:31], v[208:211], a[48:51], v[28:31]\n"
    "ds_read_b64_tr_b16 v[240:241], v175 offset:49672\n"
    "v_add_u32 v169, 1152, v148\n"
    "v_mfma_f32_16x16x32_bf16 v[28:31], v[212:215], a[52:55], v[28:31]\n"
    "ds_read_b64_tr_b16 v[242:243], v175 offset:49928\n"
    "v_add_u32 v170, 192, v148\n"
    "v_mfma_f32_16x16x32_bf16 v[28:31], v[216:219], a[56:59], v[28:31]\n"
    "ds_read_b64_tr_b16 v[244:245], v175 offset:53768\n"
    "v_bfe_u32 v148, v181, 2, 3\n"
    "v_mfma_f32_16x16x32_bf16 v[28:31], v[220:223], a[60:63], v[28:31]\n"
    "ds_read_b64_tr_b16 v[246:247], v175 offset:54024\n"
    "v_lshlrev_b32 v148, 8, v148\n"
    "v_mfma_f32_16x16x32_bf16 v[12:15], v[220:223], a[28:31], 0\n"
    "buffer_store_dwordx4 v[104:107], v180, s[72:75], s88 offen offset:1024 sc1\n"
    "v_lshrrev_b32 v149, 5, v181\n"
    "v_mfma_f32_16x16x32_bf16 v[12:15], v[216:219], a[24:27], v[12:15]\n"
    "ds_read_b64_tr_b16 v[248:249], v175 offset:57864\n"
    "v_lshl_add_u32 v148, v149, 6, v148\n"
    "v_mfma_f32_16x16x32_bf16 v[12:15], v[212:215], a[20:23], v[12:15]\n"
    "buffer_store_dwordx4 v[88:91], v180, s[72:75], s87 offen offset:1024 sc1\n"
    "v_lshrrev_b32 v149, 1, v181\n"
    "v_mfma_f32_16x16x32_bf16 v[12:15], v[208:211], a[16:19], v[12:15]\n"
    "ds_read_b64_tr_b16 v[250:251], v175 offset:58120\n"
    "v_lshrrev_b32 v150, 4, v181\n"
    "v_mfma_f32_16x16x32_bf16 v[12:15], v[204:207], a[12:15], v[12:15]\n"
    "ds_read_b64_tr_b16 v[252:253], v175 offset:61960\n"
    "v_xor_b32 v149, v149, v150\n"
    "v_mfma_f32_16x16x32_bf16 v[12:15], v[200:203], a[8:11], v[12:15]\n"
    "ds_read_b64_tr_b16 v[254:255], v175 offset:62216\n"
    "v_and_b32 v149, 1, v149\n"
    "v_mfma_f32_16x16x32_bf16 v[12:15], v[196:199], a[4:7], v[12:15]\n"
    "v_lshl_add_u32 v148, v149, 5, v148\n"
    "v_and_b32 v149, 1, v181\n"
    "v_mfma_f32_16x16x32_bf16 v[12:15], v[192:195], a[0:3], v[12:15]\n"
    "s_waitcnt lgkmcnt(3)\n"
    "v_lshl_add_u32 v148, v149, 4, v148\n"
    "v_add_u32 v171, 1024, v148\n"
    "v_mfma_f32_16x16x32_bf16 v[12:15], v[224:227], a[64:67], v[12:15]\n"
    "buffer_store_dwordx4 v[72:75], v180, s[72:75], s86 offen offset:1024 sc1\n"
    "v_add_u32 v172, 128, v148\n"
    "v_mfma_f32_16x16x32_bf16 v[12:15], v[228:231], a[68:71], v[12:15]\n"
    "v_fmac_f32 v19, v189, v155\n"
    "v_fmac_f32 v32, v190, v152\n"
    "v_mfma_f32_16x16x32_bf16 v[12:15], v[232:235], a[72:75], v[12:15]\n"
    "v_fmac_f32 v33, v190, v153\n"
    "v_fmac_f32 v34, v190, v154\n"
    "v_mfma_f32_16x16x32_bf16 v[12:15], v[236:239], a[76:79], v[12:15]\n"
    "v_fmac_f32 v35, v190, v155\n"
    "v_fmac_f32 v48, v191, v152\n"
    "v_fmac_f32 v49, v191, v153\n"
    "v_fmac_f32 v50, v191, v154\n"
    "v_fmac_f32 v51, v191, v155\n"
    "v_mfma_f32_16x16x32_bf16 v[12:15], v[240:243], a[80:83], v[12:15]\n"
    "ds_read_b128 v[152:155], v178 offset:384\n"
    "v_mul_f32 v0, v184, v0\n"
    "v_mfma_f32_16x16x32_bf16 v[12:15], v[244:247], a[84:87], v[12:15]\n"
    "s_waitcnt vmcnt(7) lgkmcnt(1)\n"
    "v_mul_f32 v1, v184, v1\n"
    "v_mul_f32 v2, v184, v2\n"
    "v_mfma_f32_16x16x32_bf16 v[12:15], v[248:251], a[88:91], v[12:15]\n"
    "s_barrier\n"
    "v_mul_f32 v3, v184, v3\n"
    "v_mul_f32 v16, v185, v16\n"
    "v_mfma_f32_16x16x32_bf16 v[12:15], v[252:255], a[92:95], v[12:15]\n"
    "s_nop 0\n"
    "v_mul_f32 v17, v185, v17\n"
    "v_mul_f32 v18, v185, v18\n"
    "v_mfma_f32_16x16x32_bf16 v[28:31], v[252:255], a[124:127], v[28:31]\n"
    "v_mul_f32 v19, v185, v19\n"
    "v_mul_f32 v32, v186, v32\n"
    "v_mul_f32 v33, v186, v33\n"
    "v_mul_f32 v34, v186, v34\n"
    "v_mul_f32 v35, v186, v35\n"
    "v_mfma_f32_16x16x32_bf16 v[28:31], v[248:251], a[120:123], v[28:31]\n"
    "v_mul_f32 v48, v187, v48\n"
    "v_mul_f32 v49, v187, v49\n"
    "v_mul_f32 v50, v187, v50\n"
    "v_mul_f32 v51, v187, v51\n"
    "v_mfma_f32_16x16x32_bf16 v[28:31], v[244:247], a[116:119], v[28:31]\n"
    "ds_read_b64_tr_b16 v[192:193], v176 offset:32768\n"
    "v_cvt_pk_bf16_f32 v64, v0, v1\n"
    "v_mfma_f32_16x16x32_bf16 v[28:31], v[240:243], a[112:115], v[28:31]\n"
    "ds_read_b64_tr_b16 v[194:195], v176 offset:33024\n"
    "v_cvt_pk_bf16_f32 v65, v2, v3\n"
    "v_mfma_f32_16x16x32_bf16 v[28:31], v[236:239], a[108:111], v[28:31]\n"
    "v_cvt_pk_bf16_f32 v80, v16, v17\n"
    "v_cvt_pk_bf16_f32 v81, v18, v19\n"
    "v_cvt_pk_bf16_f32 v96, v32, v33\n"
    "v_cvt_pk_bf16_f32 v97, v34, v35\n"
    "v_cvt_pk_bf16_f32 v112, v48, v49\n"
    "v_mfma_f32_16x16x32_bf16 v[28:31], v[232:235], a[104:107], v[28:31]\n"
    "ds_read_b64_tr_b16 v[196:197], v176 offset:36864\n"
    "v_cvt_pk_bf16_f32 v113, v50, v51\n"
    "v_mfma_f32_16x16x32_bf16 v[28:31], v[228:231], a[100:103], v[28:31]\n"
    "ds_read_b64_tr_b16 v[198:199], v176 offset:37120\n"
    "v_fmac_f32 v4, v188, v156\n"
    "v_mfma_f32_16x16x32_bf16 v[28:31], v[224:227], a[96:99], v[28:31]\n"
    "s_add_u32 m0, s22, 0\n"
    "v_fmac_f32 v5, v188, v157\n"
    "v_fmac_f32 v6, v188, v158\n"
    "v_mfma_f32_16x16x32_bf16 v[44:47], v[224:227], a[192:195], v[44:47]\n"
    "s_add_u32 s16, s22, 0\n"
    "ds_read_b64_tr_b16 v[200:201], v176 offset:40960\n"
    "v_fmac_f32 v7, v188, v159\n"
    "v_mfma_f32_16x16x32_bf16 v[44:47], v[228:231], a[196:199], v[44:47]\n"
    "buffer_load_dwordx4 v167, s[8:11], s16 offen offset:0 lds\n"
    "v_mul_f32 v4, v184, v4\n"
    "v_mfma_f32_16x16x32_bf16 v[44:47], v[232:235], a[200:203], v[44:47]\n"
    "ds_read_b64_tr_b16 v[202:203], v176 offset:41216\n"
    "v_mul_f32 v5, v184, v5\n"
    "v_mfma_f32_16x16x32_bf16 v[44:47], v[236:239], a[204:207], v[44:47]\n"
    "v_mul_f32 v6, v184, v6\n"
    "v_mul_f32 v7, v184, v7\n"
    "v_mfma_f32_16x16x32_bf16 v[44:47], v[240:243], a[208:211], v[44:47]\n"
    "ds_read_b64_tr_b16 v[204:205], v176 offset:45056\n"
    "v_cvt_pk_bf16_f32 v66, v4, v5\n"
    "v_mfma_f32_16x16x32_bf16 v[44:47], v[244:247], a[212:215], v[44:47]\n"
    "buffer_load_dwordx4 v168, s[8:11], s16 offen offset:1024 lds\n"
    "v_cvt_pk_bf16_f32 v67, v6, v7\n"
    "v_mfma_f32_16x16x32_bf16 v[44:47], v[248:251], a[216:219], v[44:47]\n"
    "ds_read_b64_tr_b16 v[206:207], v176 offset:45312\n"
    "v_fmac_f32 v20, v189, v156\n"
    "v_mfma_f32_16x16x32_bf16 v[44:47], v[252:255], a[220:223], v[44:47]\n"
    "v_fmac_f32 v21, v189, v157\n"
    "v_fmac_f32 v22, v189, v158\n"
    "v_mfma_f32_16x16x32_bf16 v[60:63], v[252:255], a[252:255], v[60:63]\n"
    "ds_read_b64_tr_b16 v[208:209], v176 offset:49152\n"
    "v_fmac_f32 v23, v189, v159\n"
    "v_mfma_f32_16x16x32_bf16 v[60:63], v[248:251], a[248:251], v[60:63]\n"
    "ds_read_b64_tr_b16 v[210:211], v176 offset:49408\n"
    "v_mul_f32 v20, v185, v20\n"
    "v_mfma_f32_16x16x32_bf16 v[60:63], v[244:247], a[244:247], v[60:63]\n"
    "ds_read_b64_tr_b16 v[212:213], v176 offset:53248\n"
    "v_mul_f32 v21, v185, v21\n"
    "v_mfma_f32_16x16x32_bf16 v[60:63], v[240:243], a[240:243], v[60:63]\n"
    "ds_read_b64_tr_b16 v[214:215], v176 offset:53504\n"
    "v_mul_f32 v22, v185, v22\n"
    "v_mfma_f32_16x16x32_bf16 v[60:63], v[236:239], a[236:239], v[60:63]\n"
    "ds_read_b64_tr_b16 v[216:217], v176 offset:57344\n"
    "v_mul_f32 v23, v185, v23\n"
    "v_mfma_f32_16x16x32_bf16 v[60:63], v[232:235], a[232:235], v[60:63]\n"
    "ds_read_b64_tr_b16 v[218:219], v176 offset:57600\n"
    "v_cvt_pk_bf16_f32 v82, v20, v21\n"
    "v_mfma_f32_16x16x32_bf16 v[60:63], v[228:231], a[228:231], v[60:63]\n"
    "ds_read_b64_tr_b16 v[220:221], v176 offset:61440\n"
    "ds_read_b64_tr_b16 v[222:223], v176 offset:61696\n"
    "v_mfma_f32_16x16x32_bf16 v[60:63], v[224:227], a[224:227], v[60:63]\n"
    "s_waitcnt lgkmcnt(6)\n"
    "buffer_load_dwordx4 v169, s[8:11], s16 offen offset:2048 lds\n"
    "v_cvt_pk_bf16_f32 v83, v22, v23\n"
    "v_mfma_f32_16x16x32_bf16 v[48:51], v[192:195], a[160:163], 0\n"
    "v_fmac_f32 v36, v190, v156\n"
    "v_fmac_f32 v37, v190, v157\n"
    "v_mfma_f32_16x16x32_bf16 v[48:51], v[196:199], a[164:167], v[48:51]\n"
    "buffer_load_dwordx4 v170, s[8:11], s16 offen offset:3072 lds\n"
    "v_fmac_f32 v38, v190, v158\n"
    "v_mfma_f32_16x16x32_bf16 v[48:51], v[200:203], a[168:171], v[48:51]\n"
    "v_fmac_f32 v39, v190, v159\n"
    "v_mul_f32 v36, v186, v36\n"
    "v_mfma_f32_16x16x32_bf16 v[48:51], v[204:207], a[172:175], v[48:51]\n"
    "v_mul_f32 v37, v186, v37\n"
    "v_mul_f32 v38, v186, v38\n"
    "v_mfma_f32_16x16x32_bf16 v[48:51], v[208:211], a[176:179], v[48:51]\n"
    "s_waitcnt lgkmcnt(0)\n"
    "v_mul_f32 v39, v186, v39\n"
    "v_cvt_pk_bf16_f32 v98, v36, v37\n"
    "v_mfma_f32_16x16x32_bf16 v[48:51], v[212:215], a[180:183], v[48:51]\n"
    "ds_read_b64_tr_b16 v[128:129], v179 offset:32768\n"
    "v_cvt_pk_bf16_f32 v99, v38, v39\n"
    "v_mfma_f32_16x16x32_bf16 v[48:51], v[216:219], a[184:187], v[48:51]\n"
    "ds_read_b64_tr_b16 v[130:131], v179 offset:33024\n"
    "v_fmac_f32 v52, v191, v156\n"
    "v_mfma_f32_16x16x32_bf16 v[48:51], v[220:223], a[188:191], v[48:51]\n"
    "ds_read_b64_tr_b16 v[132:133], v179 offset:36864\n"
    "v_fmac_f32 v53, v191, v157\n"
    "v_mfma_f32_16x16x32_bf16 v[32:35], v[220:223], a[156:159], 0\n"
    "s_add_u32 m0, s22, 16384\n"
    "ds_read_b64_tr_b16 v[134:135], v179 offset:37120\n"
    "v_fmac_f32 v54, v191, v158\n"
    "v_mfma_f32_16x16x32_bf16 v[32:35], v[216:219], a[152:155], v[32:35]\n"
    "buffer_load_dwordx4 v171, s[12:15], s16 offen offset:0 sc1 lds\n"
    "v_fmac_f32 v55, v191, v159\n"
    "v_mfma_f32_16x16x32_bf16 v[32:35], v[212:215], a[148:151], v[32:35]\n"
    "ds_read_b128 v[156:159], v178 offset:448\n"
    "v_mov_b32 v144, 0\n"
    "v_mfma_f32_16x16x32_bf16 v[32:35], v[208:211], a[144:147], v[32:35]\n"
    "ds_read_b64_tr_b16 v[136:137], v179 offset:40960\n"
    "v_mov_b32 v145, 0\n"
    "v_mfma_f32_16x16x32_bf16 v[32:35], v[204:207], a[140:143], v[32:35]\n"
    "ds_read_b64_tr_b16 v[138:139], v179 offset:41216\n"
    "v_mov_b32 v146, 0\n"
    "v_mfma_f32_16x16x32_bf16 v[32:35], v[200:203], a[136:139], v[32:35]\n"
    "ds_read_b64_tr_b16 v[140:141], v179 offset:45056\n"
    "v_mov_b32 v147, 0\n"
    "v_mfma_f32_16x16x32_bf16 v[32:35], v[196:199], a[132:135], v[32:35]\n"
    "ds_read_b64_tr_b16 v[142:143], v179 offset:45312\n"
    "v_mul_f32 v52, v187, v52\n"
    "v_mfma_f32_16x16x32_bf16 v[32:35], v[192:195], a[128:131], v[32:35]\n"
    "ds_read_b64_tr_b16 v[224:225], v176 offset:32776\n"
    "v_mul_f32 v53, v187, v53\n"
    "v_mfma_f32_16x16x32_bf16 v[16:19], v[192:195], a[32:35], 0\n"
    "ds_read_b64_tr_b16 v[226:227], v176 offset:33032\n"
    "v_mul_f32 v54, v187, v54\n"
    "v_mfma_f32_16x16x32_bf16 v[16:19], v[196:199], a[36:39], v[16:19]\n"
    "buffer_load_dwordx4 v172, s[12:15], s16 offen offset:1024 sc1 lds\n"
    "v_mul_f32 v55, v187, v55\n"
    "v_mfma_f32_16x16x32_bf16 v[16:19], v[200:203], a[40:43], v[16:19]\n"
    "ds_read_b64_tr_b16 v[228:229], v176 offset:36872\n"
    "v_cvt_pk_bf16_f32 v114, v52, v53\n"
    "v_mfma_f32_16x16x32_bf16 v[16:19], v[204:207], a[44:47], v[16:19]\n"
    "ds_read_b64_tr_b16 v[230:231], v176 offset:37128\n"
    "v_cvt_pk_bf16_f32 v115, v54, v55\n"
    "v_mfma_f32_16x16x32_bf16 v[16:19], v[208:211], a[48:51], v[16:19]\n"
    "ds_read_b64_tr_b16 v[232:233], v176 offset:40968\n"
    "v_fmac_f32 v8, v188, v152\n"
    "v_mfma_f32_16x16x32_bf16 v[16:19], v[212:215], a[52:55], v[16:19]\n"
    "ds_read_b64_tr_b16 v[234:235], v176 offset:41224\n"
    "v_fmac_f32 v9, v188, v153\n"
    "v_mfma_f32_16x16x32_bf16 v[16:19], v[216:219], a[56:59], v[16:19]\n"
    "ds_read_b64_tr_b16 v[236:237], v176 offset:45064\n"
    "v_fmac_f32 v10, v188, v154\n"
    "v_mfma_f32_16x16x32_bf16 v[16:19], v[220:223], a[60:63], v[16:19]\n"
    "ds_read_b64_tr_b16 v[238:239], v176 offset:45320\n"
    "v_fmac_f32 v11, v188, v155\n"
    "v_mfma_f32_16x16x32_bf16 v[0:3], v[220:223], a[28:31], 0\n"
    "ds_read_b64_tr_b16 v[240:241], v176 offset:49160\n"
    "v_fmac_f32 v24, v189, v152\n"
    "v_mfma_f32_16x16x32_bf16 v[0:3], v[216:219], a[24:27], v[0:3]\n"
    "ds_read_b64_tr_b16 v[242:243], v176 offset:49416\n"
    "v_fmac_f32 v25, v189, v153\n"
    "v_mfma_f32_16x16x32_bf16 v[0:3], v[212:215], a[20:23], v[0:3]\n"
    "ds_read_b64_tr_b16 v[244:245], v176 offset:53256\n"
    "v_fmac_f32 v26, v189, v154\n"
    "v_mfma_f32_16x16x32_bf16 v[0:3], v[208:211], a[16:19], v[0:3]\n"
    "ds_read_b64_tr_b16 v[246:247], v176 offset:53512\n"
    "v_fmac_f32 v27, v189, v155\n"
    "v_mfma_f32_16x16x32_bf16 v[0:3], v[204:207], a[12:15], v[0:3]\n"
    "ds_read_b64_tr_b16 v[248:249], v176 offset:57352\n"
    "v_fmac_f32 v40, v190, v152\n"
    "v_mfma_f32_16x16x32_bf16 v[0:3], v[200:203], a[8:11], v[0:3]\n"
    "ds_read_b64_tr_b16 v[250:251], v176 offset:57608\n"
    "v_fmac_f32 v41, v190, v153\n"
    "v_mfma_f32_16x16x32_bf16 v[0:3], v[196:199], a[4:7], v[0:3]\n"
    "buffer_load_dwordx4 v171, s[12:15], s16 offen offset:2048 sc1 lds\n"
    "v_fmac_f32 v42, v190, v154\n"
    "v_mfma_f32_16x16x32_bf16 v[0:3], v[192:195], a[0:3], v[0:3]\n"
    "s_waitcnt lgkmcnt(4)\n"
    "ds_read_b64_tr_b16 v[252:253], v176 offset:61448\n"
    "v_fmac_f32 v43, v190, v155\n"
    "v_smfmac_f32_16x16x64_bf16 v[144:147], v[160:163], v[128:135], v183\n"
    "ds_read_b64_tr_b16 v[254:255], v176 offset:61704\n"
    "v_fmac_f32 v56, v191, v152\n"
    "v_smfmac_f32_16x16x64_bf16 v[144:147], v[160:163], v[136:143], v183\n"
    "v_fmac_f32 v57, v191, v153\n"
    "v_fmac_f32 v58, v191, v154\n"
    "v_mfma_f32_16x16x32_bf16 v[4:7], v[224:227], a[0:3], 0\n"
    "buffer_load_dwordx4 v172, s[12:15], s16 offen offset:3072 sc1 lds\n"
    "v_fmac_f32 v59, v191, v155\n"
    "v_mfma_f32_16x16x32_bf16 v[4:7], v[228:231], a[4:7], v[4:7]\n"
    "s_add_u32 m0, s22, 32768\n"
    "ds_read_b64_tr_b16 v[128:129], v179 offset:49152\n"
    "v_mul_f32 v8, v184, v8\n"
    "v_mfma_f32_16x16x32_bf16 v[4:7], v[232:235], a[8:11], v[4:7]\n"
    "s_add_u32 s16, s22, 16384\n"
    "ds_read_b64_tr_b16 v[130:131], v179 offset:49408\n"
    "v_mul_f32 v9, v184, v9\n"
    "v_mfma_f32_16x16x32_bf16 v[4:7], v[236:239], a[12:15], v[4:7]\n"
    "ds_read_b64_tr_b16 v[132:133], v179 offset:53248\n"
    "v_mul_f32 v10, v184, v10\n"
    "v_mfma_f32_16x16x32_bf16 v[4:7], v[240:243], a[16:19], v[4:7]\n"
    "s_waitcnt lgkmcnt(3)\n"
    "ds_read_b64_tr_b16 v[134:135], v179 offset:53504\n"
    "v_mul_f32 v11, v184, v11\n"
    "v_mfma_f32_16x16x32_bf16 v[4:7], v[244:247], a[20:23], v[4:7]\n"
    "ds_read_b64_tr_b16 v[136:137], v179 offset:57344\n"
    "v_mul_f32 v24, v185, v24\n"
    "v_mfma_f32_16x16x32_bf16 v[4:7], v[248:251], a[24:27], v[4:7]\n"
    "ds_read_b64_tr_b16 v[138:139], v179 offset:57600\n"
    "v_mul_f32 v25, v185, v25\n"
    "v_mfma_f32_16x16x32_bf16 v[4:7], v[252:255], a[28:31], v[4:7]\n"
    "ds_read_b64_tr_b16 v[140:141], v179 offset:61440\n"
    "v_mul_f32 v26, v185, v26\n"
    "v_mfma_f32_16x16x32_bf16 v[20:23], v[252:255], a[60:63], 0\n"
    "ds_read_b64_tr_b16 v[142:143], v179 offset:61696\n"
    "v_mul_f32 v27, v185, v27\n"
    "v_mfma_f32_16x16x32_bf16 v[20:23], v[248:251], a[56:59], v[20:23]\n"
    "v_mul_f32 v40, v186, v40\n"
    "v_mul_f32 v41, v186, v41\n"
    "v_mfma_f32_16x16x32_bf16 v[20:23], v[244:247], a[52:55], v[20:23]\n"
    "v_mul_f32 v42, v186, v42\n"
    "v_mul_f32 v43, v186, v43\n"
    "v_mul_f32 v56, v187, v56\n"
    "v_mul_f32 v57, v187, v57\n"
    "v_mul_f32 v58, v187, v58\n"
    "v_mfma_f32_16x16x32_bf16 v[20:23], v[240:243], a[48:51], v[20:23]\n"
    "buffer_load_dwordx4 v167, s[8:11], s16 offen offset:0 lds\n"
    "v_mul_f32 v59, v187, v59\n"
    "v_mfma_f32_16x16x32_bf16 v[20:23], v[236:239], a[44:47], v[20:23]\n"
    "buffer_load_dwordx4 v168, s[8:11], s16 offen offset:1024 lds\n"
    "v_cvt_pk_bf16_f32 v68, v8, v9\n"
    "v_mfma_f32_16x16x32_bf16 v[20:23], v[232:235], a[40:43], v[20:23]\n"
    "ds_read_b64_tr_b16 v[192:193], v176 offset:33280\n"
    "v_cvt_pk_bf16_f32 v69, v10, v11\n"
    "v_mfma_f32_16x16x32_bf16 v[20:23], v[228:231], a[36:39], v[20:23]\n"
    "v_cvt_pk_bf16_f32 v84, v24, v25\n"
    "v_cvt_pk_bf16_f32 v85, v26, v27\n"
    "v_cvt_pk_bf16_f32 v100, v40, v41\n"
    "v_cvt_pk_bf16_f32 v101, v42, v43\n"
    "v_cvt_pk_bf16_f32 v116, v56, v57\n"
    "v_mfma_f32_16x16x32_bf16 v[20:23], v[224:227], a[32:35], v[20:23]\n"
    "s_waitcnt lgkmcnt(1)\n"
    "ds_read_b64_tr_b16 v[194:195], v176 offset:33536\n"
    "v_cvt_pk_bf16_f32 v117, v58, v59\n"
    "v_smfmac_f32_16x16x64_bf16 v[144:147], v[160:163], v[128:135], v183\n"
    "ds_read_b64_tr_b16 v[196:197], v176 offset:37376\n"
    "v_fmac_f32 v12, v188, v156\n"
    "v_smfmac_f32_16x16x64_bf16 v[144:147], v[160:163], v[136:143], v183\n"
    "ds_read_b64_tr_b16 v[128:129], v179 offset:0\n"
    "v_fmac_f32 v13, v188, v157\n"
    "v_mfma_f32_16x16x32_bf16 v[36:39], v[224:227], a[128:131], 0\n"
    "ds_read_b64_tr_b16 v[130:131], v179 offset:256\n"
    "v_fmac_f32 v14, v188, v158\n"
    "v_mfma_f32_16x16x32_bf16 v[36:39], v[228:231], a[132:135], v[36:39]\n"
    "ds_read_b64_tr_b16 v[132:133], v179 offset:4096\n"
    "v_fmac_f32 v15, v188, v159\n"
    "v_mfma_f32_16x16x32_bf16 v[36:39], v[232:235], a[136:139], v[36:39]\n"
    "ds_read_b64_tr_b16 v[134:135], v179 offset:4352\n"
    "v_mul_f32 v12, v184, v12\n"
    "v_mfma_f32_16x16x32_bf16 v[36:39], v[236:239], a[140:143], v[36:39]\n"
    "ds_read_b64_tr_b16 v[136:137], v179 offset:8192\n"
    "v_mul_f32 v13, v184, v13\n"
    "v_mfma_f32_16x16x32_bf16 v[36:39], v[240:243], a[144:147], v[36:39]\n"
    "ds_read_b64_tr_b16 v[138:139], v179 offset:8448\n"
    "v_mul_f32 v14, v184, v14\n"
    "v_mfma_f32_16x16x32_bf16 v[36:39], v[244:247], a[148:151], v[36:39]\n"
    "ds_read_b64_tr_b16 v[140:141], v179 offset:12288\n"
    "v_mul_f32 v15, v184, v15\n"
    "v_mfma_f32_16x16x32_bf16 v[36:39], v[248:251], a[152:155], v[36:39]\n"
    "ds_read_b64_tr_b16 v[142:143], v179 offset:12544\n"
    "v_cvt_pk_bf16_f32 v70, v12, v13\n"
    "v_mfma_f32_16x16x32_bf16 v[36:39], v[252:255], a[156:159], v[36:39]\n"
    "ds_read_b64_tr_b16 v[198:199], v176 offset:37632\n"
    "v_cvt_pk_bf16_f32 v71, v14, v15\n"
    "v_mfma_f32_16x16x32_bf16 v[52:55], v[252:255], a[188:191], 0\n"
    "ds_read_b64_tr_b16 v[200:201], v176 offset:41472\n"
    "v_fmac_f32 v28, v189, v156\n"
    "v_mfma_f32_16x16x32_bf16 v[52:55], v[248:251], a[184:187], v[52:55]\n"
    "ds_read_b64_tr_b16 v[202:203], v176 offset:41728\n"
    "v_fmac_f32 v29, v189, v157\n"
    "v_mfma_f32_16x16x32_bf16 v[52:55], v[244:247], a[180:183], v[52:55]\n"
    "ds_read_b64_tr_b16 v[204:205], v176 offset:45568\n"
    "v_fmac_f32 v30, v189, v158\n"
    "v_mfma_f32_16x16x32_bf16 v[52:55], v[240:243], a[176:179], v[52:55]\n"
    "ds_read_b64_tr_b16 v[206:207], v176 offset:45824\n"
    "v_fmac_f32 v31, v189, v159\n"
    "v_mfma_f32_16x16x32_bf16 v[52:55], v[236:239], a[172:175], v[52:55]\n"
    "ds_read_b64_tr_b16 v[208:209], v176 offset:49664\n"
    "v_mul_f32 v28, v185, v28\n"
    "v_mfma_f32_16x16x32_bf16 v[52:55], v[232:235], a[168:171], v[52:55]\n"
    "ds_read_b64_tr_b16 v[210:211], v176 offset:49920\n"
    "buffer_load_dwordx4 v169, s[8:11], s16 offen offset:2048 lds\n"
    "v_mfma_f32_16x16x32_bf16 v[52:55], v[228:231], a[164:167], v[52:55]\n"
    "ds_read_b64_tr_b16 v[212:213], v176 offset:53760\n"
    "ds_read_b64_tr_b16 v[214:215], v176 offset:54016\n"
    "v_mfma_f32_16x16x32_bf16 v[52:55], v[224:227], a[160:163], v[52:55]\n"
    "s_waitcnt lgkmcnt(6)\n"
    "ds_read_b64_tr_b16 v[216:217], v176 offset:57856\n"
    "ds_read_b64_tr_b16 v[218:219], v176 offset:58112\n"
    "v_smfmac_f32_16x16x64_bf16 v[144:147], v[160:163], v[128:135], v183\n"
    "ds_read_b64_tr_b16 v[220:221], v176 offset:61952\n"
    "ds_read_b64_tr_b16 v[222:223], v176 offset:62208\n"
    "v_smfmac_f32_16x16x64_bf16 v[144:147], v[160:163], v[136:143], v183\n"
    "buffer_load_dwordx4 v170, s[8:11], s16 offen offset:3072 lds\n"
    "v_mul_f32 v29, v185, v29\n"
    "v_mfma_f32_16x16x32_bf16 v[56:59], v[192:195], a[160:163], 0\n"
    "s_add_u32 m0, s22, 49152\n"
    "ds_read_b64_tr_b16 v[128:129], v179 offset:16384\n"
    "v_mul_f32 v30, v185, v30\n"
    "v_mfma_f32_16x16x32_bf16 v[56:59], v[196:199], a[164:167], v[56:59]\n"
    "ds_read_b64_tr_b16 v[130:131], v179 offset:16640\n"
    "v_mul_f32 v31, v185, v31\n"
    "v_mfma_f32_16x16x32_bf16 v[56:59], v[200:203], a[168:171], v[56:59]\n"
    "s_waitcnt lgkmcnt(6)\n"
    "ds_read_b64_tr_b16 v[132:133], v179 offset:20480\n"
    "v_cvt_pk_bf16_f32 v86, v28, v29\n"
    "v_mfma_f32_16x16x32_bf16 v[56:59], v[204:207], a[172:175], v[56:59]\n"
    "ds_read_b64_tr_b16 v[134:135], v179 offset:20736\n"
    "v_cvt_pk_bf16_f32 v87, v30, v31\n"
    "v_mfma_f32_16x16x32_bf16 v[56:59], v[208:211], a[176:179], v[56:59]\n"
    "ds_read_b64_tr_b16 v[136:137], v179 offset:24576\n"
    "v_fmac_f32 v44, v190, v156\n"
    "v_mfma_f32_16x16x32_bf16 v[56:59], v[212:215], a[180:183], v[56:59]\n"
    "s_waitcnt lgkmcnt(5)\n"
    "ds_read_b64_tr_b16 v[138:139], v179 offset:24832\n"
    "v_fmac_f32 v45, v190, v157\n"
    "v_mfma_f32_16x16x32_bf16 v[56:59], v[216:219], a[184:187], v[56:59]\n"
    "ds_read_b64_tr_b16 v[140:141], v179 offset:28672\n"
    "v_fmac_f32 v46, v190, v158\n"
    "v_mfma_f32_16x16x32_bf16 v[56:59], v[220:223], a[188:191], v[56:59]\n"
    "ds_read_b64_tr_b16 v[142:143], v179 offset:28928\n"
    "v_fmac_f32 v47, v190, v159\n"
    "v_mfma_f32_16x16x32_bf16 v[40:43], v[220:223], a[156:159], 0\n"
    "buffer_load_dwordx4 v171, s[12:15], s16 offen offset:0 sc1 lds\n"
    "v_mul_f32 v44, v186, v44\n"
    "v_mfma_f32_16x16x32_bf16 v[40:43], v[216:219], a[152:155], v[40:43]\n"
    "v_mul_f32 v45, v186, v45\n"
    "v_mul_f32 v46, v186, v46\n"
    "v_mfma_f32_16x16x32_bf16 v[40:43], v[212:215], a[148:151], v[40:43]\n"
    "v_mul_f32 v47, v186, v47\n"
    "v_cvt_pk_bf16_f32 v102, v44, v45\n"
    "v_mfma_f32_16x16x32_bf16 v[40:43], v[208:211], a[144:147], v[40:43]\n"
    "buffer_load_dwordx4 v172, s[12:15], s16 offen offset:1024 sc1 lds\n"
    "v_cvt_pk_bf16_f32 v103, v46, v47\n"
    "v_mfma_f32_16x16x32_bf16 v[40:43], v[204:207], a[140:143], v[40:43]\n"
    "v_fmac_f32 v60, v191, v156\n"
    "v_fmac_f32 v61, v191, v157\n"
    "v_mfma_f32_16x16x32_bf16 v[40:43], v[200:203], a[136:139], v[40:43]\n"
    "v_fmac_f32 v62, v191, v158\n"
    "v_fmac_f32 v63, v191, v159\n"
    "v_mfma_f32_16x16x32_bf16 v[40:43], v[196:199], a[132:135], v[40:43]\n"
    "v_mul_f32 v60, v187, v60\n"
    "v_mul_f32 v61, v187, v61\n"
    "v_mfma_f32_16x16x32_bf16 v[40:43], v[192:195], a[128:131], v[40:43]\n"
    "s_waitcnt lgkmcnt(0)\n"
    "v_mul_f32 v62, v187, v62\n"
    "v_mul_f32 v63, v187, v63\n"
    "v_smfmac_f32_16x16x64_bf16 v[144:147], v[160:163], v[128:135], v183\n"
    "ds_read_b64_tr_b16 v[224:225], v176 offset:33288\n"
    "v_cvt_pk_bf16_f32 v118, v60, v61\n"
    "v_smfmac_f32_16x16x64_bf16 v[144:147], v[160:163], v[136:143], v183\n"
    "ds_read_b64_tr_b16 v[226:227], v176 offset:33544\n"
    "v_cvt_pk_bf16_f32 v119, v62, v63\n"
    "v_mfma_f32_16x16x32_bf16 v[24:27], v[192:195], a[32:35], 0\n"
    "ds_read_b64_tr_b16 v[228:229], v176 offset:37384\n"
    "v_mfma_f32_16x16x32_bf16 v[24:27], v[196:199], a[36:39], v[24:27]\n"
    "ds_read_b64_tr_b16 v[230:231], v176 offset:37640\n"
    "v_mfma_f32_16x16x32_bf16 v[24:27], v[200:203], a[40:43], v[24:27]\n"
    "ds_read_b64_tr_b16 v[232:233], v176 offset:41480\n"
    "v_mfma_f32_16x16x32_bf16 v[24:27], v[204:207], a[44:47], v[24:27]\n"
    "ds_read_b64_tr_b16 v[234:235], v176 offset:41736\n"
    "v_mfma_f32_16x16x32_bf16 v[24:27], v[208:211], a[48:51], v[24:27]\n"
    "ds_read_b64_tr_b16 v[236:237], v176 offset:45576\n"
    "v_mfma_f32_16x16x32_bf16 v[24:27], v[212:215], a[52:55], v[24:27]\n"
    "v_add_f32 v144, v144, v145\n"
    "v_lshl_add_u32 v146, v181, 2, s93\n"
    "v_mfma_f32_16x16x32_bf16 v[24:27], v[216:219], a[56:59], v[24:27]\n"
    "s_nop 0\n"
    "s_mov_b64 exec, 0xffff\n"
    "ds_write_b32 v146, v144 offset:512\n"
    "s_mov_b64 exec, -1\n"
    "ds_read_b64_tr_b16 v[238:239], v176 offset:45832\n"
    "v_mfma_f32_16x16x32_bf16 v[24:27], v[220:223], a[60:63], v[24:27]\n"
    "ds_read_b64_tr_b16 v[240:241], v176 offset:49672\n"
    "v_mfma_f32_16x16x32_bf16 v[8:11], v[220:223], a[28:31], 0\n"
    "ds_read_b64_tr_b16 v[242:243], v176 offset:49928\n"
    "v_mfma_f32_16x16x32_bf16 v[8:11], v[216:219], a[24:27], v[8:11]\n"
    "buffer_load_dwordx4 v171, s[12:15], s16 offen offset:2048 sc1 lds\n"
    "v_mfma_f32_16x16x32_bf16 v[8:11], v[212:215], a[20:23], v[8:11]\n"
    "ds_read_b64_tr_b16 v[244:245], v176 offset:53768\n"
    "v_mfma_f32_16x16x32_bf16 v[8:11], v[208:211], a[16:19], v[8:11]\n"
    "ds_read_b64_tr_b16 v[246:247], v176 offset:54024\n"
    "v_mfma_f32_16x16x32_bf16 v[8:11], v[204:207], a[12:15], v[8:11]\n"
    "ds_read_b64_tr_b16 v[248:249], v176 offset:57864\n"
    "v_mfma_f32_16x16x32_bf16 v[8:11], v[200:203], a[8:11], v[8:11]\n"
    "ds_read_b64_tr_b16 v[250:251], v176 offset:58120\n"
    "v_mfma_f32_16x16x32_bf16 v[8:11], v[196:199], a[4:7], v[8:11]\n"
    "ds_read_b64_tr_b16 v[252:253], v176 offset:61960\n"
    "ds_read_b64_tr_b16 v[254:255], v176 offset:62216\n"
    "v_mfma_f32_16x16x32_bf16 v[8:11], v[192:195], a[0:3], v[8:11]\n"
    "s_waitcnt lgkmcnt(6)\n"
    "buffer_load_dwordx4 v172, s[12:15], s16 offen offset:3072 sc1 lds\n"
    "v_mfma_f32_16x16x32_bf16 v[12:15], v[224:227], a[0:3], 0\n"
    "s_add_u32 s16, s22, 32768\n"
    "v_mfma_f32_16x16x32_bf16 v[12:15], v[228:231], a[4:7], v[12:15]\n"
    "ds_read_b64_tr_b16 v[192:193], v176 offset:0\n"
    "v_mfma_f32_16x16x32_bf16 v[12:15], v[232:235], a[8:11], v[12:15]\n"
    "v_mfma_f32_16x16x32_bf16 v[12:15], v[236:239], a[12:15], v[12:15]\n"
    "ds_read_b64_tr_b16 v[194:195], v176 offset:256\n"
    "v_mfma_f32_16x16x32_bf16 v[12:15], v[240:243], a[16:19], v[12:15]\n"
    "s_waitcnt lgkmcnt(2)\n"
    "v_mfma_f32_16x16x32_bf16 v[12:15], v[244:247], a[20:23], v[12:15]\n"
    "s_waitcnt vmcnt(16) lgkmcnt(2)\n"
    "ds_read_b64_tr_b16 v[196:197], v176 offset:4096\n"
    "v_mfma_f32_16x16x32_bf16 v[12:15], v[248:251], a[24:27], v[12:15]\n"
    "s_barrier\n"
    "s_cmp_lg_u32 s25, 0\n"
    "s_cbranch_scc1 Lpb_h%=\n"
    "v_mov_b32 v136, s92\n"
    "v_mov_b32 v138, s80\n"
    "v_mov_b32 v139, s81\n"
    "s_mov_b64 exec, 1\n"
    "global_store_dwordx2 v136, v[138:139], s[82:83] offset:0 sc1\n"
    "s_mov_b64 exec, -1\n"
    "Lpb_h%=:\n"
    "v_mfma_f32_16x16x32_bf16 v[12:15], v[252:255], a[28:31], v[12:15]\n"
    "s_mov_b32 s74, 7340032\n"
    "ds_read_b64_tr_b16 v[198:199], v176 offset:4352\n"
    "v_mfma_f32_16x16x32_bf16 v[28:31], v[252:255], a[60:63], 0\n"
    "s_mov_b32 s33, 259\n"
    "v_mfma_f32_16x16x32_bf16 v[28:31], v[248:251], a[56:59], v[28:31]\n"
    "s_mov_b32 m0, s17\n"
    "ds_read_b128 v[128:131], v178 offset:512\n"
    "v_and_b32 v148, 15, v181\n"
    "v_mfma_f32_16x16x32_bf16 v[28:31], v[244:247], a[52:55], v[28:31]\n"
    "buffer_load_dwordx4 v167, s[8:11], s16 offen offset:0 lds\n"
    "v_lshrrev_b32 v149, 2, v148\n"
    "v_mfma_f32_16x16x32_bf16 v[28:31], v[240:243], a[48:51], v[28:31]\n"
    "ds_read_b64_tr_b16 v[200:201], v176 offset:8192\n"
    "v_sub_u32 v149, 0, v149\n"
    "v_mfma_f32_16x16x32_bf16 v[28:31], v[236:239], a[44:47], v[28:31]\n"
    "ds_read_b64_tr_b16 v[202:203], v176 offset:8448\n"
    "v_and_b32 v149, 3, v149\n"
    "v_mfma_f32_16x16x32_bf16 v[28:31], v[232:235], a[40:43], v[28:31]\n"
    "ds_read_b64_tr_b16 v[204:205], v176 offset:12288\n"
    "v_lshrrev_b32 v150, 4, v181\n"
    "v_mfma_f32_16x16x32_bf16 v[28:31], v[228:231], a[36:39], v[28:31]\n"
    "ds_read_b128 v[132:135], v178 offset:576\n"
    "v_xor_b32 v149, v149, v150\n"
    "v_mfma_f32_16x16x32_bf16 v[28:31], v[224:227], a[32:35], v[28:31]\n"
    "ds_read_b64_tr_b16 v[206:207], v176 offset:12544\n"
    "v_lshlrev_b32 v149, 4, v149\n"
    "v_mfma_f32_16x16x32_bf16 v[44:47], v[224:227], a[128:131], 0\n"
    "v_lshl_add_u32 v164, v148, 6, v149\n"
    "v_lshrrev_b32 v148, 5, v181\n"
    "v_mfma_f32_16x16x32_bf16 v[44:47], v[228:231], a[132:135], v[44:47]\n"
    "ds_read_b64_tr_b16 v[208:209], v176 offset:16384\n"
    "v_lshlrev_b32 v148, 11, v148\n"
    "v_mfma_f32_16x16x32_bf16 v[44:47], v[232:235], a[136:139], v[44:47]\n"
    "v_bfe_u32 v149, v181, 2, 3\n"
    "v_lshl_add_u32 v148, v149, 6, v148\n"
    "v_mfma_f32_16x16x32_bf16 v[44:47], v[236:239], a[140:143], v[44:47]\n"
    "buffer_load_dwordx4 v168, s[8:11], s16 offen offset:1024 lds\n"
    "v_and_b32 v149, 3, v181\n"
    "v_mfma_f32_16x16x32_bf16 v[44:47], v[240:243], a[144:147], v[44:47]\n"
    "ds_read_b64_tr_b16 v[210:211], v176 offset:16640\n"
    "v_lshl_add_u32 v148, v149, 3, v148\n"
    "v_mfma_f32_16x16x32_bf16 v[44:47], v[244:247], a[148:151], v[44:47]\n"
    "ds_read_b64_tr_b16 v[212:213], v176 offset:20480\n"
    "v_bfe_u32 v149, v181, 4, 1\n"
    "v_mfma_f32_16x16x32_bf16 v[44:47], v[248:251], a[152:155], v[44:47]\n"
    "v_lshl_add_u32 v165, v149, 5, v148\n"
    "v_xor_b32 v166, 32, v165\n"
    "v_mfma_f32_16x16x32_bf16 v[44:47], v[252:255], a[156:159], v[44:47]\n"
    "ds_read_b64_tr_b16 v[214:215], v176 offset:20736\n"
    "v_mov_b32 v144, 0\n"
    "v_mfma_f32_16x16x32_bf16 v[60:63], v[252:255], a[188:191], 0\n"
    "v_mov_b32 v145, 0\n"
    "v_mov_b32 v146, 0\n"
    "v_mfma_f32_16x16x32_bf16 v[60:63], v[248:251], a[184:187], v[60:63]\n"
    "ds_read_b64_tr_b16 v[216:217], v176 offset:24576\n"
    "v_mov_b32 v147, 0\n"
    "v_mfma_f32_16x16x32_bf16 v[60:63], v[244:247], a[180:183], v[60:63]\n"
    "v_mov_b32 v148, 0\n"
    "v_mov_b32 v149, 0\n"
    "v_mfma_f32_16x16x32_bf16 v[60:63], v[240:243], a[176:179], v[60:63]\n"
    "ds_read_b64_tr_b16 v[218:219], v176 offset:24832\n"
    "v_mov_b32 v150, 0\n"
    "v_mfma_f32_16x16x32_bf16 v[60:63], v[236:239], a[172:175], v[60:63]\n"
    "ds_read_b64_tr_b16 v[220:221], v176 offset:28672\n"
    "v_mov_b32 v151, 0\n"
    "v_mfma_f32_16x16x32_bf16 v[60:63], v[232:235], a[168:171], v[60:63]\n"
    "ds_read_b64_tr_b16 v[222:223], v176 offset:28928\n"
    "v_mov_b32 v152, 0\n"
    "v_mfma_f32_16x16x32_bf16 v[60:63], v[228:231], a[164:167], v[60:63]\n"
    "buffer_load_dwordx4 v169, s[8:11], s16 offen offset:2048 lds\n"
    "v_mov_b32 v153, 0\n"
    "v_mfma_f32_16x16x32_bf16 v[60:63], v[224:227], a[160:163], v[60:63]\n"
    "s_waitcnt lgkmcnt(3)\n"
    "v_mov_b32 v154, 0\n"
    "v_mov_b32 v155, 0\n"
    "v_mfma_f32_16x16x32_bf16 v[48:51], v[192:195], a[224:227], v[48:51]\n"
    "v_mov_b32 v156, 0\n"
    "v_mov_b32 v157, 0\n"
    "v_mfma_f32_16x16x32_bf16 v[48:51], v[196:199], a[228:231], v[48:51]\n"
    "ds_read_b64_tr_b16 v[224:225], v176 offset:8\n"
    "v_mov_b32 v158, 0\n"
    "v_mfma_f32_16x16x32_bf16 v[48:51], v[200:203], a[232:235], v[48:51]\n"
    "v_mov_b32 v159, 0\n"
    "v_mfma_f32_16x16x32_bf16 v[48:51], v[204:207], a[236:239], v[48:51]\n"
    "ds_read_b64_tr_b16 v[226:227], v176 offset:264\n"
    "v_mfma_f32_16x16x32_bf16 v[48:51], v[208:211], a[240:243], v[48:51]\n"
    "v_mfma_f32_16x16x32_bf16 v[48:51], v[212:215], a[244:247], v[48:51]\n"
    "s_waitcnt lgkmcnt(2)\n"
    "ds_read_b64_tr_b16 v[228:229], v176 offset:4104\n"
    "v_mfma_f32_16x16x32_bf16 v[48:51], v[216:219], a[248:251], v[48:51]\n"
    "v_mfma_f32_16x16x32_bf16 v[48:51], v[220:223], a[252:255], v[48:51]\n"
    "buffer_load_dwordx4 v170, s[8:11], s16 offen offset:3072 lds\n"
    "v_mfma_f32_16x16x32_bf16 v[32:35], v[220:223], a[220:223], v[32:35]\n"
    "ds_read_b64_tr_b16 v[230:231], v176 offset:4360\n"
    "v_accvgpr_write_b32 a0, 0\n"
    "v_mfma_f32_16x16x32_bf16 v[32:35], v[216:219], a[216:219], v[32:35]\n"
    "ds_read_b64_tr_b16 v[232:233], v176 offset:8200\n"
    "v_accvgpr_write_b32 a1, 0\n"
    "v_mfma_f32_16x16x32_bf16 v[32:35], v[212:215], a[212:215], v[32:35]\n"
    "v_accvgpr_write_b32 a2, 0\n"
    "v_accvgpr_write_b32 a3, 0\n"
    "v_mfma_f32_16x16x32_bf16 v[32:35], v[208:211], a[208:211], v[32:35]\n"
    "ds_read_b64_tr_b16 v[234:235], v176 offset:8456\n"
    "v_accvgpr_write_b32 a4, 0\n"
    "v_mfma_f32_16x16x32_bf16 v[32:35], v[204:207], a[204:207], v[32:35]\n"
    "v_fmac_f32 v48, v191, v128\n"
    "v_fmac_f32 v49, v191, v129\n"
    "v_mfma_f32_16x16x32_bf16 v[32:35], v[200:203], a[200:203], v[32:35]\n"
    "v_fmac_f32 v50, v191, v130\n"
    "v_fmac_f32 v51, v191, v131\n"
    "v_mfma_f32_16x16x32_bf16 v[32:35], v[196:199], a[196:199], v[32:35]\n"
    "v_mul_f32 v48, v187, v48\n"
    "v_mul_f32 v49, v187, v49\n"
    "v_mfma_f32_16x16x32_bf16 v[32:35], v[192:195], a[192:195], v[32:35]\n"
    "v_mul_f32 v50, v187, v50\n"
    "v_mul_f32 v51, v187, v51\n"
    "v_mfma_f32_16x16x32_bf16 v[16:19], v[192:195], a[96:99], v[16:19]\n"
    "v_cvt_pk_bf16_f32 v120, v48, v49\n"
    "v_cvt_pk_bf16_f32 v121, v50, v51\n"
    "v_mfma_f32_16x16x32_bf16 v[16:19], v[196:199], a[100:103], v[16:19]\n"
    "s_add_u32 m0, s17, 16384\n"
    "ds_read_b64_tr_b16 v[236:237], v176 offset:12296\n"
    "v_accvgpr_write_b32 a5, 0\n"
    "v_mfma_f32_16x16x32_bf16 v[16:19], v[200:203], a[104:107], v[16:19]\n"
    "buffer_load_dwordx4 v171, s[12:15], s16 offen offset:0 sc1 lds\n"
    "v_accvgpr_write_b32 a6, 0\n"
    "v_mfma_f32_16x16x32_bf16 v[16:19], v[204:207], a[108:111], v[16:19]\n"
    "ds_read_b64_tr_b16 v[238:239], v176 offset:12552\n"
    "v_accvgpr_write_b32 a7, 0\n"
    "v_mfma_f32_16x16x32_bf16 v[16:19], v[208:211], a[112:115], v[16:19]\n"
    "ds_read_b64_tr_b16 v[240:241], v176 offset:16392\n"
    "v_fmac_f32 v32, v190, v128\n"
    "v_mfma_f32_16x16x32_bf16 v[16:19], v[212:215], a[116:119], v[16:19]\n"
    "ds_read_b64_tr_b16 v[242:243], v176 offset:16648\n"
    "v_fmac_f32 v33, v190, v129\n"
    "v_mfma_f32_16x16x32_bf16 v[16:19], v[216:219], a[120:123], v[16:19]\n"
    "ds_read_b64_tr_b16 v[244:245], v176 offset:20488\n"
    "v_fmac_f32 v34, v190, v130\n"
    "v_mfma_f32_16x16x32_bf16 v[16:19], v[220:223], a[124:127], v[16:19]\n"
    "ds_read_b64_tr_b16 v[246:247], v176 offset:20744\n"
    "v_fmac_f32 v35, v190, v131\n"
    "v_mfma_f32_16x16x32_bf16 v[0:3], v[220:223], a[92:95], v[0:3]\n"
    "v_mul_f32 v32, v186, v32\n"
    "v_mul_f32 v33, v186, v33\n"
    "v_mfma_f32_16x16x32_bf16 v[0:3], v[216:219], a[88:91], v[0:3]\n"
    "ds_read_b64_tr_b16 v[248:249], v176 offset:24584\n"
    "v_mul_f32 v34, v186, v34\n"
    "v_mfma_f32_16x16x32_bf16 v[0:3], v[212:215], a[84:87], v[0:3]\n"
    "v_mul_f32 v35, v186, v35\n"
    "v_cvt_pk_bf16_f32 v104, v32, v33\n"
    "v_mfma_f32_16x16x32_bf16 v[0:3], v[208:211], a[80:83], v[0:3]\n"
    "ds_read_b64_tr_b16 v[250:251], v176 offset:24840\n"
    "v_cvt_pk_bf16_f32 v105, v34, v35\n"
    "v_mfma_f32_16x16x32_bf16 v[0:3], v[204:207], a[76:79], v[0:3]\n"
    "ds_read_b64_tr_b16 v[252:253], v176 offset:28680\n"
    "v_fmac_f32 v16, v189, v128\n"
    "v_mfma_f32_16x16x32_bf16 v[0:3], v[200:203], a[72:75], v[0:3]\n"
    "ds_read_b64_tr_b16 v[254:255], v176 offset:28936\n"
    "v_fmac_f32 v17, v189, v129\n"
    "v_mfma_f32_16x16x32_bf16 v[0:3], v[196:199], a[68:71], v[0:3]\n"
    "buffer_load_dwordx4 v172, s[12:15], s16 offen offset:1024 sc1 lds\n"
    "v_fmac_f32 v18, v189, v130\n"
    "v_mfma_f32_16x16x32_bf16 v[0:3], v[192:195], a[64:67], v[0:3]\n"
    "s_waitcnt lgkmcnt(3)\n"
    "v_fmac_f32 v19, v189, v131\n"
    "v_mul_f32 v16, v185, v16\n"
    "v_mfma_f32_16x16x32_bf16 v[4:7], v[224:227], a[64:67], v[4:7]\n"
    "v_mul_f32 v17, v185, v17\n"
    "v_mul_f32 v18, v185, v18\n"
    "v_mfma_f32_16x16x32_bf16 v[4:7], v[228:231], a[68:71], v[4:7]\n"
    "v_mul_f32 v19, v185, v19\n"
    "v_cvt_pk_bf16_f32 v88, v16, v17\n"
    "v_mfma_f32_16x16x32_bf16 v[4:7], v[232:235], a[72:75], v[4:7]\n"
    "v_cvt_pk_bf16_f32 v89, v18, v19\n"
    "v_accvgpr_write_b32 a8, 0\n"
    "v_mfma_f32_16x16x32_bf16 v[4:7], v[236:239], a[76:79], v[4:7]\n"
    "v_accvgpr_write_b32 a9, 0\n"
    "v_accvgpr_write_b32 a10, 0\n"
    "v_mfma_f32_16x16x32_bf16 v[4:7], v[240:243], a[80:83], v[4:7]\n"
    "v_fmac_f32 v0, v188, v128\n"
    "v_fmac_f32 v1, v188, v129\n"
    "v_mfma_f32_16x16x32_bf16 v[4:7], v[244:247], a[84:87], v[4:7]\n"
    "s_waitcnt lgkmcnt(0)\n"
    "buffer_load_dwordx4 v171, s[12:15], s16 offen offset:2048 sc1 lds\n"
    "v_fmac_f32 v2, v188, v130\n"
    "v_mfma_f32_16x16x32_bf16 v[4:7], v[248:251], a[88:91], v[4:7]\n"
    "v_fmac_f32 v3, v188, v131\n"
    "v_mul_f32 v0, v184, v0\n"
    "v_mfma_f32_16x16x32_bf16 v[4:7], v[252:255], a[92:95], v[4:7]\n"
    "v_mul_f32 v1, v184, v1\n"
    "v_mul_f32 v2, v184, v2\n"
    "v_mfma_f32_16x16x32_bf16 v[20:23], v[252:255], a[124:127], v[20:23]\n"
    "v_mul_f32 v3, v184, v3\n"
    "v_accvgpr_write_b32 a11, 0\n"
    "v_mfma_f32_16x16x32_bf16 v[20:23], v[248:251], a[120:123], v[20:23]\n"
    "v_cvt_pk_bf16_f32 v72, v0, v1\n"
    "v_cvt_pk_bf16_f32 v73, v2, v3\n"
    "v_mfma_f32_16x16x32_bf16 v[20:23], v[244:247], a[116:119], v[20:23]\n"
    "v_accvgpr_write_b32 a12, 0\n"
    "v_accvgpr_write_b32 a13, 0\n"
    "v_mfma_f32_16x16x32_bf16 v[20:23], v[240:243], a[112:115], v[20:23]\n"
    "ds_read_b64_tr_b16 v[192:193], v176 offset:512\n"
    "v_accvgpr_write_b32 a14, 0\n"
    "v_mfma_f32_16x16x32_bf16 v[20:23], v[236:239], a[108:111], v[20:23]\n"
    "v_fmac_f32 v4, v188, v132\n"
    "v_fmac_f32 v5, v188, v133\n"
    "v_mfma_f32_16x16x32_bf16 v[20:23], v[232:235], a[104:107], v[20:23]\n"
    "v_fmac_f32 v6, v188, v134\n"
    "v_fmac_f32 v7, v188, v135\n"
    "v_mfma_f32_16x16x32_bf16 v[20:23], v[228:231], a[100:103], v[20:23]\n"
    "ds_read_b64_tr_b16 v[194:195], v176 offset:768\n"
    "v_mul_f32 v4, v184, v4\n"
    "v_mfma_f32_16x16x32_bf16 v[20:23], v[224:227], a[96:99], v[20:23]\n"
    "buffer_load_dwordx4 v172, s[12:15], s16 offen offset:3072 sc1 lds\n"
    "v_mul_f32 v5, v184, v5\n"
    "v_mfma_f32_16x16x32_bf16 v[36:39], v[224:227], a[192:195], v[36:39]\n"
    "v_mul_f32 v6, v184, v6\n"
    "v_mul_f32 v7, v184, v7\n"
    "v_mfma_f32_16x16x32_bf16 v[36:39], v[228:231], a[196:199], v[36:39]\n"
    "ds_read_b64_tr_b16 v[196:197], v176 offset:4608\n"
    "v_accvgpr_write_b32 a15, 0\n"
    "v_mfma_f32_16x16x32_bf16 v[36:39], v[232:235], a[200:203], v[36:39]\n"
    "ds_read_b64_tr_b16 v[198:199], v176 offset:4864\n"
    "v_cvt_pk_bf16_f32 v74, v4, v5\n"
    "v_mfma_f32_16x16x32_bf16 v[36:39], v[236:239], a[204:207], v[36:39]\n"
    "ds_read_b128 v[128:131], v178 offset:640\n"
    "v_cvt_pk_bf16_f32 v75, v6, v7\n"
    "v_mfma_f32_16x16x32_bf16 v[36:39], v[240:243], a[208:211], v[36:39]\n"
    "ds_read_b64_tr_b16 v[200:201], v176 offset:8704\n"
    "v_accvgpr_write_b32 a16, 0\n"
    "v_mfma_f32_16x16x32_bf16 v[36:39], v[244:247], a[212:215], v[36:39]\n"
    "ds_read_b64_tr_b16 v[202:203], v176 offset:8960\n"
    "v_accvgpr_write_b32 a17, 0\n"
    "v_mfma_f32_16x16x32_bf16 v[36:39], v[248:251], a[216:219], v[36:39]\n"
    "ds_read_b64_tr_b16 v[204:205], v176 offset:12800\n"
    "v_accvgpr_write_b32 a18, 0\n"
    "v_mfma_f32_16x16x32_bf16 v[36:39], v[252:255], a[220:223], v[36:39]\n"
    "ds_read_b64_tr_b16 v[206:207], v176 offset:13056\n"
    "v_fmac_f32 v20, v189, v132\n"
    "v_mfma_f32_16x16x32_bf16 v[52:55], v[252:255], a[252:255], v[52:55]\n"
    "ds_read_b64_tr_b16 v[208:209], v176 offset:16896\n"
    "v_fmac_f32 v21, v189, v133\n"
    "v_mfma_f32_16x16x32_bf16 v[52:55], v[248:251], a[248:251], v[52:55]\n"
    "s_xor_b32 s17, s17, s18\n"
    "ds_read_b64_tr_b16 v[210:211], v176 offset:17152\n"
    "v_fmac_f32 v22, v189, v134\n"
    "v_mfma_f32_16x16x32_bf16 v[52:55], v[244:247], a[244:247], v[52:55]\n"
    "s_add_u32 s16, s16, 16384\n"
    "ds_read_b64_tr_b16 v[212:213], v176 offset:20992\n"
    "v_fmac_f32 v23, v189, v135\n"
    "v_mfma_f32_16x16x32_bf16 v[52:55], v[240:243], a[240:243], v[52:55]\n"
    "ds_read_b64_tr_b16 v[214:215], v176 offset:21248\n"
    "v_accvgpr_write_b32 a19, 0\n"
    "v_mfma_f32_16x16x32_bf16 v[52:55], v[236:239], a[236:239], v[52:55]\n"
    "ds_read_b64_tr_b16 v[216:217], v176 offset:25088\n"
    "v_mul_f32 v20, v185, v20\n"
    "v_mfma_f32_16x16x32_bf16 v[52:55], v[232:235], a[232:235], v[52:55]\n"
    "ds_read_b64_tr_b16 v[218:219], v176 offset:25344\n"
    "v_mul_f32 v21, v185, v21\n"
    "v_mfma_f32_16x16x32_bf16 v[52:55], v[228:231], a[228:231], v[52:55]\n"
    "ds_read_b64_tr_b16 v[220:221], v176 offset:29184\n"
    "ds_read_b64_tr_b16 v[222:223], v176 offset:29440\n"
    "v_mfma_f32_16x16x32_bf16 v[52:55], v[224:227], a[224:227], v[52:55]\n"
    "s_waitcnt lgkmcnt(6)\n"
    "v_mul_f32 v22, v185, v22\n"
    "v_mul_f32 v23, v185, v23\n"
    "v_mfma_f32_16x16x32_bf16 v[56:59], v[192:195], a[224:227], v[56:59]\n"
    "v_accvgpr_write_b32 a20, 0\n"
    "v_cvt_pk_bf16_f32 v90, v20, v21\n"
    "v_mfma_f32_16x16x32_bf16 v[56:59], v[196:199], a[228:231], v[56:59]\n"
    "v_cvt_pk_bf16_f32 v91, v22, v23\n"
    "v_accvgpr_write_b32 a21, 0\n"
    "v_mfma_f32_16x16x32_bf16 v[56:59], v[200:203], a[232:235], v[56:59]\n"
    "v_accvgpr_write_b32 a22, 0\n"
    "v_fmac_f32 v36, v190, v132\n"
    "v_mfma_f32_16x16x32_bf16 v[56:59], v[204:207], a[236:239], v[56:59]\n"
    "v_fmac_f32 v37, v190, v133\n"
    "v_fmac_f32 v38, v190, v134\n"
    "v_mfma_f32_16x16x32_bf16 v[56:59], v[208:211], a[240:243], v[56:59]\n"
    "s_waitcnt lgkmcnt(0)\n"
    "v_fmac_f32 v39, v190, v135\n"
    "v_accvgpr_write_b32 a23, 0\n"
    "v_mfma_f32_16x16x32_bf16 v[56:59], v[212:215], a[244:247], v[56:59]\n"
    "v_mul_f32 v36, v186, v36\n"
    "v_mul_f32 v37, v186, v37\n"
    "v_mfma_f32_16x16x32_bf16 v[56:59], v[216:219], a[248:251], v[56:59]\n"
    "v_mul_f32 v38, v186, v38\n"
    "v_mul_f32 v39, v186, v39\n"
    "v_mfma_f32_16x16x32_bf16 v[56:59], v[220:223], a[252:255], v[56:59]\n"
    "v_accvgpr_write_b32 a24, 0\n"
    "v_accvgpr_write_b32 a25, 0\n"
    "v_mfma_f32_16x16x32_bf16 v[40:43], v[220:223], a[220:223], v[40:43]\n"
    "v_cvt_pk_bf16_f32 v106, v36, v37\n"
    "v_cvt_pk_bf16_f32 v107, v38, v39\n"
    "v_mfma_f32_16x16x32_bf16 v[40:43], v[216:219], a[216:219], v[40:43]\n"
    "v_accvgpr_write_b32 a26, 0\n"
    "v_accvgpr_write_b32 a27, 0\n"
    "v_mfma_f32_16x16x32_bf16 v[40:43], v[212:215], a[212:215], v[40:43]\n"
    "v_fmac_f32 v52, v191, v132\n"
    "v_fmac_f32 v53, v191, v133\n"
    "v_mfma_f32_16x16x32_bf16 v[40:43], v[208:211], a[208:211], v[40:43]\n"
    "v_fmac_f32 v54, v191, v134\n"
    "v_fmac_f32 v55, v191, v135\n"
    "v_mfma_f32_16x16x32_bf16 v[40:43], v[204:207], a[204:207], v[40:43]\n"
    "v_accvgpr_write_b32 a28, 0\n"
    "v_mul_f32 v52, v187, v52\n"
    "v_mfma_f32_16x16x32_bf16 v[40:43], v[200:203], a[200:203], v[40:43]\n"
    "v_mul_f32 v53, v187, v53\n"
    "v_mul_f32 v54, v187, v54\n"
    "v_mfma_f32_16x16x32_bf16 v[40:43], v[196:199], a[196:199], v[40:43]\n"
    "v_mul_f32 v55, v187, v55\n"
    "v_accvgpr_write_b32 a29, 0\n"
    "v_mfma_f32_16x16x32_bf16 v[40:43], v[192:195], a[192:195], v[40:43]\n"
    "v_cvt_pk_bf16_f32 v122, v52, v53\n"
    "v_cvt_pk_bf16_f32 v123, v54, v55\n"
    "v_mfma_f32_16x16x32_bf16 v[24:27], v[192:195], a[96:99], v[24:27]\n"
    "v_accvgpr_write_b32 a30, 0\n"
    "v_accvgpr_write_b32 a31, 0\n"
    "v_mfma_f32_16x16x32_bf16 v[24:27], v[196:199], a[100:103], v[24:27]\n"
    "v_fmac_f32 v56, v191, v128\n"
    "v_fmac_f32 v57, v191, v129\n"
    "v_mfma_f32_16x16x32_bf16 v[24:27], v[200:203], a[104:107], v[24:27]\n"
    "v_fmac_f32 v58, v191, v130\n"
    "v_fmac_f32 v59, v191, v131\n"
    "v_mfma_f32_16x16x32_bf16 v[24:27], v[204:207], a[108:111], v[24:27]\n"
    "v_accvgpr_write_b32 a32, 0\n"
    "v_accvgpr_write_b32 a33, 0\n"
    "v_mfma_f32_16x16x32_bf16 v[24:27], v[208:211], a[112:115], v[24:27]\n"
    "ds_read_b64_tr_b16 v[224:225], v176 offset:520\n"
    "v_mul_f32 v56, v187, v56\n"
    "v_mfma_f32_16x16x32_bf16 v[24:27], v[212:215], a[116:119], v[24:27]\n"
    "ds_read_b64_tr_b16 v[226:227], v176 offset:776\n"
    "v_mul_f32 v57, v187, v57\n"
    "v_mfma_f32_16x16x32_bf16 v[24:27], v[216:219], a[120:123], v[24:27]\n"
    "v_mul_f32 v58, v187, v58\n"
    "v_mul_f32 v59, v187, v59\n"
    "v_mfma_f32_16x16x32_bf16 v[24:27], v[220:223], a[124:127], v[24:27]\n"
    "ds_read_b64_tr_b16 v[228:229], v176 offset:4616\n"
    "v_accvgpr_write_b32 a34, 0\n"
    "v_mfma_f32_16x16x32_bf16 v[8:11], v[220:223], a[92:95], v[8:11]\n"
    "ds_read_b64_tr_b16 v[230:231], v176 offset:4872\n"
    "ds_read_b128 v[132:135], v178 offset:704\n"
    "v_mfma_f32_16x16x32_bf16 v[8:11], v[216:219], a[88:91], v[8:11]\n"
    "ds_read_b64_tr_b16 v[232:233], v176 offset:8712\n"
    "ds_read_b64_tr_b16 v[234:235], v176 offset:8968\n"
    "v_cvt_pk_bf16_f32 v124, v56, v57\n"
    "v_cvt_pk_bf16_f32 v125, v58, v59\n"
    "v_mfma_f32_16x16x32_bf16 v[8:11], v[212:215], a[84:87], v[8:11]\n"
    "ds_read_b64_tr_b16 v[236:237], v176 offset:12808\n"
    "ds_read_b64_tr_b16 v[238:239], v176 offset:13064\n"
    "v_accvgpr_write_b32 a35, 0\n"
    "v_mfma_f32_16x16x32_bf16 v[8:11], v[208:211], a[80:83], v[8:11]\n"
    "ds_read_b64_tr_b16 v[240:241], v176 offset:16904\n"
    "ds_read_b64_tr_b16 v[242:243], v176 offset:17160\n"
    "v_mfma_f32_16x16x32_bf16 v[8:11], v[204:207], a[76:79], v[8:11]\n"
    "ds_read_b64_tr_b16 v[244:245], v176 offset:21000\n"
    "ds_read_b64_tr_b16 v[246:247], v176 offset:21256\n"
    "v_accvgpr_write_b32 a36, 0\n"
    "v_mfma_f32_16x16x32_bf16 v[8:11], v[200:203], a[72:75], v[8:11]\n"
    "ds_read_b64_tr_b16 v[248:249], v176 offset:25096\n"
    "ds_read_b64_tr_b16 v[250:251], v176 offset:25352\n"
    "v_fmac_f32 v40, v190, v128\n"
    "v_fmac_f32 v41, v190, v129\n"
    "v_fmac_f32 v42, v190, v130\n"
    "v_mfma_f32_16x16x32_bf16 v[8:11], v[196:199], a[68:71], v[8:11]\n"
    "ds_read_b64_tr_b16 v[252:253], v176 offset:29192\n"
    "ds_read_b64_tr_b16 v[254:255], v176 offset:29448\n"
    "v_fmac_f32 v43, v190, v131\n"
    "v_accvgpr_write_b32 a37, 0\n"
    "v_mfma_f32_16x16x32_bf16 v[8:11], v[192:195], a[64:67], v[8:11]\n"
    "s_waitcnt lgkmcnt(10)\n"
    "v_mul_f32 v40, v186, v40\n"
    "v_mul_f32 v41, v186, v41\n"
    "v_mul_f32 v42, v186, v42\n"
    "v_mul_f32 v43, v186, v43\n"
    "v_accvgpr_write_b32 a38, 0\n"
    "v_mfma_f32_16x16x32_bf16 v[12:15], v[224:227], a[64:67], v[12:15]\n"
    "v_cvt_pk_bf16_f32 v108, v40, v41\n"
    "v_cvt_pk_bf16_f32 v109, v42, v43\n"
    "v_mfma_f32_16x16x32_bf16 v[12:15], v[228:231], a[68:71], v[12:15]\n"
    "v_accvgpr_write_b32 a39, 0\n"
    "v_accvgpr_write_b32 a40, 0\n"
    "v_mfma_f32_16x16x32_bf16 v[12:15], v[232:235], a[72:75], v[12:15]\n"
    "s_waitcnt lgkmcnt(4)\n"
    "v_accvgpr_write_b32 a41, 0\n"
    "v_fmac_f32 v24, v189, v128\n"
    "v_mfma_f32_16x16x32_bf16 v[12:15], v[236:239], a[76:79], v[12:15]\n"
    "v_fmac_f32 v25, v189, v129\n"
    "v_fmac_f32 v26, v189, v130\n"
    "v_mfma_f32_16x16x32_bf16 v[12:15], v[240:243], a[80:83], v[12:15]\n"
    "v_fmac_f32 v27, v189, v131\n"
    "v_accvgpr_write_b32 a42, 0\n"
    "v_mfma_f32_16x16x32_bf16 v[12:15], v[244:247], a[84:87], v[12:15]\n"
    "s_waitcnt vmcnt(16) lgkmcnt(0)\n"
    "v_mul_f32 v24, v185, v24\n"
    "v_mul_f32 v25, v185, v25\n"
    "v_mfma_f32_16x16x32_bf16 v[12:15], v[248:251], a[88:91], v[12:15]\n"
    "s_barrier\n"
    "v_mul_f32 v26, v185, v26\n"
    "v_mul_f32 v27, v185, v27\n"
    "v_mfma_f32_16x16x32_bf16 v[12:15], v[252:255], a[92:95], v[12:15]\n"
    "ds_read_b128 a[128:131], v164 offset:0\n"
    "v_accvgpr_write_b32 a43, 0\n"
    "v_mfma_f32_16x16x32_bf16 v[28:31], v[252:255], a[124:127], v[28:31]\n"
    "v_cvt_pk_bf16_f32 v92, v24, v25\n"
    "v_cvt_pk_bf16_f32 v93, v26, v27\n"
    "v_mfma_f32_16x16x32_bf16 v[28:31], v[248:251], a[120:123], v[28:31]\n"
    "ds_read_b128 a[132:135], v164 offset:1024\n"
    "v_accvgpr_write_b32 a44, 0\n"
    "v_mfma_f32_16x16x32_bf16 v[28:31], v[244:247], a[116:119], v[28:31]\n"
    "ds_read_b128 a[136:139], v164 offset:2048\n"
    "v_accvgpr_write_b32 a45, 0\n"
    "v_mfma_f32_16x16x32_bf16 v[28:31], v[240:243], a[112:115], v[28:31]\n"
    "ds_read_b128 a[140:143], v164 offset:3072\n"
    "v_fmac_f32 v8, v188, v128\n"
    "v_mfma_f32_16x16x32_bf16 v[28:31], v[236:239], a[108:111], v[28:31]\n"
    "v_fmac_f32 v9, v188, v129\n"
    "v_fmac_f32 v10, v188, v130\n"
    "v_mfma_f32_16x16x32_bf16 v[28:31], v[232:235], a[104:107], v[28:31]\n"
    "v_fmac_f32 v11, v188, v131\n"
    "v_accvgpr_write_b32 a46, 0\n"
    "v_mfma_f32_16x16x32_bf16 v[28:31], v[228:231], a[100:103], v[28:31]\n"
    "ds_read_b128 a[144:147], v164 offset:4096\n"
    "v_mul_f32 v8, v184, v8\n"
    "v_mfma_f32_16x16x32_bf16 v[28:31], v[224:227], a[96:99], v[28:31]\n"
    "ds_read_b128 a[148:151], v164 offset:5120\n"
    "v_mul_f32 v9, v184, v9\n"
    "v_mul_f32 v10, v184, v10\n"
    "v_mul_f32 v11, v184, v11\n"
    "v_accvgpr_write_b32 a47, 0\n"
    "v_mfma_f32_16x16x32_bf16 v[44:47], v[224:227], a[192:195], v[44:47]\n"
    "v_cvt_pk_bf16_f32 v76, v8, v9\n"
    "v_cvt_pk_bf16_f32 v77, v10, v11\n"
    "v_mfma_f32_16x16x32_bf16 v[44:47], v[228:231], a[196:199], v[44:47]\n"
    "ds_read_b128 a[152:155], v164 offset:6144\n"
    "v_accvgpr_write_b32 a48, 0\n"
    "v_mfma_f32_16x16x32_bf16 v[44:47], v[232:235], a[200:203], v[44:47]\n"
    "ds_read_b128 a[156:159], v164 offset:7168\n"
    "v_accvgpr_write_b32 a49, 0\n"
    "v_mfma_f32_16x16x32_bf16 v[44:47], v[236:239], a[204:207], v[44:47]\n"
    "ds_read_b128 a[160:163], v164 offset:8192\n"
    "v_accvgpr_write_b32 a50, 0\n"
    "v_mfma_f32_16x16x32_bf16 v[44:47], v[240:243], a[208:211], v[44:47]\n"
    "v_fmac_f32 v12, v188, v132\n"
    "v_fmac_f32 v13, v188, v133\n"
    "v_mfma_f32_16x16x32_bf16 v[44:47], v[244:247], a[212:215], v[44:47]\n"
    "ds_read_b128 a[164:167], v164 offset:9216\n"
    "v_fmac_f32 v14, v188, v134\n"
    "v_fmac_f32 v15, v188, v135\n"
    "v_mfma_f32_16x16x32_bf16 v[44:47], v[248:251], a[216:219], v[44:47]\n"
    "v_accvgpr_write_b32 a51, 0\n"
    "v_mul_f32 v12, v184, v12\n"
    "v_mfma_f32_16x16x32_bf16 v[44:47], v[252:255], a[220:223], v[44:47]\n"
    "ds_read_b128 a[168:171], v164 offset:10240\n"
    "v_mul_f32 v13, v184, v13\n"
    "v_mul_f32 v14, v184, v14\n"
    "v_mul_f32 v15, v184, v15\n"
    "v_mfma_f32_16x16x32_bf16 v[60:63], v[252:255], a[252:255], v[60:63]\n"
    "ds_read_b128 a[172:175], v164 offset:11264\n"
    "v_accvgpr_write_b32 a52, 0\n"
    "v_mfma_f32_16x16x32_bf16 v[60:63], v[248:251], a[248:251], v[60:63]\n"
    "v_cvt_pk_bf16_f32 v78, v12, v13\n"
    "v_cvt_pk_bf16_f32 v79, v14, v15\n"
    "v_accvgpr_write_b32 a53, 0\n"
    "v_mfma_f32_16x16x32_bf16 v[60:63], v[244:247], a[244:247], v[60:63]\n"
    "ds_read_b128 a[176:179], v164 offset:12288\n"
    "v_accvgpr_write_b32 a54, 0\n"
    "v_mfma_f32_16x16x32_bf16 v[60:63], v[240:243], a[240:243], v[60:63]\n"
    "ds_read_b128 a[180:183], v164 offset:13312\n"
    "v_fmac_f32 v28, v189, v132\n"
    "v_mfma_f32_16x16x32_bf16 v[60:63], v[236:239], a[236:239], v[60:63]\n"
    "v_fmac_f32 v29, v189, v133\n"
    "v_fmac_f32 v30, v189, v134\n"
    "v_mfma_f32_16x16x32_bf16 v[60:63], v[232:235], a[232:235], v[60:63]\n"
    "ds_read_b128 a[184:187], v164 offset:14336\n"
    "v_fmac_f32 v31, v189, v135\n"
    "v_accvgpr_write_b32 a55, 0\n"
    "v_mfma_f32_16x16x32_bf16 v[60:63], v[228:231], a[228:231], v[60:63]\n"
    "ds_read_b128 a[188:191], v164 offset:15360\n"
    "v_mul_f32 v28, v185, v28\n"
    "v_mfma_f32_16x16x32_bf16 v[60:63], v[224:227], a[224:227], v[60:63]\n"
    "s_waitcnt lgkmcnt(4)\n"
    "v_mul_f32 v29, v185, v29\n"
    "v_mul_f32 v30, v185, v30\n"
    "v_mul_f32 v31, v185, v31\n"
    "v_accvgpr_write_b32 a56, 0\n"
    "v_mfma_f32_16x16x32_bf16 v[0:3], a[128:131], v[64:67], 0\n"
    "v_accvgpr_write_b32 a57, 0\n"
    "v_cvt_pk_bf16_f32 v94, v28, v29\n"
    "v_mfma_f32_16x16x32_bf16 v[0:3], a[132:135], v[68:71], v[0:3]\n"
    "v_cvt_pk_bf16_f32 v95, v30, v31\n"
    "v_accvgpr_write_b32 a58, 0\n"
    "v_mfma_f32_16x16x32_bf16 v[0:3], a[136:139], v[72:75], v[0:3]\n"
    "v_fmac_f32 v44, v190, v132\n"
    "v_fmac_f32 v45, v190, v133\n"
    "v_mfma_f32_16x16x32_bf16 v[0:3], a[140:143], v[76:79], v[0:3]\n"
    "v_fmac_f32 v46, v190, v134\n"
    "v_fmac_f32 v47, v190, v135\n"
    "v_mfma_f32_16x16x32_bf16 v[16:19], a[128:131], v[80:83], 0\n"
    "v_accvgpr_write_b32 a59, 0\n"
    "v_mul_f32 v44, v186, v44\n"
    "v_mfma_f32_16x16x32_bf16 v[16:19], a[132:135], v[84:87], v[16:19]\n"
    "v_mul_f32 v45, v186, v45\n"
    "v_mul_f32 v46, v186, v46\n"
    "v_mul_f32 v47, v186, v47\n"
    "v_mfma_f32_16x16x32_bf16 v[16:19], a[136:139], v[88:91], v[16:19]\n"
    "v_accvgpr_write_b32 a60, 0\n"
    "v_cvt_pk_bf16_f32 v110, v44, v45\n"
    "v_mfma_f32_16x16x32_bf16 v[16:19], a[140:143], v[92:95], v[16:19]\n"
    "v_max3_f32 v184, v0, v1, v2\n"
    "v_max_f32 v184, v184, v3\n"
    "v_mov_b32 v185, v184\n"
    "v_cvt_pk_bf16_f32 v111, v46, v47\n"
    "v_fmac_f32 v60, v191, v132\n"
    "v_mfma_f32_16x16x32_bf16 v[32:35], a[128:131], v[96:99], 0\n"
    "v_fmac_f32 v61, v191, v133\n"
    "v_fmac_f32 v62, v191, v134\n"
    "v_fmac_f32 v63, v191, v135\n"
    "v_accvgpr_write_b32 a61, 0\n"
    "v_permlane16_swap_b32 v184, v185\n"
    "v_mfma_f32_16x16x32_bf16 v[32:35], a[132:135], v[100:103], v[32:35]\n"
    "v_max_f32 v184, v184, v185\n"
    "v_mul_f32 v60, v187, v60\n"
    "v_mul_f32 v61, v187, v61\n"
    "v_mul_f32 v62, v187, v62\n"
    "v_mul_f32 v63, v187, v63\n"
    "v_mfma_f32_16x16x32_bf16 v[32:35], a[136:139], v[104:107], v[32:35]\n"
    "v_accvgpr_write_b32 a62, 0\n"
    "v_mov_b32 v185, v184\n"
    "v_mfma_f32_16x16x32_bf16 v[32:35], a[140:143], v[108:111], v[32:35]\n"
    "v_max3_f32 v186, v16, v17, v18\n"
    "v_max_f32 v186, v186, v19\n"
    "v_permlane32_swap_b32 v184, v185\n"
    "v_mov_b32 v187, v186\n"
    "v_cvt_pk_bf16_f32 v126, v60, v61\n"
    "v_mfma_f32_16x16x32_bf16 v[48:51], a[128:131], v[112:115], 0\n"
    "v_cvt_pk_bf16_f32 v127, v62, v63\n"
    "v_accvgpr_write_b32 a63, 0\n"
    "v_permlane16_swap_b32 v186, v187\n"
    "v_min_f32 v128, -v184, -v185\n"
    "v_min_f32 v129, -v184, -v185\n"
    "v_mfma_f32_16x16x32_bf16 v[48:51], a[132:135], v[116:119], v[48:51]\n"
    "v_min_f32 v130, -v184, -v185\n"
    "v_min_f32 v131, -v184, -v185\n"
    "v_max_f32 v186, v186, v187\n"
    "v_mov_b32 v187, v186\n"
    "v_mfma_f32_16x16x32_bf16 v[48:51], a[136:139], v[120:123], v[48:51]\n"
    "s_nop 0\n"
    "v_permlane32_swap_b32 v186, v187\n"
    "v_min_f32 v132, -v186, -v187\n"
    "v_mfma_f32_16x16x32_bf16 v[48:51], a[140:143], v[124:127], v[48:51]\n"
    "v_min_f32 v133, -v186, -v187\n"
    "v_min_f32 v134, -v186, -v187\n"
    "v_mfma_f32_16x16x32_bf16 v[0:3], a[128:131], v[64:67], v[128:131]\n"
    "ds_read_b64_tr_b16 a[192:193], v165 offset:16384\n"
    "v_min_f32 v135, -v186, -v187\n"
    "v_mfma_f32_16x16x32_bf16 v[0:3], a[132:135], v[68:71], v[0:3]\n"
    "ds_read_b64_tr_b16 a[194:195], v165 offset:20480\n"
    "v_max3_f32 v188, v32, v33, v34\n"
    "v_mfma_f32_16x16x32_bf16 v[0:3], a[136:139], v[72:75], v[0:3]\n"
    "ds_read_b64_tr_b16 a[196:197], v165 offset:24576\n"
    "v_max_f32 v188, v188, v35\n"
    "v_mfma_f32_16x16x32_bf16 v[0:3], a[140:143], v[76:79], v[0:3]\n"
    "ds_read_b64_tr_b16 a[198:199], v165 offset:28672\n"
    "v_max3_f32 v190, v48, v49, v50\n"
    "v_mfma_f32_16x16x32_bf16 v[16:19], a[140:143], v[92:95], v[132:135]\n"
    "ds_read_b64_tr_b16 a[200:201], v166 offset:16384\n"
    "v_max_f32 v190, v190, v51\n"
    "v_mfma_f32_16x16x32_bf16 v[16:19], a[136:139], v[88:91], v[16:19]\n"
    "ds_read_b64_tr_b16 a[202:203], v166 offset:20480\n"
    "v_mov_b32 v189, v188\n"
    "v_mfma_f32_16x16x32_bf16 v[16:19], a[132:135], v[84:87], v[16:19]\n"
    "ds_read_b64_tr_b16 a[204:205], v166 offset:24576\n"
    "v_mov_b32 v191, v190\n"
    "v_mfma_f32_16x16x32_bf16 v[16:19], a[128:131], v[80:83], v[16:19]\n"
    "ds_read_b64_tr_b16 a[206:207], v166 offset:28672\n"
    "v_permlane16_swap_b32 v188, v189\n"
    "v_mfma_f32_16x16x32_bf16 v[20:23], a[144:147], v[80:83], v[132:135]\n"
    "ds_read_b64_tr_b16 a[208:209], v165 offset:16896\n"
    "v_permlane16_swap_b32 v190, v191\n"
    "v_mfma_f32_16x16x32_bf16 v[20:23], a[148:151], v[84:87], v[20:23]\n"
    "ds_read_b64_tr_b16 a[210:211], v165 offset:20992\n"
    "v_max_f32 v188, v188, v189\n"
    "v_mfma_f32_16x16x32_bf16 v[20:23], a[152:155], v[88:91], v[20:23]\n"
    "ds_read_b64_tr_b16 a[212:213], v165 offset:25088\n"
    "v_max_f32 v190, v190, v191\n"
    "v_mfma_f32_16x16x32_bf16 v[20:23], a[156:159], v[92:95], v[20:23]\n"
    "ds_read_b64_tr_b16 a[214:215], v165 offset:29184\n"
    "v_mov_b32 v189, v188\n"
    "v_mfma_f32_16x16x32_bf16 v[4:7], a[156:159], v[76:79], v[128:131]\n"
    "ds_read_b64_tr_b16 a[216:217], v166 offset:16896\n"
    "v_mov_b32 v191, v190\n"
    "v_mfma_f32_16x16x32_bf16 v[4:7], a[152:155], v[72:75], v[4:7]\n"
    "ds_read_b64_tr_b16 a[218:219], v166 offset:20992\n"
    "v_permlane32_swap_b32 v188, v189\n"
    "v_mfma_f32_16x16x32_bf16 v[4:7], a[148:151], v[68:71], v[4:7]\n"
    "v_exp_f32 v0, v0\n"
    "v_mfma_f32_16x16x32_bf16 v[4:7], a[144:147], v[64:67], v[4:7]\n"
    "v_exp_f32 v1, v1\n"
    "v_mfma_f32_16x16x32_bf16 v[8:11], a[160:163], v[64:67], v[128:131]\n"
    "v_exp_f32 v2, v2\n"
    "v_mfma_f32_16x16x32_bf16 v[8:11], a[164:167], v[68:71], v[8:11]\n"
    "v_exp_f32 v3, v3\n"
    "v_mfma_f32_16x16x32_bf16 v[8:11], a[168:171], v[72:75], v[8:11]\n"
    "v_exp_f32 v16, v16\n"
    "v_mfma_f32_16x16x32_bf16 v[8:11], a[172:175], v[76:79], v[8:11]\n"
    "v_exp_f32 v17, v17\n"
    "v_mfma_f32_16x16x32_bf16 v[24:27], a[172:175], v[92:95], v[132:135]\n"
    "v_exp_f32 v18, v18\n"
    "v_mfma_f32_16x16x32_bf16 v[24:27], a[168:171], v[88:91], v[24:27]\n"
    "v_exp_f32 v19, v19\n"
    "v_mfma_f32_16x16x32_bf16 v[24:27], a[164:167], v[84:87], v[24:27]\n"
    "v_exp_f32 v20, v20\n"
    "v_mfma_f32_16x16x32_bf16 v[24:27], a[160:163], v[80:83], v[24:27]\n"
    "s_waitcnt lgkmcnt(0)\n"
    "v_exp_f32 v21, v21\n"
    "v_mfma_f32_16x16x32_bf16 v[28:31], a[176:179], v[80:83], v[132:135]\n"
    "v_exp_f32 v22, v22\n"
    "v_mfma_f32_16x16x32_bf16 v[28:31], a[180:183], v[84:87], v[28:31]\n"
    "v_exp_f32 v23, v23\n"
    "v_mfma_f32_16x16x32_bf16 v[28:31], a[184:187], v[88:91], v[28:31]\n"
    "v_exp_f32 v4, v4\n"
    "v_mfma_f32_16x16x32_bf16 v[28:31], a[188:191], v[92:95], v[28:31]\n"
    "v_exp_f32 v5, v5\n"
    "v_mfma_f32_16x16x32_bf16 v[12:15], a[188:191], v[76:79], v[128:131]\n"
    "v_exp_f32 v6, v6\n"
    "v_mfma_f32_16x16x32_bf16 v[12:15], a[184:187], v[72:75], v[12:15]\n"
    "v_exp_f32 v7, v7\n"
    "v_mfma_f32_16x16x32_bf16 v[12:15], a[180:183], v[68:71], v[12:15]\n"
    "v_exp_f32 v8, v8\n"
    "v_mfma_f32_16x16x32_bf16 v[12:15], a[176:179], v[64:67], v[12:15]\n"
    "v_exp_f32 v9, v9\n"
    "v_mfma_f32_16x16x32_bf16 a[64:67], v[144:147], v[144:147], 0\n"
    "v_exp_f32 v10, v10\n"
    "v_mfma_f32_16x16x32_bf16 a[68:71], v[144:147], v[144:147], 0\n"
    "v_exp_f32 v11, v11\n"
    "v_mfma_f32_16x16x32_bf16 a[72:75], v[144:147], v[144:147], 0\n"
    "v_exp_f32 v24, v24\n"
    "v_mfma_f32_16x16x32_bf16 a[76:79], v[144:147], v[144:147], 0\n"
    "v_exp_f32 v25, v25\n"
    "v_mfma_f32_16x16x32_bf16 a[80:83], v[144:147], v[144:147], 0\n"
    "v_exp_f32 v26, v26\n"
    "v_mfma_f32_16x16x32_bf16 a[84:87], v[144:147], v[144:147], 0\n"
    "v_exp_f32 v27, v27\n"
    "v_exp_f32 v28, v28\n"
    "v_mfma_f32_16x16x32_bf16 a[88:91], v[144:147], v[144:147], 0\n"
    "v_exp_f32 v29, v29\n"
    "v_exp_f32 v30, v30\n"
    "v_mfma_f32_16x16x32_bf16 a[92:95], v[144:147], v[144:147], 0\n"
    "v_permlane32_swap_b32 v190, v191\n"
    "v_min_f32 v136, -v188, -v189\n"
    "v_mfma_f32_16x16x32_bf16 a[96:99], v[144:147], v[144:147], 0\n"
    "v_min_f32 v137, -v188, -v189\n"
    "v_min_f32 v138, -v188, -v189\n"
    "v_mfma_f32_16x16x32_bf16 a[100:103], v[144:147], v[144:147], 0\n"
    "v_min_f32 v139, -v188, -v189\n"
    "v_min_f32 v140, -v190, -v191\n"
    "v_mfma_f32_16x16x32_bf16 a[104:107], v[144:147], v[144:147], 0\n"
    "v_min_f32 v141, -v190, -v191\n"
    "v_min_f32 v142, -v190, -v191\n"
    "v_mfma_f32_16x16x32_bf16 a[108:111], v[144:147], v[144:147], 0\n"
    "v_min_f32 v143, -v190, -v191\n"
    "v_cvt_pk_bf16_f32 v0, v0, v1\n"
    "v_mfma_f32_16x16x32_bf16 a[112:115], v[144:147], v[144:147], 0\n"
    "v_cvt_pk_bf16_f32 v1, v2, v3\n"
    "v_cvt_pk_bf16_f32 v16, v16, v17\n"
    "v_mfma_f32_16x16x32_bf16 a[116:119], v[144:147], v[144:147], 0\n"
    "v_cvt_pk_bf16_f32 v17, v18, v19\n"
    "v_cvt_pk_bf16_f32 v18, v20, v21\n"
    "v_mfma_f32_16x16x32_bf16 a[120:123], v[144:147], v[144:147], 0\n"
    "v_cvt_pk_bf16_f32 v19, v22, v23\n"
    "v_cvt_pk_bf16_f32 v2, v4, v5\n"
    "v_mfma_f32_16x16x32_bf16 a[124:127], v[144:147], v[144:147], 0\n"
    "v_cvt_pk_bf16_f32 v3, v6, v7\n"
    "v_cvt_pk_bf16_f32 v4, v8, v9\n"
    "v_cvt_pk_bf16_f32 v5, v10, v11\n"
    "v_cvt_pk_bf16_f32 v20, v24, v25\n"
    "v_cvt_pk_bf16_f32 v21, v26, v27\n"
    "v_cvt_pk_bf16_f32 v22, v28, v29\n"
    "v_xor_b32 v164, s18, v164\n"
    "s_xor_b32 s18, s18, 98304\n"
    "s_waitcnt lgkmcnt(0)\n"
    "Lent%=:\n"
    "v_mfma_f32_16x16x32_bf16 v[32:35], a[128:131], v[96:99], v[136:139]\n"
    "v_exp_f32 v31, v31\n"
    "v_mfma_f32_16x16x32_bf16 v[32:35], a[132:135], v[100:103], v[32:35]\n"
    "ds_read_b64_tr_b16 a[220:221], v166 offset:25088\n"
    "v_cvt_pk_bf16_f32 v23, v30, v31\n"
    "v_mfma_f32_16x16x32_bf16 v[32:35], a[136:139], v[104:107], v[32:35]\n"
    "v_exp_f32 v12, v12\n"
    "v_mfma_f32_16x16x32_bf16 v[32:35], a[140:143], v[108:111], v[32:35]\n"
    "ds_read_b64_tr_b16 a[222:223], v166 offset:29184\n"
    "v_mfma_f32_16x16x32_bf16 v[48:51], a[140:143], v[124:127], v[140:143]\n"
    "v_exp_f32 v13, v13\n"
    "v_mfma_f32_16x16x32_bf16 v[48:51], a[136:139], v[120:123], v[48:51]\n"
    "ds_read_b64_tr_b16 a[224:225], v165 offset:17408\n"
    "v_cvt_pk_bf16_f32 v6, v12, v13\n"
    "v_mfma_f32_16x16x32_bf16 v[48:51], a[132:135], v[116:119], v[48:51]\n"
    "v_exp_f32 v14, v14\n"
    "v_mfma_f32_16x16x32_bf16 v[48:51], a[128:131], v[112:115], v[48:51]\n"
    "ds_read_b64_tr_b16 a[226:227], v165 offset:21504\n"
    "v_mfma_f32_16x16x32_bf16 v[52:55], a[144:147], v[112:115], v[140:143]\n"
    "v_exp_f32 v15, v15\n"
    "v_mfma_f32_16x16x32_bf16 v[52:55], a[148:151], v[116:119], v[52:55]\n"
    "ds_read_b64_tr_b16 a[228:229], v165 offset:25600\n"
    "v_cvt_pk_bf16_f32 v7, v14, v15\n"
    "v_mfma_f32_16x16x32_bf16 v[52:55], a[152:155], v[120:123], v[52:55]\n"
    ".p2align 6\n"
    "Lloop%=:\n"
    "v_mfma_f32_16x16x32_bf16 v[52:55], a[156:159], v[124:127], v[52:55]\n"
    "ds_read_b64_tr_b16 a[230:231], v165 offset:29696\n"
    "v_mfma_f32_16x16x32_bf16 v[36:39], a[156:159], v[108:111], v[136:139]\n"
    "s_waitcnt vmcnt(8)\n"
    "v_exp_f32 v32, v32\n"
    "v_mfma_f32_16x16x32_bf16 v[36:39], a[152:155], v[104:107], v[36:39]\n"
    "s_barrier\n"
    "ds_read_b64_tr_b16 a[232:233], v166 offset:17408\n"
    "v_mfma_f32_16x16x32_bf16 v[36:39], a[148:151], v[100:103], v[36:39]\n"
    "s_bitcmp1_b32 s33, s20\n"
    "v_exp_f32 v33, v33\n"
    "v_mfma_f32_16x16x32_bf16 v[36:39], a[144:147], v[96:99], v[36:39]\n"
    "s_cbranch_scc1 Lwarm%=\n"
    "Lwarmd%=:\n"
    "ds_read_b64_tr_b16 a[234:235], v166 offset:21504\n"
    "v_cvt_pk_bf16_f32 v32, v32, v33\n"
    "v_mfma_f32_16x16x32_bf16 v[40:43], a[160:163], v[96:99], v[136:139]\n"
    "v_exp_f32 v34, v34\n"
    "v_mfma_f32_16x16x32_bf16 v[40:43], a[164:167], v[100:103], v[40:43]\n"
    "ds_read_b64_tr_b16 a[236:237], v166 offset:25600\n"
    "v_mfma_f32_16x16x32_bf16 v[40:43], a[168:171], v[104:107], v[40:43]\n"
    "v_exp_f32 v35, v35\n"
    "v_mfma_f32_16x16x32_bf16 v[40:43], a[172:175], v[108:111], v[40:43]\n"
    "ds_read_b64_tr_b16 a[238:239], v166 offset:29696\n"
    "v_cvt_pk_bf16_f32 v33, v34, v35\n"
    "v_mfma_f32_16x16x32_bf16 v[56:59], a[172:175], v[124:127], v[140:143]\n"
    "v_exp_f32 v48, v48\n"
    "v_mfma_f32_16x16x32_bf16 v[56:59], a[168:171], v[120:123], v[56:59]\n"
    "ds_read_b64_tr_b16 a[240:241], v165 offset:17920\n"
    "v_mfma_f32_16x16x32_bf16 v[56:59], a[164:167], v[116:119], v[56:59]\n"
    "v_exp_f32 v49, v49\n"
    "v_mfma_f32_16x16x32_bf16 v[56:59], a[160:163], v[112:115], v[56:59]\n"
    "ds_read_b64_tr_b16 a[242:243], v165 offset:22016\n"
    "v_cvt_pk_bf16_f32 v48, v48, v49\n"
    "v_mfma_f32_16x16x32_bf16 v[60:63], a[176:179], v[112:115], v[140:143]\n"
    "v_exp_f32 v50, v50\n"
    "v_mfma_f32_16x16x32_bf16 v[60:63], a[180:183], v[116:119], v[60:63]\n"
    "ds_read_b64_tr_b16 a[244:245], v165 offset:26112\n"
    "v_mfma_f32_16x16x32_bf16 v[60:63], a[184:187], v[120:123], v[60:63]\n"
    "v_exp_f32 v51, v51\n"
    "v_mfma_f32_16x16x32_bf16 v[60:63], a[188:191], v[124:127], v[60:63]\n"
    "ds_read_b64_tr_b16 a[246:247], v165 offset:30208\n"
    "v_cvt_pk_bf16_f32 v49, v50, v51\n"
    "v_mfma_f32_16x16x32_bf16 v[44:47], a[188:191], v[108:111], v[136:139]\n"
    "v_exp_f32 v52, v52\n"
    "v_mfma_f32_16x16x32_bf16 v[44:47], a[184:187], v[104:107], v[44:47]\n"
    "ds_read_b64_tr_b16 a[248:249], v166 offset:17920\n"
    "v_mfma_f32_16x16x32_bf16 v[44:47], a[180:183], v[100:103], v[44:47]\n"
    "v_exp_f32 v53, v53\n"
    "v_mfma_f32_16x16x32_bf16 v[44:47], a[176:179], v[96:99], v[44:47]\n"
    "ds_read_b64_tr_b16 a[250:251], v166 offset:22016\n"
    "v_cvt_pk_bf16_f32 v50, v52, v53\n"
    "v_smfmac_f32_16x16x64_bf16 v[144:147], v[160:163], v[0:7], v183\n"
    "v_exp_f32 v54, v54\n"
    "v_smfmac_f32_16x16x64_bf16 v[148:151], v[160:163], v[16:23], v183\n"
    "ds_read_b64_tr_b16 a[252:253], v166 offset:26112\n"
    "v_mfma_f32_16x16x32_bf16 a[0:3], a[192:195], v[0:3], a[0:3]\n"
    "v_exp_f32 v55, v55\n"
    "v_mfma_f32_16x16x32_bf16 a[0:3], a[196:199], v[4:7], a[0:3]\n"
    "ds_read_b64_tr_b16 a[254:255], v166 offset:30208\n"
    "v_cvt_pk_bf16_f32 v51, v54, v55\n"
    "v_mfma_f32_16x16x32_bf16 a[32:35], a[196:199], v[20:23], a[32:35]\n"
    "v_exp_f32 v36, v36\n"
    "v_mfma_f32_16x16x32_bf16 a[32:35], a[192:195], v[16:19], a[32:35]\n"
    "ds_read_b128 a[128:131], v164 offset:0\n"
    "v_xor_b32 v165, s19, v165\n"
    "v_mfma_f32_16x16x32_bf16 a[36:39], a[200:203], v[16:19], a[36:39]\n"
    "v_exp_f32 v37, v37\n"
    "v_mfma_f32_16x16x32_bf16 a[36:39], a[204:207], v[20:23], a[36:39]\n"
    "ds_read_b128 a[132:135], v164 offset:1024\n"
    "v_cvt_pk_bf16_f32 v34, v36, v37\n"
    "v_mfma_f32_16x16x32_bf16 a[4:7], a[204:207], v[4:7], a[4:7]\n"
    "v_exp_f32 v38, v38\n"
    "v_mfma_f32_16x16x32_bf16 a[4:7], a[200:203], v[0:3], a[4:7]\n"
    "ds_read_b128 a[136:139], v164 offset:2048\n"
    "v_xor_b32 v166, s19, v166\n"
    "v_mfma_f32_16x16x32_bf16 a[8:11], a[208:211], v[0:3], a[8:11]\n"
    "s_waitcnt lgkmcnt(4)\n"
    "s_xor_b32 s19, s19, 98304\n"
    "v_exp_f32 v39, v39\n"
    "v_mfma_f32_16x16x32_bf16 a[8:11], a[212:215], v[4:7], a[8:11]\n"
    "ds_read_b128 a[140:143], v164 offset:3072\n"
    "v_cvt_pk_bf16_f32 v35, v38, v39\n"
    "v_mfma_f32_16x16x32_bf16 a[40:43], a[212:215], v[20:23], a[40:43]\n"
    "v_exp_f32 v40, v40\n"
    "v_mfma_f32_16x16x32_bf16 a[40:43], a[208:211], v[16:19], a[40:43]\n"
    "ds_read_b128 a[144:147], v164 offset:4096\n"
    "v_mfma_f32_16x16x32_bf16 a[44:47], a[216:219], v[16:19], a[44:47]\n"
    "v_exp_f32 v41, v41\n"
    "v_mfma_f32_16x16x32_bf16 a[44:47], a[220:223], v[20:23], a[44:47]\n"
    "ds_read_b128 a[148:151], v164 offset:5120\n"
    "v_cvt_pk_bf16_f32 v36, v40, v41\n"
    "v_mfma_f32_16x16x32_bf16 a[12:15], a[220:223], v[4:7], a[12:15]\n"
    "v_exp_f32 v42, v42\n"
    "v_mfma_f32_16x16x32_bf16 a[12:15], a[216:219], v[0:3], a[12:15]\n"
    "ds_read_b128 a[152:155], v164 offset:6144\n"
    "v_mfma_f32_16x16x32_bf16 a[16:19], a[224:227], v[0:3], a[16:19]\n"
    "v_exp_f32 v43, v43\n"
    "v_mfma_f32_16x16x32_bf16 a[16:19], a[228:231], v[4:7], a[16:19]\n"
    "ds_read_b128 a[156:159], v164 offset:7168\n"
    "v_cvt_pk_bf16_f32 v37, v42, v43\n"
    "v_mfma_f32_16x16x32_bf16 a[48:51], a[228:231], v[20:23], a[48:51]\n"
    "v_exp_f32 v56, v56\n"
    "v_mfma_f32_16x16x32_bf16 a[48:51], a[224:227], v[16:19], a[48:51]\n"
    "ds_read_b128 a[160:163], v164 offset:8192\n"
    "v_mfma_f32_16x16x32_bf16 a[52:55], a[232:235], v[16:19], a[52:55]\n"
    "v_exp_f32 v57, v57\n"
    "v_mfma_f32_16x16x32_bf16 a[52:55], a[236:239], v[20:23], a[52:55]\n"
    "ds_read_b128 a[164:167], v164 offset:9216\n"
    "v_cvt_pk_bf16_f32 v52, v56, v57\n"
    "v_mfma_f32_16x16x32_bf16 a[20:23], a[236:239], v[4:7], a[20:23]\n"
    "v_exp_f32 v58, v58\n"
    "v_mfma_f32_16x16x32_bf16 a[20:23], a[232:235], v[0:3], a[20:23]\n"
    "ds_read_b128 a[168:171], v164 offset:10240\n"
    "v_mfma_f32_16x16x32_bf16 a[24:27], a[240:243], v[0:3], a[24:27]\n"
    "v_exp_f32 v59, v59\n"
    "v_mfma_f32_16x16x32_bf16 a[24:27], a[244:247], v[4:7], a[24:27]\n"
    "ds_read_b128 a[172:175], v164 offset:11264\n"
    "v_cvt_pk_bf16_f32 v53, v58, v59\n"
    "v_mfma_f32_16x16x32_bf16 a[56:59], a[244:247], v[20:23], a[56:59]\n"
    "v_exp_f32 v60, v60\n"
    "v_mfma_f32_16x16x32_bf16 a[56:59], a[240:243], v[16:19], a[56:59]\n"
    "ds_read_b128 a[176:179], v164 offset:12288\n"
    "v_mfma_f32_16x16x32_bf16 a[60:63], a[248:251], v[16:19], a[60:63]\n"
    "s_waitcnt lgkmcnt(4)\n"
    "v_exp_f32 v61, v61\n"
    "v_mfma_f32_16x16x32_bf16 a[60:63], a[252:255], v[20:23], a[60:63]\n"
    "ds_read_b128 a[180:183], v164 offset:13312\n"
    "v_cvt_pk_bf16_f32 v54, v60, v61\n"
    "v_mfma_f32_16x16x32_bf16 a[28:31], a[252:255], v[4:7], a[28:31]\n"
    "v_exp_f32 v62, v62\n"
    "v_mfma_f32_16x16x32_bf16 a[28:31], a[248:251], v[0:3], a[28:31]\n"
    "ds_read_b128 a[184:187], v164 offset:14336\n"
    "v_mfma_f32_16x16x32_bf16 v[0:3], a[128:131], v[64:67], v[128:131]\n"
    "v_exp_f32 v63, v63\n"
    "v_mfma_f32_16x16x32_bf16 v[0:3], a[132:135], v[68:71], v[0:3]\n"
    "ds_read_b128 a[188:191], v164 offset:15360\n"
    "v_cvt_pk_bf16_f32 v55, v62, v63\n"
    "v_mfma_f32_16x16x32_bf16 v[0:3], a[136:139], v[72:75], v[0:3]\n"
    "v_exp_f32 v44, v44\n"
    "v_mfma_f32_16x16x32_bf16 v[0:3], a[140:143], v[76:79], v[0:3]\n"
    "v_xor_b32 v164, s18, v164\n"
    "v_mfma_f32_16x16x32_bf16 v[16:19], a[140:143], v[92:95], v[132:135]\n"
    "v_exp_f32 v45, v45\n"
    "v_mfma_f32_16x16x32_bf16 v[16:19], a[136:139], v[88:91], v[16:19]\n"
    "v_cvt_pk_bf16_f32 v38, v44, v45\n"
    "v_mfma_f32_16x16x32_bf16 v[16:19], a[132:135], v[84:87], v[16:19]\n"
    "v_exp_f32 v46, v46\n"
    "v_mfma_f32_16x16x32_bf16 v[16:19], a[128:131], v[80:83], v[16:19]\n"
    "v_mfma_f32_16x16x32_bf16 v[20:23], a[144:147], v[80:83], v[132:135]\n"
    "v_exp_f32 v47, v47\n"
    "v_mfma_f32_16x16x32_bf16 v[20:23], a[148:151], v[84:87], v[20:23]\n"
    "s_mov_b32 m0, s17\n"
    "v_cvt_pk_bf16_f32 v39, v46, v47\n"
    "v_mfma_f32_16x16x32_bf16 v[20:23], a[152:155], v[88:91], v[20:23]\n"
    "buffer_load_dwordx4 v167, s[8:11], s16 offen offset:0 lds\n"
    "v_mfma_f32_16x16x32_bf16 v[20:23], a[156:159], v[92:95], v[20:23]\n"
    "v_mfma_f32_16x16x32_bf16 v[4:7], a[156:159], v[76:79], v[128:131]\n"
    "v_exp_f32 v0, v0\n"
    "v_mfma_f32_16x16x32_bf16 v[4:7], a[152:155], v[72:75], v[4:7]\n"
    "buffer_load_dwordx4 v168, s[8:11], s16 offen offset:1024 lds\n"
    "v_mfma_f32_16x16x32_bf16 v[4:7], a[148:151], v[68:71], v[4:7]\n"
    "v_exp_f32 v1, v1\n"
    "v_mfma_f32_16x16x32_bf16 v[4:7], a[144:147], v[64:67], v[4:7]\n"
    "v_cvt_pk_bf16_f32 v0, v0, v1\n"
    "v_mfma_f32_16x16x32_bf16 v[8:11], a[160:163], v[64:67], v[128:131]\n"
    "s_waitcnt lgkmcnt(0)\n"
    "v_exp_f32 v2, v2\n"
    "v_mfma_f32_16x16x32_bf16 v[8:11], a[164:167], v[68:71], v[8:11]\n"
    "buffer_load_dwordx4 v169, s[8:11], s16 offen offset:2048 lds\n"
    "v_mfma_f32_16x16x32_bf16 v[8:11], a[168:171], v[72:75], v[8:11]\n"
    "v_exp_f32 v3, v3\n"
    "v_mfma_f32_16x16x32_bf16 v[8:11], a[172:175], v[76:79], v[8:11]\n"
    "v_cvt_pk_bf16_f32 v1, v2, v3\n"
    "v_mfma_f32_16x16x32_bf16 v[24:27], a[172:175], v[92:95], v[132:135]\n"
    "v_exp_f32 v16, v16\n"
    "v_mfma_f32_16x16x32_bf16 v[24:27], a[168:171], v[88:91], v[24:27]\n"
    "buffer_load_dwordx4 v170, s[8:11], s16 offen offset:3072 lds\n"
    "v_mfma_f32_16x16x32_bf16 v[24:27], a[164:167], v[84:87], v[24:27]\n"
    "v_exp_f32 v17, v17\n"
    "v_mfma_f32_16x16x32_bf16 v[24:27], a[160:163], v[80:83], v[24:27]\n"
    "v_cvt_pk_bf16_f32 v16, v16, v17\n"
    "v_mfma_f32_16x16x32_bf16 v[28:31], a[176:179], v[80:83], v[132:135]\n"
    "s_add_u32 m0, s17, 16384\n"
    "v_exp_f32 v18, v18\n"
    "v_mfma_f32_16x16x32_bf16 v[28:31], a[180:183], v[84:87], v[28:31]\n"
    "buffer_load_dwordx4 v171, s[12:15], s16 offen offset:0 sc1 lds\n"
    "v_mfma_f32_16x16x32_bf16 v[28:31], a[184:187], v[88:91], v[28:31]\n"
    "v_exp_f32 v19, v19\n"
    "v_mfma_f32_16x16x32_bf16 v[28:31], a[188:191], v[92:95], v[28:31]\n"
    "v_cvt_pk_bf16_f32 v17, v18, v19\n"
    "v_mfma_f32_16x16x32_bf16 v[12:15], a[188:191], v[76:79], v[128:131]\n"
    "v_exp_f32 v20, v20\n"
    "v_mfma_f32_16x16x32_bf16 v[12:15], a[184:187], v[72:75], v[12:15]\n"
    "buffer_load_dwordx4 v172, s[12:15], s16 offen offset:1024 sc1 lds\n"
    "v_mfma_f32_16x16x32_bf16 v[12:15], a[180:183], v[68:71], v[12:15]\n"
    "v_exp_f32 v21, v21\n"
    "v_mfma_f32_16x16x32_bf16 v[12:15], a[176:179], v[64:67], v[12:15]\n"
    "v_cvt_pk_bf16_f32 v18, v20, v21\n"
    "v_smfmac_f32_16x16x64_bf16 v[152:155], v[160:163], v[32:39], v183\n"
    "v_exp_f32 v22, v22\n"
    "v_smfmac_f32_16x16x64_bf16 v[156:159], v[160:163], v[48:55], v183\n"
    "buffer_load_dwordx4 v171, s[12:15], s16 offen offset:2048 sc1 lds\n"
    "v_mfma_f32_16x16x32_bf16 a[64:67], a[192:195], v[32:35], a[64:67]\n"
    "v_exp_f32 v23, v23\n"
    "v_mfma_f32_16x16x32_bf16 a[64:67], a[196:199], v[36:39], a[64:67]\n"
    "buffer_load_dwordx4 v172, s[12:15], s16 offen offset:3072 sc1 lds\n"
    "v_cvt_pk_bf16_f32 v19, v22, v23\n"
    "v_mfma_f32_16x16x32_bf16 a[96:99], a[196:199], v[52:55], a[96:99]\n"
    "s_xor_b32 s17, s17, s18\n"
    "v_exp_f32 v4, v4\n"
    "v_mfma_f32_16x16x32_bf16 a[96:99], a[192:195], v[48:51], a[96:99]\n"
    "s_add_u32 s16, s16, 16384\n"
    "v_mfma_f32_16x16x32_bf16 a[100:103], a[200:203], v[48:51], a[100:103]\n"
    "s_xor_b32 s18, s18, 98304\n"
    "v_exp_f32 v5, v5\n"
    "v_mfma_f32_16x16x32_bf16 a[100:103], a[204:207], v[52:55], a[100:103]\n"
    "ds_read_b64_tr_b16 a[192:193], v165 offset:16384\n"
    "v_cvt_pk_bf16_f32 v2, v4, v5\n"
    "v_mfma_f32_16x16x32_bf16 a[68:71], a[204:207], v[36:39], a[68:71]\n"
    "v_exp_f32 v6, v6\n"
    "v_mfma_f32_16x16x32_bf16 a[68:71], a[200:203], v[32:35], a[68:71]\n"
    "ds_read_b64_tr_b16 a[194:195], v165 offset:20480\n"
    "v_mfma_f32_16x16x32_bf16 a[72:75], a[208:211], v[32:35], a[72:75]\n"
    "v_exp_f32 v7, v7\n"
    "v_mfma_f32_16x16x32_bf16 a[72:75], a[212:215], v[36:39], a[72:75]\n"
    "ds_read_b64_tr_b16 a[196:197], v165 offset:24576\n"
    "v_cvt_pk_bf16_f32 v3, v6, v7\n"
    "v_mfma_f32_16x16x32_bf16 a[104:107], a[212:215], v[52:55], a[104:107]\n"
    "v_exp_f32 v8, v8\n"
    "v_mfma_f32_16x16x32_bf16 a[104:107], a[208:211], v[48:51], a[104:107]\n"
    "ds_read_b64_tr_b16 a[198:199], v165 offset:28672\n"
    "v_mfma_f32_16x16x32_bf16 a[108:111], a[216:219], v[48:51], a[108:111]\n"
    "v_exp_f32 v9, v9\n"
    "v_mfma_f32_16x16x32_bf16 a[108:111], a[220:223], v[52:55], a[108:111]\n"
    "ds_read_b64_tr_b16 a[200:201], v166 offset:16384\n"
    "v_cvt_pk_bf16_f32 v4, v8, v9\n"
    "v_mfma_f32_16x16x32_bf16 a[76:79], a[220:223], v[36:39], a[76:79]\n"
    "v_exp_f32 v10, v10\n"
    "v_mfma_f32_16x16x32_bf16 a[76:79], a[216:219], v[32:35], a[76:79]\n"
    "ds_read_b64_tr_b16 a[202:203], v166 offset:20480\n"
    "v_mfma_f32_16x16x32_bf16 a[80:83], a[224:227], v[32:35], a[80:83]\n"
    "v_exp_f32 v11, v11\n"
    "v_mfma_f32_16x16x32_bf16 a[80:83], a[228:231], v[36:39], a[80:83]\n"
    "ds_read_b64_tr_b16 a[204:205], v166 offset:24576\n"
    "v_cvt_pk_bf16_f32 v5, v10, v11\n"
    "v_mfma_f32_16x16x32_bf16 a[112:115], a[228:231], v[52:55], a[112:115]\n"
    "v_exp_f32 v24, v24\n"
    "v_mfma_f32_16x16x32_bf16 a[112:115], a[224:227], v[48:51], a[112:115]\n"
    "ds_read_b64_tr_b16 a[206:207], v166 offset:28672\n"
    "v_mfma_f32_16x16x32_bf16 a[116:119], a[232:235], v[48:51], a[116:119]\n"
    "v_exp_f32 v25, v25\n"
    "v_mfma_f32_16x16x32_bf16 a[116:119], a[236:239], v[52:55], a[116:119]\n"
    "ds_read_b64_tr_b16 a[208:209], v165 offset:16896\n"
    "v_cvt_pk_bf16_f32 v20, v24, v25\n"
    "v_mfma_f32_16x16x32_bf16 a[84:87], a[236:239], v[36:39], a[84:87]\n"
    "v_exp_f32 v26, v26\n"
    "v_mfma_f32_16x16x32_bf16 a[84:87], a[232:235], v[32:35], a[84:87]\n"
    "ds_read_b64_tr_b16 a[210:211], v165 offset:20992\n"
    "v_mfma_f32_16x16x32_bf16 a[88:91], a[240:243], v[32:35], a[88:91]\n"
    "v_exp_f32 v27, v27\n"
    "v_mfma_f32_16x16x32_bf16 a[88:91], a[244:247], v[36:39], a[88:91]\n"
    "ds_read_b64_tr_b16 a[212:213], v165 offset:25088\n"
    "v_cvt_pk_bf16_f32 v21, v26, v27\n"
    "v_mfma_f32_16x16x32_bf16 a[120:123], a[244:247], v[52:55], a[120:123]\n"
    "v_exp_f32 v28, v28\n"
    "v_mfma_f32_16x16x32_bf16 a[120:123], a[240:243], v[48:51], a[120:123]\n"
    "ds_read_b64_tr_b16 a[214:215], v165 offset:29184\n"
    "v_mfma_f32_16x16x32_bf16 a[124:127], a[248:251], v[48:51], a[124:127]\n"
    "v_exp_f32 v29, v29\n"
    "v_mfma_f32_16x16x32_bf16 a[124:127], a[252:255], v[52:55], a[124:127]\n"
    "ds_read_b64_tr_b16 a[216:217], v166 offset:16896\n"
    "v_cvt_pk_bf16_f32 v22, v28, v29\n"
    "v_mfma_f32_16x16x32_bf16 a[92:95], a[252:255], v[36:39], a[92:95]\n"
    "v_exp_f32 v30, v30\n"
    "v_mfma_f32_16x16x32_bf16 a[92:95], a[248:251], v[32:35], a[92:95]\n"
    "s_waitcnt lgkmcnt(3)\n"
    "ds_read_b64_tr_b16 a[218:219], v166 offset:20992\n"
    "v_mfma_f32_16x16x32_bf16 v[32:35], a[128:131], v[96:99], v[136:139]\n"
    "v_exp_f32 v31, v31\n"
    "v_mfma_f32_16x16x32_bf16 v[32:35], a[132:135], v[100:103], v[32:35]\n"
    "ds_read_b64_tr_b16 a[220:221], v166 offset:25088\n"
    "v_cvt_pk_bf16_f32 v23, v30, v31\n"
    "v_mfma_f32_16x16x32_bf16 v[32:35], a[136:139], v[104:107], v[32:35]\n"
    "v_exp_f32 v12, v12\n"
    "v_mfma_f32_16x16x32_bf16 v[32:35], a[140:143], v[108:111], v[32:35]\n"
    "ds_read_b64_tr_b16 a[222:223], v166 offset:29184\n"
    "v_mfma_f32_16x16x32_bf16 v[48:51], a[140:143], v[124:127], v[140:143]\n"
    "v_exp_f32 v13, v13\n"
    "v_mfma_f32_16x16x32_bf16 v[48:51], a[136:139], v[120:123], v[48:51]\n"
    "ds_read_b64_tr_b16 a[224:225], v165 offset:17408\n"
    "v_cvt_pk_bf16_f32 v6, v12, v13\n"
    "v_mfma_f32_16x16x32_bf16 v[48:51], a[132:135], v[116:119], v[48:51]\n"
    "v_exp_f32 v14, v14\n"
    "v_mfma_f32_16x16x32_bf16 v[48:51], a[128:131], v[112:115], v[48:51]\n"
    "ds_read_b64_tr_b16 a[226:227], v165 offset:21504\n"
    "v_mfma_f32_16x16x32_bf16 v[52:55], a[144:147], v[112:115], v[140:143]\n"
    "v_exp_f32 v15, v15\n"
    "v_mfma_f32_16x16x32_bf16 v[52:55], a[148:151], v[116:119], v[52:55]\n"
    "s_sub_u32 s20, s20, 1\n"
    "ds_read_b64_tr_b16 a[228:229], v165 offset:25600\n"
    "v_cvt_pk_bf16_f32 v7, v14, v15\n"
    "v_mfma_f32_16x16x32_bf16 v[52:55], a[152:155], v[120:123], v[52:55]\n"
    "s_cbranch_scc0 Lloop%=\n"
    "v_mfma_f32_16x16x32_bf16 v[52:55], a[156:159], v[124:127], v[52:55]\n"
    "s_add_u32 m0, s22, 98304\n"
    "s_add_u32 s84, s46, 262144\n"
    "ds_read_b64_tr_b16 a[230:231], v165 offset:29696\n"
    "v_mfma_f32_16x16x32_bf16 v[36:39], a[156:159], v[108:111], v[136:139]\n"
    "v_exp_f32 v32, v32\n"
    "v_mfma_f32_16x16x32_bf16 v[36:39], a[152:155], v[104:107], v[36:39]\n"
    "s_barrier\n"
    "ds_read_b64_tr_b16 a[232:233], v166 offset:17408\n"
    "v_mfma_f32_16x16x32_bf16 v[36:39], a[148:151], v[100:103], v[36:39]\n"
    "s_lshl_b32 s30, s90, 17\n"
    "s_lshl_b32 s31, s26, 12\n"
    "s_add_u32 s86, s30, s31\n"
    "v_exp_f32 v33, v33\n"
    "v_mfma_f32_16x16x32_bf16 v[36:39], a[144:147], v[96:99], v[36:39]\n"
    "s_add_u32 s87, s86, 131072\n"
    "s_add_u32 s88, s86, 262144\n"
    "ds_read_b64_tr_b16 a[234:235], v166 offset:21504\n"
    "v_cvt_pk_bf16_f32 v32, v32, v33\n"
    "v_mfma_f32_16x16x32_bf16 v[40:43], a[160:163], v[96:99], v[136:139]\n"
    "s_add_u32 s89, s86, 393216\n"
    "v_exp_f32 v34, v34\n"
    "v_mfma_f32_16x16x32_bf16 v[40:43], a[164:167], v[100:103], v[40:43]\n"
    "ds_read_b64_tr_b16 a[236:237], v166 offset:25600\n"
    "v_mfma_f32_16x16x32_bf16 v[40:43], a[168:171], v[104:107], v[40:43]\n"
    "v_exp_f32 v35, v35\n"
    "v_mfma_f32_16x16x32_bf16 v[40:43], a[172:175], v[108:111], v[40:43]\n"
    "ds_read_b64_tr_b16 a[238:239], v166 offset:29696\n"
    "v_cvt_pk_bf16_f32 v33, v34, v35\n"
    "v_mfma_f32_16x16x32_bf16 v[56:59], a[172:175], v[124:127], v[140:143]\n"
    "v_exp_f32 v48, v48\n"
    "v_mfma_f32_16x16x32_bf16 v[56:59], a[168:171], v[120:123], v[56:59]\n"
    "ds_read_b64_tr_b16 a[240:241], v165 offset:17920\n"
    "v_mfma_f32_16x16x32_bf16 v[56:59], a[164:167], v[116:119], v[56:59]\n"
    "v_exp_f32 v49, v49\n"
    "v_mfma_f32_16x16x32_bf16 v[56:59], a[160:163], v[112:115], v[56:59]\n"
    "ds_read_b64_tr_b16 a[242:243], v165 offset:22016\n"
    "v_cvt_pk_bf16_f32 v48, v48, v49\n"
    "v_mfma_f32_16x16x32_bf16 v[60:63], a[176:179], v[112:115], v[140:143]\n"
    "v_exp_f32 v50, v50\n"
    "v_mfma_f32_16x16x32_bf16 v[60:63], a[180:183], v[116:119], v[60:63]\n"
    "ds_read_b64_tr_b16 a[244:245], v165 offset:26112\n"
    "v_mfma_f32_16x16x32_bf16 v[60:63], a[184:187], v[120:123], v[60:63]\n"
    "v_exp_f32 v51, v51\n"
    "v_mfma_f32_16x16x32_bf16 v[60:63], a[188:191], v[124:127], v[60:63]\n"
    "ds_read_b64_tr_b16 a[246:247], v165 offset:30208\n"
    "v_cvt_pk_bf16_f32 v49, v50, v51\n"
    "v_mfma_f32_16x16x32_bf16 v[44:47], a[188:191], v[108:111], v[136:139]\n"
    "v_exp_f32 v52, v52\n"
    "v_mfma_f32_16x16x32_bf16 v[44:47], a[184:187], v[104:107], v[44:47]\n"
    "ds_read_b64_tr_b16 a[248:249], v166 offset:17920\n"
    "v_mfma_f32_16x16x32_bf16 v[44:47], a[180:183], v[100:103], v[44:47]\n"
    "v_exp_f32 v53, v53\n"
    "v_mfma_f32_16x16x32_bf16 v[44:47], a[176:179], v[96:99], v[44:47]\n"
    "ds_read_b64_tr_b16 a[250:251], v166 offset:22016\n"
    "v_cvt_pk_bf16_f32 v50, v52, v53\n"
    "v_smfmac_f32_16x16x64_bf16 v[144:147], v[160:163], v[0:7], v183\n"
    "v_exp_f32 v54, v54\n"
    "v_smfmac_f32_16x16x64_bf16 v[148:151], v[160:163], v[16:23], v183\n"
    "ds_read_b64_tr_b16 a[252:253], v166 offset:26112\n"
    "v_mfma_f32_16x16x32_bf16 a[0:3], a[192:195], v[0:3], a[0:3]\n"
    "v_exp_f32 v55, v55\n"
    "v_mfma_f32_16x16x32_bf16 a[0:3], a[196:199], v[4:7], a[0:3]\n"
    "ds_read_b64_tr_b16 a[254:255], v166 offset:30208\n"
    "v_cvt_pk_bf16_f32 v51, v54, v55\n"
    "v_mfma_f32_16x16x32_bf16 a[32:35], a[196:199], v[20:23], a[32:35]\n"
    "v_exp_f32 v36, v36\n"
    "v_mfma_f32_16x16x32_bf16 a[32:35], a[192:195], v[16:19], a[32:35]\n"
    "buffer_load_dwordx4 v196, s[60:63], s84 offen offset:0 lds\n"
    "v_mfma_f32_16x16x32_bf16 a[36:39], a[200:203], v[16:19], a[36:39]\n"
    "v_exp_f32 v37, v37\n"
    "v_mfma_f32_16x16x32_bf16 a[36:39], a[204:207], v[20:23], a[36:39]\n"
    "buffer_load_dwordx4 v197, s[60:63], s84 offen offset:1024 lds\n"
    "v_cvt_pk_bf16_f32 v34, v36, v37\n"
    "v_mfma_f32_16x16x32_bf16 a[4:7], a[204:207], v[4:7], a[4:7]\n"
    "v_exp_f32 v38, v38\n"
    "v_mfma_f32_16x16x32_bf16 a[4:7], a[200:203], v[0:3], a[4:7]\n"
    "buffer_load_dwordx4 v198, s[60:63], s84 offen offset:2048 lds\n"
    "v_add_f32 v144, v144, v145\n"
    "v_mfma_f32_16x16x32_bf16 a[8:11], a[208:211], v[0:3], a[8:11]\n"
    "s_waitcnt lgkmcnt(1)\n"
    "v_exp_f32 v39, v39\n"
    "v_mfma_f32_16x16x32_bf16 a[8:11], a[212:215], v[4:7], a[8:11]\n"
    "buffer_load_dwordx4 v199, s[60:63], s84 offen offset:3072 lds\n"
    "v_cvt_pk_bf16_f32 v35, v38, v39\n"
    "v_mfma_f32_16x16x32_bf16 a[40:43], a[212:215], v[20:23], a[40:43]\n"
    "s_add_u32 m0, s22, 114688\n"
    "s_add_u32 s84, s48, 8192\n"
    "v_exp_f32 v40, v40\n"
    "v_mfma_f32_16x16x32_bf16 a[40:43], a[208:211], v[16:19], a[40:43]\n"
    "buffer_load_dwordx4 v180, s[72:75], s84 offen offset:0 sc1 lds\n"
    "v_add_f32 v148, v148, v149\n"
    "v_mfma_f32_16x16x32_bf16 a[44:47], a[216:219], v[16:19], a[44:47]\n"
    "v_exp_f32 v41, v41\n"
    "v_mfma_f32_16x16x32_bf16 a[44:47], a[220:223], v[20:23], a[44:47]\n"
    "buffer_load_dwordx4 v180, s[72:75], s84 offen offset:1024 sc1 lds\n"
    "v_cvt_pk_bf16_f32 v36, v40, v41\n"
    "v_mfma_f32_16x16x32_bf16 a[12:15], a[220:223], v[4:7], a[12:15]\n"
    "v_exp_f32 v42, v42\n"
    "v_mfma_f32_16x16x32_bf16 a[12:15], a[216:219], v[0:3], a[12:15]\n"
    "buffer_load_dwordx4 v180, s[72:75], s84 offen offset:2048 sc1 lds\n"
    "v_mfma_f32_16x16x32_bf16 a[16:19], a[224:227], v[0:3], a[16:19]\n"
    "v_exp_f32 v43, v43\n"
    "v_mfma_f32_16x16x32_bf16 a[16:19], a[228:231], v[4:7], a[16:19]\n"
    "buffer_load_dwordx4 v180, s[72:75], s84 offen offset:3072 sc1 lds\n"
    "v_cvt_pk_bf16_f32 v37, v42, v43\n"
    "v_mfma_f32_16x16x32_bf16 a[48:51], a[228:231], v[20:23], a[48:51]\n"
    "s_add_u32 m0, s22, 131072\n"
    "s_add_u32 s84, s46, 393216\n"
    "v_exp_f32 v56, v56\n"
    "v_mfma_f32_16x16x32_bf16 a[48:51], a[224:227], v[16:19], a[48:51]\n"
    "buffer_load_dwordx4 v196, s[60:63], s84 offen offset:0 lds\n"
    "v_mfma_f32_16x16x32_bf16 a[52:55], a[232:235], v[16:19], a[52:55]\n"
    "v_exp_f32 v57, v57\n"
    "v_mfma_f32_16x16x32_bf16 a[52:55], a[236:239], v[20:23], a[52:55]\n"
    "buffer_load_dwordx4 v197, s[60:63], s84 offen offset:1024 lds\n"
    "v_cvt_pk_bf16_f32 v52, v56, v57\n"
    "v_mfma_f32_16x16x32_bf16 a[20:23], a[236:239], v[4:7], a[20:23]\n"
    "v_exp_f32 v58, v58\n"
    "v_mfma_f32_16x16x32_bf16 a[20:23], a[232:235], v[0:3], a[20:23]\n"
    "v_rcp_f32 v146, v144\n"
    "v_mfma_f32_16x16x32_bf16 a[24:27], a[240:243], v[0:3], a[24:27]\n"
    "v_exp_f32 v59, v59\n"
    "v_mfma_f32_16x16x32_bf16 a[24:27], a[244:247], v[4:7], a[24:27]\n"
    "buffer_load_dwordx4 v198, s[60:63], s84 offen offset:2048 lds\n"
    "v_cvt_pk_bf16_f32 v53, v58, v59\n"
    "v_mfma_f32_16x16x32_bf16 a[56:59], a[244:247], v[20:23], a[56:59]\n"
    "v_exp_f32 v60, v60\n"
    "v_mfma_f32_16x16x32_bf16 a[56:59], a[240:243], v[16:19], a[56:59]\n"
    "v_rcp_f32 v150, v148\n"
    "v_mfma_f32_16x16x32_bf16 a[60:63], a[248:251], v[16:19], a[60:63]\n"
    "s_waitcnt lgkmcnt(0)\n"
    "v_exp_f32 v61, v61\n"
    "v_mfma_f32_16x16x32_bf16 a[60:63], a[252:255], v[20:23], a[60:63]\n"
    "buffer_load_dwordx4 v199, s[60:63], s84 offen offset:3072 lds\n"
    "v_cvt_pk_bf16_f32 v54, v60, v61\n"
    "v_mfma_f32_16x16x32_bf16 a[28:31], a[252:255], v[4:7], a[28:31]\n"
    "s_add_u32 m0, s22, 147456\n"
    "s_add_u32 s84, s48, 12288\n"
    "v_exp_f32 v62, v62\n"
    "v_mfma_f32_16x16x32_bf16 a[28:31], a[248:251], v[0:3], a[28:31]\n"
    "v_exp_f32 v63, v63\n"
    "s_nop 0\n"
    "v_cvt_pk_bf16_f32 v55, v62, v63\n"
    "v_exp_f32 v44, v44\n"
    "v_exp_f32 v45, v45\n"
    "s_nop 0\n"
    "v_cvt_pk_bf16_f32 v38, v44, v45\n"
    "v_exp_f32 v46, v46\n"
    "v_exp_f32 v47, v47\n"
    "s_nop 0\n"
    "v_cvt_pk_bf16_f32 v39, v46, v47\n"
    "s_nop 1\n"
    "v_smfmac_f32_16x16x64_bf16 v[152:155], v[160:163], v[32:39], v183\n"
    "v_accvgpr_read_b32 v0, a0\n"
    "v_accvgpr_read_b32 v1, a1\n"
    "v_accvgpr_read_b32 v2, a2\n"
    "v_accvgpr_read_b32 v3, a3\n"
    "v_accvgpr_read_b32 v4, a4\n"
    "v_smfmac_f32_16x16x64_bf16 v[156:159], v[160:163], v[48:55], v183\n"
    "v_accvgpr_read_b32 v5, a5\n"
    "v_accvgpr_read_b32 v6, a6\n"
    "v_accvgpr_read_b32 v7, a7\n"
    "v_mul_f32 v0, v146, v0\n"
    "v_mul_f32 v1, v146, v1\n"
    "v_mfma_f32_16x16x32_bf16 a[64:67], a[192:195], v[32:35], a[64:67]\n"
    "v_mul_f32 v2, v146, v2\n"
    "v_mul_f32 v3, v146, v3\n"
    "v_mul_f32 v4, v146, v4\n"
    "v_mul_f32 v5, v146, v5\n"
    "v_mul_f32 v6, v146, v6\n"
    "v_mfma_f32_16x16x32_bf16 a[64:67], a[196:199], v[36:39], a[64:67]\n"
    "buffer_load_dwordx4 v180, s[72:75], s84 offen offset:0 sc1 lds\n"
    "v_mul_f32 v7, v146, v7\n"
    "v_cvt_pk_bf16_f32 v16, v0, v1\n"
    "v_cvt_pk_bf16_f32 v17, v2, v3\n"
    "v_cvt_pk_bf16_f32 v18, v4, v5\n"
    "v_mfma_f32_16x16x32_bf16 a[96:99], a[196:199], v[52:55], a[96:99]\n"
    "v_cvt_pk_bf16_f32 v19, v6, v7\n"
    "v_accvgpr_read_b32 v8, a8\n"
    "v_accvgpr_read_b32 v9, a9\n"
    "v_accvgpr_read_b32 v10, a10\n"
    "v_accvgpr_read_b32 v11, a11\n"
    "v_mfma_f32_16x16x32_bf16 a[96:99], a[192:195], v[48:51], a[96:99]\n"
    "buffer_store_dwordx4 v[16:19], v180, s[76:79], s86 offen offset:0 sc1\n"
    "v_accvgpr_read_b32 v12, a12\n"
    "v_accvgpr_read_b32 v13, a13\n"
    "v_accvgpr_read_b32 v14, a14\n"
    "v_accvgpr_read_b32 v15, a15\n"
    "v_mfma_f32_16x16x32_bf16 a[100:103], a[200:203], v[48:51], a[100:103]\n"
    "buffer_load_dwordx4 v180, s[72:75], s84 offen offset:1024 sc1 lds\n"
    "v_mul_f32 v8, v146, v8\n"
    "v_mul_f32 v9, v146, v9\n"
    "v_mul_f32 v10, v146, v10\n"
    "v_mul_f32 v11, v146, v11\n"
    "v_mfma_f32_16x16x32_bf16 a[100:103], a[204:207], v[52:55], a[100:103]\n"
    "v_mul_f32 v12, v146, v12\n"
    "v_mul_f32 v13, v146, v13\n"
    "v_mul_f32 v14, v146, v14\n"
    "v_mul_f32 v15, v146, v15\n"
    "v_cvt_pk_bf16_f32 v20, v8, v9\n"
    "v_mfma_f32_16x16x32_bf16 a[68:71], a[204:207], v[36:39], a[68:71]\n"
    "buffer_load_dwordx4 v180, s[72:75], s84 offen offset:2048 sc1 lds\n"
    "v_cvt_pk_bf16_f32 v21, v10, v11\n"
    "v_cvt_pk_bf16_f32 v22, v12, v13\n"
    "v_cvt_pk_bf16_f32 v23, v14, v15\n"
    "v_accvgpr_read_b32 v0, a16\n"
    "v_mfma_f32_16x16x32_bf16 a[68:71], a[200:203], v[32:35], a[68:71]\n"
    "buffer_store_dwordx4 v[20:23], v180, s[76:79], s86 offen offset:1024 sc1\n"
    "v_accvgpr_read_b32 v1, a17\n"
    "v_accvgpr_read_b32 v2, a18\n"
    "v_accvgpr_read_b32 v3, a19\n"
    "v_accvgpr_read_b32 v4, a20\n"
    "v_mfma_f32_16x16x32_bf16 a[72:75], a[208:211], v[32:35], a[72:75]\n"
    "buffer_load_dwordx4 v180, s[72:75], s84 offen offset:3072 sc1 lds\n"
    "v_accvgpr_read_b32 v5, a21\n"
    "v_accvgpr_read_b32 v6, a22\n"
    "v_accvgpr_read_b32 v7, a23\n"
    "v_mul_f32 v0, v146, v0\n"
    "v_mfma_f32_16x16x32_bf16 a[72:75], a[212:215], v[36:39], a[72:75]\n"
    "v_mul_f32 v1, v146, v1\n"
    "v_mul_f32 v2, v146, v2\n"
    "v_mul_f32 v3, v146, v3\n"
    "v_mul_f32 v4, v146, v4\n"
    "v_mul_f32 v5, v146, v5\n"
    "v_mfma_f32_16x16x32_bf16 a[104:107], a[212:215], v[52:55], a[104:107]\n"
    "v_mul_f32 v6, v146, v6\n"
    "v_mul_f32 v7, v146, v7\n"
    "v_cvt_pk_bf16_f32 v24, v0, v1\n"
    "v_cvt_pk_bf16_f32 v25, v2, v3\n"
    "v_cvt_pk_bf16_f32 v26, v4, v5\n"
    "v_mfma_f32_16x16x32_bf16 a[104:107], a[208:211], v[48:51], a[104:107]\n"
    "v_cvt_pk_bf16_f32 v27, v6, v7\n"
    "v_accvgpr_read_b32 v8, a24\n"
    "v_accvgpr_read_b32 v9, a25\n"
    "v_accvgpr_read_b32 v10, a26\n"
    "v_accvgpr_read_b32 v11, a27\n"
    "v_mfma_f32_16x16x32_bf16 a[108:111], a[216:219], v[48:51], a[108:111]\n"
    "buffer_store_dwordx4 v[24:27], v180, s[76:79], s86 offen offset:2048 sc1\n"
    "v_accvgpr_read_b32 v12, a28\n"
    "v_accvgpr_read_b32 v13, a29\n"
    "v_accvgpr_read_b32 v14, a30\n"
    "v_accvgpr_read_b32 v15, a31\n"
    "v_mfma_f32_16x16x32_bf16 a[108:111], a[220:223], v[52:55], a[108:111]\n"
    "v_mul_f32 v8, v146, v8\n"
    "v_mul_f32 v9, v146, v9\n"
    "v_mul_f32 v10, v146, v10\n"
    "v_mul_f32 v11, v146, v11\n"
    "v_mul_f32 v12, v146, v12\n"
    "v_mfma_f32_16x16x32_bf16 a[76:79], a[220:223], v[36:39], a[76:79]\n"
    "v_mul_f32 v13, v146, v13\n"
    "v_mul_f32 v14, v146, v14\n"
    "v_mul_f32 v15, v146, v15\n"
    "v_cvt_pk_bf16_f32 v28, v8, v9\n"
    "v_cvt_pk_bf16_f32 v29, v10, v11\n"
    "v_mfma_f32_16x16x32_bf16 a[76:79], a[216:219], v[32:35], a[76:79]\n"
    "v_cvt_pk_bf16_f32 v30, v12, v13\n"
    "v_cvt_pk_bf16_f32 v31, v14, v15\n"
    "v_accvgpr_read_b32 v0, a32\n"
    "v_accvgpr_read_b32 v1, a33\n"
    "v_accvgpr_read_b32 v2, a34\n"
    "v_mfma_f32_16x16x32_bf16 a[80:83], a[224:227], v[32:35], a[80:83]\n"
    "buffer_store_dwordx4 v[28:31], v180, s[76:79], s86 offen offset:3072 sc1\n"
    "v_accvgpr_read_b32 v3, a35\n"
    "v_accvgpr_read_b32 v4, a36\n"
    "v_accvgpr_read_b32 v5, a37\n"
    "v_accvgpr_read_b32 v6, a38\n"
    "v_mfma_f32_16x16x32_bf16 a[80:83], a[228:231], v[36:39], a[80:83]\n"
    "v_accvgpr_read_b32 v7, a39\n"
    "v_mul_f32 v0, v150, v0\n"
    "v_mul_f32 v1, v150, v1\n"
    "v_mul_f32 v2, v150, v2\n"
    "v_mul_f32 v3, v150, v3\n"
    "v_mfma_f32_16x16x32_bf16 a[112:115], a[228:231], v[52:55], a[112:115]\n"
    "v_mul_f32 v4, v150, v4\n"
    "v_mul_f32 v5, v150, v5\n"
    "v_mul_f32 v6, v150, v6\n"
    "v_mul_f32 v7, v150, v7\n"
    "v_cvt_pk_bf16_f32 v16, v0, v1\n"
    "v_mfma_f32_16x16x32_bf16 a[112:115], a[224:227], v[48:51], a[112:115]\n"
    "v_cvt_pk_bf16_f32 v17, v2, v3\n"
    "v_cvt_pk_bf16_f32 v18, v4, v5\n"
    "v_cvt_pk_bf16_f32 v19, v6, v7\n"
    "v_accvgpr_read_b32 v8, a40\n"
    "v_accvgpr_read_b32 v9, a41\n"
    "v_mfma_f32_16x16x32_bf16 a[116:119], a[232:235], v[48:51], a[116:119]\n"
    "buffer_store_dwordx4 v[16:19], v180, s[76:79], s87 offen offset:0 sc1\n"
    "v_accvgpr_read_b32 v10, a42\n"
    "v_accvgpr_read_b32 v11, a43\n"
    "v_accvgpr_read_b32 v12, a44\n"
    "v_accvgpr_read_b32 v13, a45\n"
    "v_mfma_f32_16x16x32_bf16 a[116:119], a[236:239], v[52:55], a[116:119]\n"
    "v_accvgpr_read_b32 v14, a46\n"
    "v_accvgpr_read_b32 v15, a47\n"
    "v_mul_f32 v8, v150, v8\n"
    "v_mul_f32 v9, v150, v9\n"
    "v_mul_f32 v10, v150, v10\n"
    "v_mfma_f32_16x16x32_bf16 a[84:87], a[236:239], v[36:39], a[84:87]\n"
    "v_mul_f32 v11, v150, v11\n"
    "v_mul_f32 v12, v150, v12\n"
    "v_mul_f32 v13, v150, v13\n"
    "v_mul_f32 v14, v150, v14\n"
    "v_mul_f32 v15, v150, v15\n"
    "v_mfma_f32_16x16x32_bf16 a[84:87], a[232:235], v[32:35], a[84:87]\n"
    "v_cvt_pk_bf16_f32 v20, v8, v9\n"
    "v_cvt_pk_bf16_f32 v21, v10, v11\n"
    "v_cvt_pk_bf16_f32 v22, v12, v13\n"
    "v_cvt_pk_bf16_f32 v23, v14, v15\n"
    "v_accvgpr_read_b32 v0, a48\n"
    "v_mfma_f32_16x16x32_bf16 a[88:91], a[240:243], v[32:35], a[88:91]\n"
    "buffer_store_dwordx4 v[20:23], v180, s[76:79], s87 offen offset:1024 sc1\n"
    "v_accvgpr_read_b32 v1, a49\n"
    "v_accvgpr_read_b32 v2, a50\n"
    "v_accvgpr_read_b32 v3, a51\n"
    "v_accvgpr_read_b32 v4, a52\n"
    "v_mfma_f32_16x16x32_bf16 a[88:91], a[244:247], v[36:39], a[88:91]\n"
    "v_accvgpr_read_b32 v5, a53\n"
    "v_accvgpr_read_b32 v6, a54\n"
    "v_accvgpr_read_b32 v7, a55\n"
    "v_mul_f32 v0, v150, v0\n"
    "v_mul_f32 v1, v150, v1\n"
    "v_mfma_f32_16x16x32_bf16 a[120:123], a[244:247], v[52:55], a[120:123]\n"
    "v_mul_f32 v2, v150, v2\n"
    "v_mul_f32 v3, v150, v3\n"
    "v_mul_f32 v4, v150, v4\n"
    "v_mul_f32 v5, v150, v5\n"
    "v_mul_f32 v6, v150, v6\n"
    "v_mfma_f32_16x16x32_bf16 a[120:123], a[240:243], v[48:51], a[120:123]\n"
    "v_mul_f32 v7, v150, v7\n"
    "v_cvt_pk_bf16_f32 v24, v0, v1\n"
    "v_cvt_pk_bf16_f32 v25, v2, v3\n"
    "v_cvt_pk_bf16_f32 v26, v4, v5\n"
    "v_accvgpr_read_b32 v8, a56\n"
    "v_mfma_f32_16x16x32_bf16 a[124:127], a[248:251], v[48:51], a[124:127]\n"
    "v_cvt_pk_bf16_f32 v27, v6, v7\n"
    "v_accvgpr_read_b32 v9, a57\n"
    "v_accvgpr_read_b32 v10, a58\n"
    "v_accvgpr_read_b32 v11, a59\n"
    "v_accvgpr_read_b32 v12, a60\n"
    "v_mfma_f32_16x16x32_bf16 a[124:127], a[252:255], v[52:55], a[124:127]\n"
    "buffer_store_dwordx4 v[24:27], v180, s[76:79], s87 offen offset:2048 sc1\n"
    "v_accvgpr_read_b32 v13, a61\n"
    "v_accvgpr_read_b32 v14, a62\n"
    "v_accvgpr_read_b32 v15, a63\n"
    "v_mul_f32 v8, v150, v8\n"
    "v_mfma_f32_16x16x32_bf16 a[92:95], a[252:255], v[36:39], a[92:95]\n"
    "v_mul_f32 v9, v150, v9\n"
    "v_mul_f32 v10, v150, v10\n"
    "v_mul_f32 v11, v150, v11\n"
    "v_mul_f32 v12, v150, v12\n"
    "v_mul_f32 v13, v150, v13\n"
    "v_mfma_f32_16x16x32_bf16 a[92:95], a[248:251], v[32:35], a[92:95]\n"
    "v_mul_f32 v14, v150, v14\n"
    "v_mul_f32 v15, v150, v15\n"
    "v_cvt_pk_bf16_f32 v28, v8, v9\n"
    "v_cvt_pk_bf16_f32 v29, v10, v11\n"
    "v_cvt_pk_bf16_f32 v30, v12, v13\n"
    "v_cvt_pk_bf16_f32 v31, v14, v15\n"
    "buffer_store_dwordx4 v[28:31], v180, s[76:79], s87 offen offset:3072 sc1\n"
    "v_add_f32 v152, v152, v153\n"
    "v_add_f32 v156, v156, v157\n"
    "v_rcp_f32 v154, v152\n"
    "v_rcp_f32 v158, v156\n"
    "v_accvgpr_read_b32 v0, a64\n"
    "v_accvgpr_read_b32 v1, a65\n"
    "v_accvgpr_read_b32 v2, a66\n"
    "v_accvgpr_read_b32 v3, a67\n"
    "v_accvgpr_read_b32 v4, a68\n"
    "v_accvgpr_read_b32 v5, a69\n"
    "v_accvgpr_read_b32 v6, a70\n"
    "v_accvgpr_read_b32 v7, a71\n"
    "v_pk_mul_f32 v[0:1], v[0:1], v[154:155] op_sel_hi:[1,0]\n"
    "v_pk_mul_f32 v[2:3], v[2:3], v[154:155] op_sel_hi:[1,0]\n"
    "v_pk_mul_f32 v[4:5], v[4:5], v[154:155] op_sel_hi:[1,0]\n"
    "v_pk_mul_f32 v[6:7], v[6:7], v[154:155] op_sel_hi:[1,0]\n"
    "v_cvt_pk_bf16_f32 v16, v0, v1\n"
    "v_cvt_pk_bf16_f32 v17, v2, v3\n"
    "v_cvt_pk_bf16_f32 v18, v4, v5\n"
    "v_cvt_pk_bf16_f32 v19, v6, v7\n"
    "buffer_store_dwordx4 v[16:19], v180, s[76:79], s88 offen offset:0 sc1\n"
    "v_accvgpr_read_b32 v8, a72\n"
    "v_accvgpr_read_b32 v9, a73\n"
    "v_accvgpr_read_b32 v10, a74\n"
    "v_accvgpr_read_b32 v11, a75\n"
    "v_accvgpr_read_b32 v12, a76\n"
    "v_accvgpr_read_b32 v13, a77\n"
    "v_accvgpr_read_b32 v14, a78\n"
    "v_accvgpr_read_b32 v15, a79\n"
    "v_pk_mul_f32 v[8:9], v[8:9], v[154:155] op_sel_hi:[1,0]\n"
    "v_pk_mul_f32 v[10:11], v[10:11], v[154:155] op_sel_hi:[1,0]\n"
    "v_pk_mul_f32 v[12:13], v[12:13], v[154:155] op_sel_hi:[1,0]\n"
    "v_pk_mul_f32 v[14:15], v[14:15], v[154:155] op_sel_hi:[1,0]\n"
    "v_cvt_pk_bf16_f32 v20, v8, v9\n"
    "v_cvt_pk_bf16_f32 v21, v10, v11\n"
    "v_cvt_pk_bf16_f32 v22, v12, v13\n"
    "v_cvt_pk_bf16_f32 v23, v14, v15\n"
    "buffer_store_dwordx4 v[20:23], v180, s[76:79], s88 offen offset:1024 sc1\n"
    "v_accvgpr_read_b32 v0, a80\n"
    "v_accvgpr_read_b32 v1, a81\n"
    "v_accvgpr_read_b32 v2, a82\n"
    "v_accvgpr_read_b32 v3, a83\n"
    "v_accvgpr_read_b32 v4, a84\n"
    "v_accvgpr_read_b32 v5, a85\n"
    "v_accvgpr_read_b32 v6, a86\n"
    "v_accvgpr_read_b32 v7, a87\n"
    "v_pk_mul_f32 v[0:1], v[0:1], v[154:155] op_sel_hi:[1,0]\n"
    "v_pk_mul_f32 v[2:3], v[2:3], v[154:155] op_sel_hi:[1,0]\n"
    "v_pk_mul_f32 v[4:5], v[4:5], v[154:155] op_sel_hi:[1,0]\n"
    "v_pk_mul_f32 v[6:7], v[6:7], v[154:155] op_sel_hi:[1,0]\n"
    "v_cvt_pk_bf16_f32 v24, v0, v1\n"
    "v_cvt_pk_bf16_f32 v25, v2, v3\n"
    "v_cvt_pk_bf16_f32 v26, v4, v5\n"
    "v_cvt_pk_bf16_f32 v27, v6, v7\n"
    "buffer_store_dwordx4 v[24:27], v180, s[76:79], s88 offen offset:2048 sc1\n"
    "v_accvgpr_read_b32 v8, a88\n"
    "v_accvgpr_read_b32 v9, a89\n"
    "v_accvgpr_read_b32 v10, a90\n"
    "v_accvgpr_read_b32 v11, a91\n"
    "v_accvgpr_read_b32 v12, a92\n"
    "v_accvgpr_read_b32 v13, a93\n"
    "v_accvgpr_read_b32 v14, a94\n"
    "v_accvgpr_read_b32 v15, a95\n"
    "v_pk_mul_f32 v[8:9], v[8:9], v[154:155] op_sel_hi:[1,0]\n"
    "v_pk_mul_f32 v[10:11], v[10:11], v[154:155] op_sel_hi:[1,0]\n"
    "v_pk_mul_f32 v[12:13], v[12:13], v[154:155] op_sel_hi:[1,0]\n"
    "v_pk_mul_f32 v[14:15], v[14:15], v[154:155] op_sel_hi:[1,0]\n"
    "v_cvt_pk_bf16_f32 v28, v8, v9\n"
    "v_cvt_pk_bf16_f32 v29, v10, v11\n"
    "v_cvt_pk_bf16_f32 v30, v12, v13\n"
    "v_cvt_pk_bf16_f32 v31, v14, v15\n"
    "buffer_store_dwordx4 v[28:31], v180, s[76:79], s88 offen offset:3072 sc1\n"
    "v_accvgpr_read_b32 v0, a96\n"
    "v_accvgpr_read_b32 v1, a97\n"
    "v_accvgpr_read_b32 v2, a98\n"
    "v_accvgpr_read_b32 v3, a99\n"
    "v_accvgpr_read_b32 v4, a100\n"
    "v_accvgpr_read_b32 v5, a101\n"
    "v_accvgpr_read_b32 v6, a102\n"
    "v_accvgpr_read_b32 v7, a103\n"
    "v_pk_mul_f32 v[0:1], v[0:1], v[158:159] op_sel_hi:[1,0]\n"
    "v_pk_mul_f32 v[2:3], v[2:3], v[158:159] op_sel_hi:[1,0]\n"
    "v_pk_mul_f32 v[4:5], v[4:5], v[158:159] op_sel_hi:[1,0]\n"
    "v_pk_mul_f32 v[6:7], v[6:7], v[158:159] op_sel_hi:[1,0]\n"
    "v_cvt_pk_bf16_f32 v32, v0, v1\n"
    "v_cvt_pk_bf16_f32 v33, v2, v3\n"
    "v_cvt_pk_bf16_f32 v34, v4, v5\n"
    "v_cvt_pk_bf16_f32 v35, v6, v7\n"
    "buffer_store_dwordx4 v[32:35], v180, s[76:79], s89 offen offset:0 sc1\n"
    "v_accvgpr_read_b32 v8, a104\n"
    "v_accvgpr_read_b32 v9, a105\n"
    "v_accvgpr_read_b32 v10, a106\n"
    "v_accvgpr_read_b32 v11, a107\n"
    "v_accvgpr_read_b32 v12, a108\n"
    "v_accvgpr_read_b32 v13, a109\n"
    "v_accvgpr_read_b32 v14, a110\n"
    "v_accvgpr_read_b32 v15, a111\n"
    "v_pk_mul_f32 v[8:9], v[8:9], v[158:159] op_sel_hi:[1,0]\n"
    "v_pk_mul_f32 v[10:11], v[10:11], v[158:159] op_sel_hi:[1,0]\n"
    "v_pk_mul_f32 v[12:13], v[12:13], v[158:159] op_sel_hi:[1,0]\n"
    "v_pk_mul_f32 v[14:15], v[14:15], v[158:159] op_sel_hi:[1,0]\n"
    "v_cvt_pk_bf16_f32 v36, v8, v9\n"
    "v_cvt_pk_bf16_f32 v37, v10, v11\n"
    "v_cvt_pk_bf16_f32 v38, v12, v13\n"
    "v_cvt_pk_bf16_f32 v39, v14, v15\n"
    "buffer_store_dwordx4 v[36:39], v180, s[76:79], s89 offen offset:1024 sc1\n"
    "v_accvgpr_read_b32 v0, a112\n"
    "v_accvgpr_read_b32 v1, a113\n"
    "v_accvgpr_read_b32 v2, a114\n"
    "v_accvgpr_read_b32 v3, a115\n"
    "v_accvgpr_read_b32 v4, a116\n"
    "v_accvgpr_read_b32 v5, a117\n"
    "v_accvgpr_read_b32 v6, a118\n"
    "v_accvgpr_read_b32 v7, a119\n"
    "v_pk_mul_f32 v[0:1], v[0:1], v[158:159] op_sel_hi:[1,0]\n"
    "v_pk_mul_f32 v[2:3], v[2:3], v[158:159] op_sel_hi:[1,0]\n"
    "v_pk_mul_f32 v[4:5], v[4:5], v[158:159] op_sel_hi:[1,0]\n"
    "v_pk_mul_f32 v[6:7], v[6:7], v[158:159] op_sel_hi:[1,0]\n"
    "v_cvt_pk_bf16_f32 v40, v0, v1\n"
    "v_cvt_pk_bf16_f32 v41, v2, v3\n"
    "v_cvt_pk_bf16_f32 v42, v4, v5\n"
    "v_cvt_pk_bf16_f32 v43, v6, v7\n"
    "buffer_store_dwordx4 v[40:43], v180, s[76:79], s89 offen offset:2048 sc1\n"
    "v_accvgpr_read_b32 v8, a120\n"
    "v_accvgpr_read_b32 v9, a121\n"
    "v_accvgpr_read_b32 v10, a122\n"
    "v_accvgpr_read_b32 v11, a123\n"
    "v_accvgpr_read_b32 v12, a124\n"
    "v_accvgpr_read_b32 v13, a125\n"
    "v_accvgpr_read_b32 v14, a126\n"
    "v_accvgpr_read_b32 v15, a127\n"
    "v_pk_mul_f32 v[8:9], v[8:9], v[158:159] op_sel_hi:[1,0]\n"
    "v_pk_mul_f32 v[10:11], v[10:11], v[158:159] op_sel_hi:[1,0]\n"
    "v_pk_mul_f32 v[12:13], v[12:13], v[158:159] op_sel_hi:[1,0]\n"
    "v_pk_mul_f32 v[14:15], v[14:15], v[158:159] op_sel_hi:[1,0]\n"
    "v_cvt_pk_bf16_f32 v44, v8, v9\n"
    "v_cvt_pk_bf16_f32 v45, v10, v11\n"
    "v_cvt_pk_bf16_f32 v46, v12, v13\n"
    "v_cvt_pk_bf16_f32 v47, v14, v15\n"
    "buffer_store_dwordx4 v[44:47], v180, s[76:79], s89 offen offset:3072 sc1\n"
    "v_add_f32 v190, v144, v148\n"
    "v_add_f32 v191, v152, v156\n"
    "v_add_f32 v190, v190, v191\n"
    "v_cmp_nge_f32 vcc, 0x4f800000, v190\n"
    "s_cmp_lg_u64 vcc, 0\n"
    "s_cselect_b32 s28, 1, 0\n"
    "s_andn2_b32 s28, s28, s21\n"
    "s_lshl_b32 s29, s25, 2\n"
    "s_add_u32 s29, s29, 65536\n"
    "v_mov_b32 v189, s29\n"
    "v_mov_b32 v190, s28\n"
    "ds_write_b32 v189, v190\n"
    "v_mov_b32 v189, 65536\n"
    "s_waitcnt lgkmcnt(0)\n"
    "s_waitcnt vmcnt(40)\n"
    "s_barrier\n"
    "ds_read_b128 v[48:51], v189\n"
    "ds_read_b64_tr_b16 a[128:129], v207 offset:0\n"
    "ds_read_b64_tr_b16 a[130:131], v207 offset:2048\n"
    "ds_read_b128 a[192:195], v213 offset:0\n"
    "ds_read_b64_tr_b16 a[132:133], v207 offset:4096\n"
    "ds_read_b64_tr_b16 a[134:135], v207 offset:6144\n"
    "ds_read_b128 a[196:199], v213 offset:1024\n"
    "ds_read_b64_tr_b16 a[136:137], v207 offset:8192\n"
    "ds_read_b64_tr_b16 a[138:139], v207 offset:10240\n"
    "ds_read_b128 a[200:203], v213 offset:2048\n"
    "ds_read_b64_tr_b16 a[140:141], v207 offset:12288\n"
    "ds_read_b64_tr_b16 a[142:143], v207 offset:14336\n"
    "ds_read_b128 a[204:207], v213 offset:3072\n"
    "ds_read_b128 a[220:223], v213 offset:7168\n"
    "ds_read_b128 a[216:219], v213 offset:6144\n"
    "ds_read_b128 a[212:215], v213 offset:5120\n"
    "ds_read_b128 a[208:211], v213 offset:4096\n"
    "ds_read_b64_tr_b16 a[144:145], v208 offset:0\n"
    "ds_read_b64_tr_b16 a[146:147], v208 offset:2048\n"
    "ds_read_b64_tr_b16 a[148:149], v208 offset:4096\n"
    "ds_read_b64_tr_b16 a[150:151], v208 offset:6144\n"
    "ds_read_b64_tr_b16 a[152:153], v208 offset:8192\n"
    "ds_read_b64_tr_b16 a[154:155], v208 offset:10240\n"
    "ds_read_b64_tr_b16 a[156:157], v208 offset:12288\n"
    "ds_read_b64_tr_b16 a[158:159], v208 offset:14336\n"
    "s_waitcnt lgkmcnt(15)\n"
    "v_or3_b32 v48, v48, v49, v50\n"
    "v_or_b32 v48, v48, v51\n"
    "s_nop 0\n"
    "v_readfirstlane_b32 s28, v48\n"
    "s_cmp_lg_u32 s28, 0\n"
    "s_cbranch_scc1 Lfallback%=\n"
    "s_waitcnt lgkmcnt(0)\n"
    "v_mfma_f32_16x16x32_bf16 v[0:3], a[128:131], a[192:195], 0\n"
    "s_waitcnt vmcnt(32)\n"
    "v_mfma_f32_16x16x32_bf16 v[0:3], a[132:135], a[196:199], v[0:3]\n"
    "s_barrier\n"
    "v_mfma_f32_16x16x32_bf16 v[0:3], a[136:139], a[200:203], v[0:3]\n"
    "s_add_u32 m0, s22, 81920\n"
    "s_add_u32 s84, s48, 16384\n"
    "ds_read_b64_tr_b16 a[160:161], v207 offset:32768\n"
    "ds_read_b64_tr_b16 a[162:163], v207 offset:34816\n"
    "v_mfma_f32_16x16x32_bf16 v[0:3], a[140:143], a[204:207], v[0:3]\n"
    "buffer_load_dwordx4 v180, s[72:75], s84 offen offset:0 sc1 lds\n"
    "buffer_load_dwordx4 v180, s[72:75], s84 offen offset:1024 sc1 lds\n"
    "v_mfma_f32_16x16x32_bf16 v[8:11], a[140:143], a[220:223], 0\n"
    "buffer_load_dwordx4 v180, s[72:75], s84 offen offset:2048 sc1 lds\n"
    "buffer_load_dwordx4 v180, s[72:75], s84 offen offset:3072 sc1 lds\n"
    "v_mfma_f32_16x16x32_bf16 v[8:11], a[136:139], a[216:219], v[8:11]\n"
    "s_add_u32 m0, s22, 65536\n"
    "s_add_u32 s84, s46, 524288\n"
    "ds_read_b128 a[224:227], v213 offset:32768\n"
    "ds_read_b64_tr_b16 a[164:165], v207 offset:36864\n"
    "v_mfma_f32_16x16x32_bf16 v[8:11], a[132:135], a[212:215], v[8:11]\n"
    "ds_read_b64_tr_b16 a[166:167], v207 offset:38912\n"
    "ds_read_b128 a[228:231], v213 offset:33792\n"
    "v_mfma_f32_16x16x32_bf16 v[8:11], a[128:131], a[208:211], v[8:11]\n"
    "ds_read_b64_tr_b16 a[168:169], v207 offset:40960\n"
    "ds_read_b64_tr_b16 a[170:171], v207 offset:43008\n"
    "v_mfma_f32_16x16x32_bf16 v[12:15], a[144:147], a[208:211], 0\n"
    "ds_read_b128 a[232:235], v213 offset:34816\n"
    "ds_read_b64_tr_b16 a[172:173], v207 offset:45056\n"
    "v_mfma_f32_16x16x32_bf16 v[12:15], a[148:151], a[212:215], v[12:15]\n"
    "ds_read_b64_tr_b16 a[174:175], v207 offset:47104\n"
    "ds_read_b128 a[236:239], v213 offset:35840\n"
    "v_mfma_f32_16x16x32_bf16 v[12:15], a[152:155], a[216:219], v[12:15]\n"
    "buffer_load_dwordx4 v196, s[60:63], s84 offen offset:0 lds\n"
    "buffer_load_dwordx4 v197, s[60:63], s84 offen offset:1024 lds\n"
    "v_mfma_f32_16x16x32_bf16 v[12:15], a[156:159], a[220:223], v[12:15]\n"
    "buffer_load_dwordx4 v198, s[60:63], s84 offen offset:2048 lds\n"
    "buffer_load_dwordx4 v199, s[60:63], s84 offen offset:3072 lds\n"
    "v_mfma_f32_16x16x32_bf16 v[4:7], a[156:159], a[204:207], 0\n"
    "ds_read_b128 a[252:255], v213 offset:39936\n"
    "ds_read_b128 a[248:251], v213 offset:38912\n"
    "v_mfma_f32_16x16x32_bf16 v[4:7], a[152:155], a[200:203], v[4:7]\n"
    "ds_read_b128 a[244:247], v213 offset:37888\n"
    "ds_read_b128 a[240:243], v213 offset:36864\n"
    "v_mfma_f32_16x16x32_bf16 v[4:7], a[148:151], a[196:199], v[4:7]\n"
    "ds_read_b64_tr_b16 a[176:177], v208 offset:32768\n"
    "ds_read_b64_tr_b16 a[178:179], v208 offset:34816\n"
    "v_mfma_f32_16x16x32_bf16 v[4:7], a[144:147], a[192:195], v[4:7]\n"
    "s_waitcnt lgkmcnt(4)\n"
    "ds_read_b64_tr_b16 a[180:181], v208 offset:36864\n"
    "ds_read_b64_tr_b16 a[182:183], v208 offset:38912\n"
    "v_mfma_f32_16x16x32_bf16 v[0:3], a[160:163], a[224:227], v[0:3]\n"
    "s_waitcnt vmcnt(8)\n"
    "ds_read_b64_tr_b16 a[184:185], v208 offset:40960\n"
    "ds_read_b64_tr_b16 a[186:187], v208 offset:43008\n"
    "v_mfma_f32_16x16x32_bf16 v[0:3], a[164:167], a[228:231], v[0:3]\n"
    "s_barrier\n"
    "s_cmp_lg_u32 s25, 0\n"
    "s_cbranch_scc1 Lpb_o%=\n"
    "v_mov_b32 v184, s92\n"
    "v_mov_b32 v186, s80\n"
    "v_mov_b32 v187, s81\n"
    "s_mov_b64 exec, 1\n"
    "global_store_dwordx2 v184, v[186:187], s[82:83] offset:1024 sc1\n"
    "s_mov_b64 exec, -1\n"
    "Lpb_o%=:\n"
    "ds_read_b64_tr_b16 a[188:189], v208 offset:45056\n"
    "ds_read_b64_tr_b16 a[190:191], v208 offset:47104\n"
    "v_mfma_f32_16x16x32_bf16 v[0:3], a[168:171], a[232:235], v[0:3]\n"
    "s_add_u32 m0, s22, 16384\n"
    "s_add_u32 s84, s48, 20480\n"
    "ds_read_b64_tr_b16 a[128:129], v209 offset:32768\n"
    "ds_read_b64_tr_b16 a[130:131], v209 offset:34816\n"
    "v_mfma_f32_16x16x32_bf16 v[0:3], a[172:175], a[236:239], v[0:3]\n"
    "buffer_load_dwordx4 v180, s[72:75], s84 offen offset:0 sc1 lds\n"
    "buffer_load_dwordx4 v180, s[72:75], s84 offen offset:1024 sc1 lds\n"
    "v_mfma_f32_16x16x32_bf16 v[8:11], a[172:175], a[252:255], v[8:11]\n"
    "buffer_load_dwordx4 v180, s[72:75], s84 offen offset:2048 sc1 lds\n"
    "buffer_load_dwordx4 v180, s[72:75], s84 offen offset:3072 sc1 lds\n"
    "v_mfma_f32_16x16x32_bf16 v[8:11], a[168:171], a[248:251], v[8:11]\n"
    "s_waitcnt lgkmcnt(0)\n"
    "s_add_u32 m0, s22, 0\n"
    "s_add_u32 s84, s46, 655360\n"
    "ds_read_b128 a[192:195], v214 offset:32768\n"
    "ds_read_b64_tr_b16 a[132:133], v209 offset:36864\n"
    "v_mfma_f32_16x16x32_bf16 v[8:11], a[164:167], a[244:247], v[8:11]\n"
    "ds_read_b64_tr_b16 a[134:135], v209 offset:38912\n"
    "ds_read_b128 a[196:199], v214 offset:33792\n"
    "v_mfma_f32_16x16x32_bf16 v[8:11], a[160:163], a[240:243], v[8:11]\n"
    "ds_read_b64_tr_b16 a[136:137], v209 offset:40960\n"
    "ds_read_b64_tr_b16 a[138:139], v209 offset:43008\n"
    "v_mfma_f32_16x16x32_bf16 v[12:15], a[176:179], a[240:243], v[12:15]\n"
    "ds_read_b128 a[200:203], v214 offset:34816\n"
    "ds_read_b64_tr_b16 a[140:141], v209 offset:45056\n"
    "v_mfma_f32_16x16x32_bf16 v[12:15], a[180:183], a[244:247], v[12:15]\n"
    "ds_read_b64_tr_b16 a[142:143], v209 offset:47104\n"
    "ds_read_b128 a[204:207], v214 offset:35840\n"
    "v_mfma_f32_16x16x32_bf16 v[12:15], a[184:187], a[248:251], v[12:15]\n"
    "buffer_load_dwordx4 v196, s[60:63], s84 offen offset:0 lds\n"
    "buffer_load_dwordx4 v197, s[60:63], s84 offen offset:1024 lds\n"
    "v_mfma_f32_16x16x32_bf16 v[12:15], a[188:191], a[252:255], v[12:15]\n"
    "buffer_load_dwordx4 v198, s[60:63], s84 offen offset:2048 lds\n"
    "buffer_load_dwordx4 v199, s[60:63], s84 offen offset:3072 lds\n"
    "v_mfma_f32_16x16x32_bf16 v[4:7], a[188:191], a[236:239], v[4:7]\n"
    "ds_read_b128 a[220:223], v214 offset:39936\n"
    "ds_read_b128 a[216:219], v214 offset:38912\n"
    "v_mfma_f32_16x16x32_bf16 v[4:7], a[184:187], a[232:235], v[4:7]\n"
    "ds_read_b128 a[212:215], v214 offset:37888\n"
    "ds_read_b128 a[208:211], v214 offset:36864\n"
    "v_mfma_f32_16x16x32_bf16 v[4:7], a[180:183], a[228:231], v[4:7]\n"
    "ds_read_b64_tr_b16 a[144:145], v210 offset:32768\n"
    "ds_read_b64_tr_b16 a[146:147], v210 offset:34816\n"
    "v_mfma_f32_16x16x32_bf16 v[4:7], a[176:179], a[224:227], v[4:7]\n"
    "s_waitcnt lgkmcnt(4)\n"
    "ds_read_b64_tr_b16 a[148:149], v210 offset:36864\n"
    "ds_read_b64_tr_b16 a[150:151], v210 offset:38912\n"
    "v_mfma_f32_16x16x32_bf16 v[0:3], a[128:131], a[192:195], v[0:3]\n"
    "s_waitcnt vmcnt(30) lgkmcnt(15)\n"
    "ds_read_b64_tr_b16 a[152:153], v210 offset:40960\n"
    "ds_read_b64_tr_b16 a[154:155], v210 offset:43008\n"
    "v_mfma_f32_16x16x32_bf16 v[0:3], a[132:135], a[196:199], v[0:3]\n"
    "s_barrier\n"
    "ds_read_b64_tr_b16 a[156:157], v210 offset:45056\n"
    "ds_read_b64_tr_b16 a[158:159], v210 offset:47104\n"
    "v_mfma_f32_16x16x32_bf16 v[0:3], a[136:139], a[200:203], v[0:3]\n"
    "s_add_u32 m0, s22, 32768\n"
    "s_add_u32 s84, s47, 0\n"
    "ds_read_b64_tr_b16 a[160:161], v211 offset:0\n"
    "ds_read_b64_tr_b16 a[162:163], v211 offset:2048\n"
    "v_mfma_f32_16x16x32_bf16 v[0:3], a[140:143], a[204:207], v[0:3]\n"
    "buffer_load_dwordx4 v192, s[64:67], s84 offen offset:0 lds\n"
    "buffer_load_dwordx4 v193, s[64:67], s84 offen offset:1024 lds\n"
    "v_mfma_f32_16x16x32_bf16 v[8:11], a[140:143], a[220:223], v[8:11]\n"
    "ds_read_b128 a[224:227], v215 offset:0\n"
    "ds_read_b64_tr_b16 a[164:165], v211 offset:4096\n"
    "v_mfma_f32_16x16x32_bf16 v[8:11], a[136:139], a[216:219], v[8:11]\n"
    "s_waitcnt lgkmcnt(2)\n"
    "ds_read_b64_tr_b16 a[166:167], v211 offset:6144\n"
    "ds_read_b128 a[228:231], v215 offset:1024\n"
    "v_mfma_f32_16x16x32_bf16 v[8:11], a[132:135], a[212:215], v[8:11]\n"
    "ds_read_b64_tr_b16 a[168:169], v211 offset:8192\n"
    "ds_read_b64_tr_b16 a[170:171], v211 offset:10240\n"
    "v_mfma_f32_16x16x32_bf16 v[8:11], a[128:131], a[208:211], v[8:11]\n"
    "ds_read_b128 a[232:235], v215 offset:2048\n"
    "ds_read_b64_tr_b16 a[172:173], v211 offset:12288\n"
    "v_mfma_f32_16x16x32_bf16 v[12:15], a[144:147], a[208:211], v[12:15]\n"
    "ds_read_b64_tr_b16 a[174:175], v211 offset:14336\n"
    "ds_read_b128 a[236:239], v215 offset:3072\n"
    "v_mfma_f32_16x16x32_bf16 v[12:15], a[148:151], a[212:215], v[12:15]\n"
    "buffer_load_dwordx4 v194, s[64:67], s84 offen offset:2048 lds\n"
    "buffer_load_dwordx4 v195, s[64:67], s84 offen offset:3072 lds\n"
    "v_mfma_f32_16x16x32_bf16 v[12:15], a[152:155], a[216:219], v[12:15]\n"
    "global_load_dwordx2 v[222:223], v218, s[82:83] offset:1024 sc1\n"
    "ds_read_b128 a[252:255], v215 offset:7168\n"
    "v_mfma_f32_16x16x32_bf16 v[12:15], a[156:159], a[220:223], v[12:15]\n"
    "ds_read_b128 a[248:251], v215 offset:6144\n"
    "ds_read_b128 a[244:247], v215 offset:5120\n"
    "v_mfma_f32_16x16x32_bf16 v[4:7], a[156:159], a[204:207], v[4:7]\n"
    "ds_read_b128 a[240:243], v215 offset:4096\n"
    "ds_read_b64_tr_b16 a[176:177], v212 offset:0\n"
    "v_mfma_f32_16x16x32_bf16 v[4:7], a[152:155], a[200:203], v[4:7]\n"
    "ds_read_b64_tr_b16 a[178:179], v212 offset:2048\n"
    "ds_read_b64_tr_b16 a[180:181], v212 offset:4096\n"
    "v_mfma_f32_16x16x32_bf16 v[4:7], a[148:151], a[196:199], v[4:7]\n"
    "ds_read_b64_tr_b16 a[182:183], v212 offset:6144\n"
    "ds_read_b64_tr_b16 a[184:185], v212 offset:8192\n"
    "v_mfma_f32_16x16x32_bf16 v[4:7], a[144:147], a[192:195], v[4:7]\n"
    "s_waitcnt lgkmcnt(4)\n"
    "ds_read_b64_tr_b16 a[186:187], v212 offset:10240\n"
    "ds_read_b64_tr_b16 a[188:189], v212 offset:12288\n"
    "v_mfma_f32_16x16x32_bf16 v[0:3], a[160:163], a[224:227], v[0:3]\n"
    "s_waitcnt vmcnt(13) lgkmcnt(15)\n"
    "ds_read_b64_tr_b16 a[190:191], v212 offset:14336\n"
    "v_mfma_f32_16x16x32_bf16 v[0:3], a[164:167], a[228:231], v[0:3]\n"
    "s_barrier\n"
    "v_mfma_f32_16x16x32_bf16 v[0:3], a[168:171], a[232:235], v[0:3]\n"
    "s_waitcnt vmcnt(0)\n"
    "s_mov_b32 s28, 0x40000\n"
    "Lpk_o%=:\n"
    "v_cmp_ne_u64 vcc, s[80:81], v[222:223]\n"
    "s_cmp_eq_u64 vcc, 0\n"
    "s_cbranch_scc1 Lpkd_o%=\n"
    "global_load_dwordx2 v[222:223], v218, s[82:83] offset:1024 sc1\n"
    "s_waitcnt vmcnt(0)\n"
    "s_sub_u32 s28, s28, 1\n"
    "s_cbranch_scc0 Lpk_o%=\n"
    "Lpkd_o%=:\n"
    "s_add_u32 m0, s22, 49152\n"
    "ds_read_b64_tr_b16 a[128:129], v209 offset:0\n"
    "ds_read_b64_tr_b16 a[130:131], v209 offset:2048\n"
    "v_mfma_f32_16x16x32_bf16 v[0:3], a[172:175], a[236:239], v[0:3]\n"
    "s_add_u32 s84, s49, 0\n"
    "ds_read_b128 a[192:195], v214 offset:0\n"
    "ds_read_b64_tr_b16 a[132:133], v209 offset:4096\n"
    "v_mfma_f32_16x16x32_bf16 v[8:11], a[172:175], a[252:255], v[8:11]\n"
    "buffer_load_dwordx4 v180, s[76:79], s84 offen offset:0 sc1 lds\n"
    "buffer_load_dwordx4 v180, s[76:79], s84 offen offset:1024 sc1 lds\n"
    "v_mfma_f32_16x16x32_bf16 v[8:11], a[168:171], a[248:251], v[8:11]\n"
    "buffer_load_dwordx4 v200, s[76:79], s84 offen offset:2048 sc1 lds\n"
    "buffer_load_dwordx4 v200, s[76:79], s84 offen offset:3072 sc1 lds\n"
    "v_mfma_f32_16x16x32_bf16 v[8:11], a[164:167], a[244:247], v[8:11]\n"
    "s_add_u32 m0, s22, 114688\n"
    "s_add_u32 s84, s49, 2048\n"
    "ds_read_b64_tr_b16 a[134:135], v209 offset:6144\n"
    "ds_read_b128 a[196:199], v214 offset:1024\n"
    "v_mfma_f32_16x16x32_bf16 v[8:11], a[160:163], a[240:243], v[8:11]\n"
    "s_waitcnt lgkmcnt(2)\n"
    "buffer_load_dwordx4 v180, s[76:79], s84 offen offset:0 sc1 lds\n"
    "buffer_load_dwordx4 v180, s[76:79], s84 offen offset:1024 sc1 lds\n"
    "v_mfma_f32_16x16x32_bf16 v[12:15], a[176:179], a[240:243], v[12:15]\n"
    "ds_read_b64_tr_b16 a[136:137], v209 offset:8192\n"
    "ds_read_b64_tr_b16 a[138:139], v209 offset:10240\n"
    "v_mfma_f32_16x16x32_bf16 v[12:15], a[180:183], a[244:247], v[12:15]\n"
    "ds_read_b128 a[200:203], v214 offset:2048\n"
    "ds_read_b64_tr_b16 a[140:141], v209 offset:12288\n"
    "v_mfma_f32_16x16x32_bf16 v[12:15], a[184:187], a[248:251], v[12:15]\n"
    "ds_read_b64_tr_b16 a[142:143], v209 offset:14336\n"
    "ds_read_b128 a[204:207], v214 offset:3072\n"
    "v_mfma_f32_16x16x32_bf16 v[12:15], a[188:191], a[252:255], v[12:15]\n"
    "buffer_load_dwordx4 v200, s[76:79], s84 offen offset:2048 sc1 lds\n"
    "buffer_load_dwordx4 v200, s[76:79], s84 offen offset:3072 sc1 lds\n"
    "v_mfma_f32_16x16x32_bf16 v[4:7], a[188:191], a[236:239], v[4:7]\n"
    "s_add_u32 m0, s22, 98304\n"
    "s_add_u32 s84, s47, 65536\n"
    "ds_read_b128 a[220:223], v214 offset:7168\n"
    "ds_read_b128 a[216:219], v214 offset:6144\n"
    "v_mfma_f32_16x16x32_bf16 v[4:7], a[184:187], a[232:235], v[4:7]\n"
    "buffer_load_dwordx4 v192, s[64:67], s84 offen offset:0 lds\n"
    "buffer_load_dwordx4 v193, s[64:67], s84 offen offset:1024 lds\n"
    "buffer_load_dwordx4 v194, s[64:67], s84 offen offset:2048 lds\n"
    "v_mfma_f32_16x16x32_bf16 v[4:7], a[180:183], a[228:231], v[4:7]\n"
    "buffer_load_dwordx4 v195, s[64:67], s84 offen offset:3072 lds\n"
    "ds_read_b128 a[212:215], v214 offset:5120\n"
    "v_mfma_f32_16x16x32_bf16 v[4:7], a[176:179], a[224:227], v[4:7]\n"
    "ds_read_b128 a[208:211], v214 offset:4096\n"
    "ds_read_b64_tr_b16 a[144:145], v210 offset:0\n"
    "v_mfma_f32_16x16x32_bf16 v[0:3], a[128:131], a[192:195], v[0:3]\n"
    "s_waitcnt vmcnt(17) lgkmcnt(3)\n"
    "ds_read_b64_tr_b16 a[146:147], v210 offset:2048\n"
    "ds_read_b64_tr_b16 a[148:149], v210 offset:4096\n"
    "v_mfma_f32_16x16x32_bf16 v[0:3], a[132:135], a[196:199], v[0:3]\n"
    "s_barrier\n"
    "ds_read_b64_tr_b16 a[150:151], v210 offset:6144\n"
    "ds_read_b64_tr_b16 a[152:153], v210 offset:8192\n"
    "v_mfma_f32_16x16x32_bf16 v[0:3], a[136:139], a[200:203], v[0:3]\n"
    "s_add_u32 m0, s22, 147456\n"
    "s_add_u32 s84, s49, 4096\n"
    "ds_read_b64_tr_b16 a[154:155], v210 offset:10240\n"
    "ds_read_b64_tr_b16 a[156:157], v210 offset:12288\n"
    "v_mfma_f32_16x16x32_bf16 v[0:3], a[140:143], a[204:207], v[0:3]\n"
    "ds_read_b64_tr_b16 a[158:159], v210 offset:14336\n"
    "buffer_load_dwordx4 v180, s[76:79], s84 offen offset:0 sc1 lds\n"
    "v_mfma_f32_16x16x32_bf16 v[8:11], a[140:143], a[220:223], v[8:11]\n"
    "buffer_load_dwordx4 v180, s[76:79], s84 offen offset:1024 sc1 lds\n"
    "buffer_load_dwordx4 v200, s[76:79], s84 offen offset:2048 sc1 lds\n"
    "v_mfma_f32_16x16x32_bf16 v[8:11], a[136:139], a[216:219], v[8:11]\n"
    "s_waitcnt lgkmcnt(1)\n"
    "buffer_load_dwordx4 v200, s[76:79], s84 offen offset:3072 sc1 lds\n"
    "ds_read_b64_tr_b16 a[160:161], v207 offset:0\n"
    "v_mfma_f32_16x16x32_bf16 v[8:11], a[132:135], a[212:215], v[8:11]\n"
    "s_add_u32 m0, s22, 131072\n"
    "s_add_u32 s84, s47, 131072\n"
    "ds_read_b64_tr_b16 a[162:163], v207 offset:2048\n"
    "ds_read_b128 a[224:227], v213 offset:0\n"
    "v_mfma_f32_16x16x32_bf16 v[8:11], a[128:131], a[208:211], v[8:11]\n"
    "ds_read_b64_tr_b16 a[164:165], v207 offset:4096\n"
    "ds_read_b64_tr_b16 a[166:167], v207 offset:6144\n"
    "v_mfma_f32_16x16x32_bf16 v[12:15], a[144:147], a[208:211], v[12:15]\n"
    "ds_read_b128 a[228:231], v213 offset:1024\n"
    "ds_read_b64_tr_b16 a[168:169], v207 offset:8192\n"
    "v_mfma_f32_16x16x32_bf16 v[12:15], a[148:151], a[212:215], v[12:15]\n"
    "ds_read_b64_tr_b16 a[170:171], v207 offset:10240\n"
    "ds_read_b128 a[232:235], v213 offset:2048\n"
    "v_mfma_f32_16x16x32_bf16 v[12:15], a[152:155], a[216:219], v[12:15]\n"
    "s_waitcnt lgkmcnt(4)\n"
    "ds_read_b64_tr_b16 a[172:173], v207 offset:12288\n"
    "ds_read_b64_tr_b16 a[174:175], v207 offset:14336\n"
    "v_mfma_f32_16x16x32_bf16 v[12:15], a[156:159], a[220:223], v[12:15]\n"
    "ds_read_b128 a[236:239], v213 offset:3072\n"
    "buffer_load_dwordx4 v192, s[64:67], s84 offen offset:0 lds\n"
    "v_mfma_f32_16x16x32_bf16 v[4:7], a[156:159], a[204:207], v[4:7]\n"
    "buffer_load_dwordx4 v193, s[64:67], s84 offen offset:1024 lds\n"
    "buffer_load_dwordx4 v194, s[64:67], s84 offen offset:2048 lds\n"
    "v_mfma_f32_16x16x32_bf16 v[4:7], a[152:155], a[200:203], v[4:7]\n"
    "buffer_load_dwordx4 v195, s[64:67], s84 offen offset:3072 lds\n"
    "ds_read_b128 a[252:255], v213 offset:7168\n"
    "v_mfma_f32_16x16x32_bf16 v[4:7], a[148:151], a[196:199], v[4:7]\n"
    "ds_read_b128 a[248:251], v213 offset:6144\n"
    "ds_read_b128 a[244:247], v213 offset:5120\n"
    "v_mfma_f32_16x16x32_bf16 v[4:7], a[144:147], a[192:195], v[4:7]\n"
    "ds_read_b128 a[240:243], v213 offset:4096\n"
    "ds_read_b64_tr_b16 a[176:177], v208 offset:0\n"
    "v_mfma_f32_16x16x32_bf16 v[0:3], a[160:163], a[224:227], v[0:3]\n"
    "s_waitcnt vmcnt(16) lgkmcnt(4)\n"
    "ds_read_b64_tr_b16 a[178:179], v208 offset:2048\n"
    "ds_read_b64_tr_b16 a[180:181], v208 offset:4096\n"
    "v_mfma_f32_16x16x32_bf16 v[0:3], a[164:167], a[228:231], v[0:3]\n"
    "s_barrier\n"
    "ds_read_b64_tr_b16 a[182:183], v208 offset:6144\n"
    "ds_read_b64_tr_b16 a[184:185], v208 offset:8192\n"
    "v_mfma_f32_16x16x32_bf16 v[0:3], a[168:171], a[232:235], v[0:3]\n"
    "s_add_u32 m0, s22, 81920\n"
    "s_add_u32 s84, s49, 6144\n"
    "ds_read_b64_tr_b16 a[186:187], v208 offset:10240\n"
    "ds_read_b64_tr_b16 a[188:189], v208 offset:12288\n"
    "v_mfma_f32_16x16x32_bf16 v[0:3], a[172:175], a[236:239], v[0:3]\n"
    "ds_read_b64_tr_b16 a[190:191], v208 offset:14336\n"
    "buffer_load_dwordx4 v180, s[76:79], s84 offen offset:0 sc1 lds\n"
    "v_mfma_f32_16x16x32_bf16 v[8:11], a[172:175], a[252:255], v[8:11]\n"
    "s_waitcnt lgkmcnt(3)\n"
    "buffer_load_dwordx4 v180, s[76:79], s84 offen offset:1024 sc1 lds\n"
    "buffer_load_dwordx4 v200, s[76:79], s84 offen offset:2048 sc1 lds\n"
    "v_mfma_f32_16x16x32_bf16 v[8:11], a[168:171], a[248:251], v[8:11]\n"
    "buffer_load_dwordx4 v200, s[76:79], s84 offen offset:3072 sc1 lds\n"
    "ds_read_b64_tr_b16 a[128:129], v201 offset:32768\n"
    "v_mfma_f32_16x16x32_bf16 v[8:11], a[164:167], a[244:247], v[8:11]\n"
    "s_add_u32 m0, s22, 65536\n"
    "s_add_u32 s84, s47, 196608\n"
    "ds_read_b64_tr_b16 a[130:131], v201 offset:36864\n"
    "ds_read_b128 a[192:195], v213 offset:32768\n"
    "v_mfma_f32_16x16x32_bf16 v[8:11], a[160:163], a[240:243], v[8:11]\n"
    "ds_read_b64_tr_b16 a[132:133], v201 offset:40960\n"
    "ds_read_b64_tr_b16 a[134:135], v201 offset:45056\n"
    "v_mfma_f32_16x16x32_bf16 v[12:15], a[176:179], a[240:243], v[12:15]\n"
    "ds_read_b128 a[196:199], v213 offset:33792\n"
    "ds_read_b128 a[204:207], v213 offset:35840\n"
    "v_mfma_f32_16x16x32_bf16 v[12:15], a[180:183], a[244:247], v[12:15]\n"
    "s_waitcnt lgkmcnt(4)\n"
    "ds_read_b128 a[200:203], v213 offset:34816\n"
    "buffer_load_dwordx4 v192, s[64:67], s84 offen offset:0 lds\n"
    "v_mfma_f32_16x16x32_bf16 v[12:15], a[184:187], a[248:251], v[12:15]\n"
    "buffer_load_dwordx4 v193, s[64:67], s84 offen offset:1024 lds\n"
    "buffer_load_dwordx4 v194, s[64:67], s84 offen offset:2048 lds\n"
    "v_mfma_f32_16x16x32_bf16 v[12:15], a[188:191], a[252:255], v[12:15]\n"
    "buffer_load_dwordx4 v195, s[64:67], s84 offen offset:3072 lds\n"
    "ds_read_b128 a[208:211], v213 offset:36864\n"
    "v_mfma_f32_16x16x32_bf16 v[4:7], a[188:191], a[236:239], v[4:7]\n"
    "ds_read_b128 a[212:215], v213 offset:37888\n"
    "ds_read_b128 a[220:223], v213 offset:39936\n"
    "v_mfma_f32_16x16x32_bf16 v[4:7], a[184:187], a[232:235], v[4:7]\n"
    "ds_read_b128 a[216:219], v213 offset:38912\n"
    "ds_read_b64_tr_b16 a[136:137], v202 offset:32768\n"
    "v_mfma_f32_16x16x32_bf16 v[4:7], a[180:183], a[228:231], v[4:7]\n"
    "ds_read_b64_tr_b16 a[138:139], v202 offset:36864\n"
    "ds_read_b64_tr_b16 a[140:141], v202 offset:40960\n"
    "v_mfma_f32_16x16x32_bf16 v[4:7], a[176:179], a[224:227], v[4:7]\n"
    "ds_read_b64_tr_b16 a[142:143], v202 offset:45056\n"
    "ds_read_b64_tr_b16 a[144:145], v201 offset:33280\n"
    "v_mfma_f32_16x16x32_bf16 v[64:67], a[128:131], a[192:195], 0\n"
    "s_waitcnt vmcnt(16) lgkmcnt(4)\n"
    "ds_read_b64_tr_b16 a[146:147], v201 offset:37376\n"
    "v_mfma_f32_16x16x32_bf16 v[64:67], a[132:135], a[196:199], v[64:67]\n"
    "s_add_u32 m0, s22, 16384\n"
    "s_add_u32 s84, s49, 8192\n"
    "s_barrier\n"
    "ds_read_b64_tr_b16 a[148:149], v201 offset:41472\n"
    "v_mfma_f32_16x16x32_bf16 v[80:83], a[132:135], a[204:207], 0\n"
    "ds_read_b64_tr_b16 a[150:151], v201 offset:45568\n"
    "v_mfma_f32_16x16x32_bf16 v[80:83], a[128:131], a[200:203], v[80:83]\n"
    "buffer_load_dwordx4 v180, s[76:79], s84 offen offset:0 sc1 lds\n"
    "v_mfma_f32_16x16x32_bf16 v[96:99], a[128:131], a[208:211], 0\n"
    "ds_read_b64_tr_b16 a[152:153], v202 offset:33280\n"
    "v_mfma_f32_16x16x32_bf16 v[96:99], a[132:135], a[212:215], v[96:99]\n"
    "ds_read_b64_tr_b16 a[154:155], v202 offset:37376\n"
    "v_mfma_f32_16x16x32_bf16 v[112:115], a[132:135], a[220:223], 0\n"
    "ds_read_b64_tr_b16 a[156:157], v202 offset:41472\n"
    "v_mfma_f32_16x16x32_bf16 v[112:115], a[128:131], a[216:219], v[112:115]\n"
    "s_waitcnt lgkmcnt(2)\n"
    "buffer_load_dwordx4 v180, s[76:79], s84 offen offset:1024 sc1 lds\n"
    "v_mfma_f32_16x16x32_bf16 v[116:119], a[136:139], a[216:219], 0\n"
    "ds_read_b64_tr_b16 a[158:159], v202 offset:45568\n"
    "v_mfma_f32_16x16x32_bf16 v[116:119], a[140:143], a[220:223], v[116:119]\n"
    "ds_read_b64_tr_b16 a[160:161], v203 offset:32768\n"
    "v_mfma_f32_16x16x32_bf16 v[100:103], a[140:143], a[212:215], 0\n"
    "ds_read_b64_tr_b16 a[162:163], v203 offset:36864\n"
    "v_mfma_f32_16x16x32_bf16 v[100:103], a[136:139], a[208:211], v[100:103]\n"
    "buffer_load_dwordx4 v200, s[76:79], s84 offen offset:2048 sc1 lds\n"
    "v_mfma_f32_16x16x32_bf16 v[84:87], a[136:139], a[200:203], 0\n"
    "ds_read_b128 a[224:227], v214 offset:32768\n"
    "v_mfma_f32_16x16x32_bf16 v[84:87], a[140:143], a[204:207], v[84:87]\n"
    "ds_read_b64_tr_b16 a[164:165], v203 offset:40960\n"
    "v_mfma_f32_16x16x32_bf16 v[68:71], a[140:143], a[196:199], 0\n"
    "ds_read_b64_tr_b16 a[166:167], v203 offset:45056\n"
    "v_mfma_f32_16x16x32_bf16 v[68:71], a[136:139], a[192:195], v[68:71]\n"
    "buffer_load_dwordx4 v200, s[76:79], s84 offen offset:3072 sc1 lds\n"
    "v_mfma_f32_16x16x32_bf16 v[72:75], a[144:147], a[192:195], 0\n"
    "ds_read_b128 a[228:231], v214 offset:33792\n"
    "v_mfma_f32_16x16x32_bf16 v[72:75], a[148:151], a[196:199], v[72:75]\n"
    "s_add_u32 m0, s22, 0\n"
    "s_add_u32 s84, s47, 262144\n"
    "ds_read_b128 a[236:239], v214 offset:35840\n"
    "v_mfma_f32_16x16x32_bf16 v[88:91], a[148:151], a[204:207], 0\n"
    "ds_read_b128 a[232:235], v214 offset:34816\n"
    "v_mfma_f32_16x16x32_bf16 v[88:91], a[144:147], a[200:203], v[88:91]\n"
    "buffer_load_dwordx4 v192, s[64:67], s84 offen offset:0 lds\n"
    "v_mfma_f32_16x16x32_bf16 v[104:107], a[144:147], a[208:211], 0\n"
    "ds_read_b128 a[240:243], v214 offset:36864\n"
    "v_mfma_f32_16x16x32_bf16 v[104:107], a[148:151], a[212:215], v[104:107]\n"
    "ds_read_b128 a[244:247], v214 offset:37888\n"
    "v_mfma_f32_16x16x32_bf16 v[120:123], a[148:151], a[220:223], 0\n"
    "ds_read_b128 a[252:255], v214 offset:39936\n"
    "v_mfma_f32_16x16x32_bf16 v[120:123], a[144:147], a[216:219], v[120:123]\n"
    "s_waitcnt lgkmcnt(2)\n"
    "buffer_load_dwordx4 v193, s[64:67], s84 offen offset:1024 lds\n"
    "v_mfma_f32_16x16x32_bf16 v[124:127], a[152:155], a[216:219], 0\n"
    "ds_read_b128 a[248:251], v214 offset:38912\n"
    "v_mfma_f32_16x16x32_bf16 v[124:127], a[156:159], a[220:223], v[124:127]\n"
    "ds_read_b64_tr_b16 a[168:169], v204 offset:32768\n"
    "v_mfma_f32_16x16x32_bf16 v[108:111], a[156:159], a[212:215], 0\n"
    "ds_read_b64_tr_b16 a[170:171], v204 offset:36864\n"
    "v_mfma_f32_16x16x32_bf16 v[108:111], a[152:155], a[208:211], v[108:111]\n"
    "buffer_load_dwordx4 v194, s[64:67], s84 offen offset:2048 lds\n"
    "v_mfma_f32_16x16x32_bf16 v[92:95], a[152:155], a[200:203], 0\n"
    "ds_read_b64_tr_b16 a[172:173], v204 offset:40960\n"
    "v_mfma_f32_16x16x32_bf16 v[92:95], a[156:159], a[204:207], v[92:95]\n"
    "ds_read_b64_tr_b16 a[174:175], v204 offset:45056\n"
    "v_mfma_f32_16x16x32_bf16 v[76:79], a[156:159], a[196:199], 0\n"
    "ds_read_b64_tr_b16 a[176:177], v203 offset:33280\n"
    "v_mfma_f32_16x16x32_bf16 v[76:79], a[152:155], a[192:195], v[76:79]\n"
    "buffer_load_dwordx4 v195, s[64:67], s84 offen offset:3072 lds\n"
    "v_mfma_f32_16x16x32_bf16 v[64:67], a[160:163], a[224:227], v[64:67]\n"
    "s_waitcnt vmcnt(16) lgkmcnt(15)\n"
    "ds_read_b64_tr_b16 a[178:179], v203 offset:37376\n"
    "v_mfma_f32_16x16x32_bf16 v[64:67], a[164:167], a[228:231], v[64:67]\n"
    "s_add_u32 m0, s22, 49152\n"
    "s_add_u32 s84, s49, 10240\n"
    "s_barrier\n"
    "ds_read_b64_tr_b16 a[180:181], v203 offset:41472\n"
    "v_mfma_f32_16x16x32_bf16 v[80:83], a[164:167], a[236:239], v[80:83]\n"
    "ds_read_b64_tr_b16 a[182:183], v203 offset:45568\n"
    "v_mfma_f32_16x16x32_bf16 v[80:83], a[160:163], a[232:235], v[80:83]\n"
    "buffer_load_dwordx4 v180, s[76:79], s84 offen offset:0 sc1 lds\n"
    "v_mfma_f32_16x16x32_bf16 v[96:99], a[160:163], a[240:243], v[96:99]\n"
    "s_waitcnt lgkmcnt(1)\n"
    "ds_read_b64_tr_b16 a[184:185], v204 offset:33280\n"
    "v_mfma_f32_16x16x32_bf16 v[96:99], a[164:167], a[244:247], v[96:99]\n"
    "ds_read_b64_tr_b16 a[186:187], v204 offset:37376\n"
    "v_mfma_f32_16x16x32_bf16 v[112:115], a[164:167], a[252:255], v[112:115]\n"
    "ds_read_b64_tr_b16 a[188:189], v204 offset:41472\n"
    "v_mfma_f32_16x16x32_bf16 v[112:115], a[160:163], a[248:251], v[112:115]\n"
    "buffer_load_dwordx4 v180, s[76:79], s84 offen offset:1024 sc1 lds\n"
    "v_mfma_f32_16x16x32_bf16 v[116:119], a[168:171], a[248:251], v[116:119]\n"
    "ds_read_b64_tr_b16 a[190:191], v204 offset:45568\n"
    "v_mfma_f32_16x16x32_bf16 v[116:119], a[172:175], a[252:255], v[116:119]\n"
    "ds_read_b64_tr_b16 a[128:129], v205 offset:0\n"
    "v_mfma_f32_16x16x32_bf16 v[100:103], a[172:175], a[244:247], v[100:103]\n"
    "ds_read_b64_tr_b16 a[130:131], v205 offset:4096\n"
    "v_mfma_f32_16x16x32_bf16 v[100:103], a[168:171], a[240:243], v[100:103]\n"
    "buffer_load_dwordx4 v200, s[76:79], s84 offen offset:2048 sc1 lds\n"
    "v_mfma_f32_16x16x32_bf16 v[84:87], a[168:171], a[232:235], v[84:87]\n"
    "ds_read_b128 a[192:195], v215 offset:0\n"
    "v_mfma_f32_16x16x32_bf16 v[84:87], a[172:175], a[236:239], v[84:87]\n"
    "ds_read_b64_tr_b16 a[132:133], v205 offset:8192\n"
    "v_mfma_f32_16x16x32_bf16 v[68:71], a[172:175], a[228:231], v[68:71]\n"
    "ds_read_b64_tr_b16 a[134:135], v205 offset:12288\n"
    "v_mfma_f32_16x16x32_bf16 v[68:71], a[168:171], a[224:227], v[68:71]\n"
    "buffer_load_dwordx4 v200, s[76:79], s84 offen offset:3072 sc1 lds\n"
    "v_mfma_f32_16x16x32_bf16 v[72:75], a[176:179], a[224:227], v[72:75]\n"
    "s_waitcnt lgkmcnt(1)\n"
    "ds_read_b128 a[196:199], v215 offset:1024\n"
    "v_mfma_f32_16x16x32_bf16 v[72:75], a[180:183], a[228:231], v[72:75]\n"
    "s_add_u32 m0, s22, 32768\n"
    "s_add_u32 s84, s47, 327680\n"
    "ds_read_b128 a[204:207], v215 offset:3072\n"
    "v_mfma_f32_16x16x32_bf16 v[88:91], a[180:183], a[236:239], v[88:91]\n"
    "ds_read_b128 a[200:203], v215 offset:2048\n"
    "v_mfma_f32_16x16x32_bf16 v[88:91], a[176:179], a[232:235], v[88:91]\n"
    "buffer_load_dwordx4 v192, s[64:67], s84 offen offset:0 lds\n"
    "v_mfma_f32_16x16x32_bf16 v[104:107], a[176:179], a[240:243], v[104:107]\n"
    "ds_read_b128 a[208:211], v215 offset:4096\n"
    "v_mfma_f32_16x16x32_bf16 v[104:107], a[180:183], a[244:247], v[104:107]\n"
    "ds_read_b128 a[212:215], v215 offset:5120\n"
    "v_mfma_f32_16x16x32_bf16 v[120:123], a[180:183], a[252:255], v[120:123]\n"
    "ds_read_b128 a[220:223], v215 offset:7168\n"
    "v_mfma_f32_16x16x32_bf16 v[120:123], a[176:179], a[248:251], v[120:123]\n"
    "buffer_load_dwordx4 v193, s[64:67], s84 offen offset:1024 lds\n"
    "v_mfma_f32_16x16x32_bf16 v[124:127], a[184:187], a[248:251], v[124:127]\n"
    "ds_read_b128 a[216:219], v215 offset:6144\n"
    "v_mfma_f32_16x16x32_bf16 v[124:127], a[188:191], a[252:255], v[124:127]\n"
    "ds_read_b64_tr_b16 a[136:137], v206 offset:0\n"
    "v_mfma_f32_16x16x32_bf16 v[108:111], a[188:191], a[244:247], v[108:111]\n"
    "ds_read_b64_tr_b16 a[138:139], v206 offset:4096\n"
    "v_mfma_f32_16x16x32_bf16 v[108:111], a[184:187], a[240:243], v[108:111]\n"
    "buffer_load_dwordx4 v194, s[64:67], s84 offen offset:2048 lds\n"
    "v_mfma_f32_16x16x32_bf16 v[92:95], a[184:187], a[232:235], v[92:95]\n"
    "ds_read_b64_tr_b16 a[140:141], v206 offset:8192\n"
    "v_mfma_f32_16x16x32_bf16 v[92:95], a[188:191], a[236:239], v[92:95]\n"
    "ds_read_b64_tr_b16 a[142:143], v206 offset:12288\n"
    "v_mfma_f32_16x16x32_bf16 v[76:79], a[188:191], a[228:231], v[76:79]\n"
    "ds_read_b64_tr_b16 a[144:145], v205 offset:512\n"
    "v_mfma_f32_16x16x32_bf16 v[76:79], a[184:187], a[224:227], v[76:79]\n"
    "buffer_load_dwordx4 v195, s[64:67], s84 offen offset:3072 lds\n"
    "v_mfma_f32_16x16x32_bf16 v[64:67], a[128:131], a[192:195], v[64:67]\n"
    "s_waitcnt vmcnt(16) lgkmcnt(1)\n"
    "ds_read_b64_tr_b16 a[146:147], v205 offset:4608\n"
    "v_mfma_f32_16x16x32_bf16 v[64:67], a[132:135], a[196:199], v[64:67]\n"
    "s_add_u32 m0, s22, 114688\n"
    "s_add_u32 s84, s49, 12288\n"
    "s_barrier\n"
    "ds_read_b64_tr_b16 a[148:149], v205 offset:8704\n"
    "v_mfma_f32_16x16x32_bf16 v[80:83], a[132:135], a[204:207], v[80:83]\n"
    "ds_read_b64_tr_b16 a[150:151], v205 offset:12800\n"
    "v_mfma_f32_16x16x32_bf16 v[80:83], a[128:131], a[200:203], v[80:83]\n"
    "buffer_load_dwordx4 v180, s[76:79], s84 offen offset:0 sc1 lds\n"
    "v_mfma_f32_16x16x32_bf16 v[96:99], a[128:131], a[208:211], v[96:99]\n"
    "ds_read_b64_tr_b16 a[152:153], v206 offset:512\n"
    "v_mfma_f32_16x16x32_bf16 v[96:99], a[132:135], a[212:215], v[96:99]\n"
    "ds_read_b64_tr_b16 a[154:155], v206 offset:4608\n"
    "v_mfma_f32_16x16x32_bf16 v[112:115], a[132:135], a[220:223], v[112:115]\n"
    "ds_read_b64_tr_b16 a[156:157], v206 offset:8704\n"
    "v_mfma_f32_16x16x32_bf16 v[112:115], a[128:131], a[216:219], v[112:115]\n"
    "buffer_load_dwordx4 v180, s[76:79], s84 offen offset:1024 sc1 lds\n"
    "v_mfma_f32_16x16x32_bf16 v[116:119], a[136:139], a[216:219], v[116:119]\n"
    "ds_read_b64_tr_b16 a[158:159], v206 offset:12800\n"
    "v_mfma_f32_16x16x32_bf16 v[116:119], a[140:143], a[220:223], v[116:119]\n"
    "ds_read_b64_tr_b16 a[160:161], v203 offset:0\n"
    "v_mfma_f32_16x16x32_bf16 v[100:103], a[140:143], a[212:215], v[100:103]\n"
    "ds_read_b64_tr_b16 a[162:163], v203 offset:4096\n"
    "v_mfma_f32_16x16x32_bf16 v[100:103], a[136:139], a[208:211], v[100:103]\n"
    "buffer_load_dwordx4 v200, s[76:79], s84 offen offset:2048 sc1 lds\n"
    "v_mfma_f32_16x16x32_bf16 v[84:87], a[136:139], a[200:203], v[84:87]\n"
    "ds_read_b128 a[224:227], v214 offset:0\n"
    "v_mfma_f32_16x16x32_bf16 v[84:87], a[140:143], a[204:207], v[84:87]\n"
    "ds_read_b64_tr_b16 a[164:165], v203 offset:8192\n"
    "v_mfma_f32_16x16x32_bf16 v[68:71], a[140:143], a[196:199], v[68:71]\n"
    "ds_read_b64_tr_b16 a[166:167], v203 offset:12288\n"
    "v_mfma_f32_16x16x32_bf16 v[68:71], a[136:139], a[192:195], v[68:71]\n"
    "s_waitcnt lgkmcnt(2)\n"
    "buffer_load_dwordx4 v200, s[76:79], s84 offen offset:3072 sc1 lds\n"
    "v_mfma_f32_16x16x32_bf16 v[72:75], a[144:147], a[192:195], v[72:75]\n"
    "ds_read_b128 a[228:231], v214 offset:1024\n"
    "v_mfma_f32_16x16x32_bf16 v[72:75], a[148:151], a[196:199], v[72:75]\n"
    "s_add_u32 m0, s22, 98304\n"
    "s_add_u32 s84, s47, 393216\n"
    "ds_read_b128 a[236:239], v214 offset:3072\n"
    "v_mfma_f32_16x16x32_bf16 v[88:91], a[148:151], a[204:207], v[88:91]\n"
    "ds_read_b128 a[232:235], v214 offset:2048\n"
    "v_mfma_f32_16x16x32_bf16 v[88:91], a[144:147], a[200:203], v[88:91]\n"
    "buffer_load_dwordx4 v192, s[64:67], s84 offen offset:0 lds\n"
    "v_mfma_f32_16x16x32_bf16 v[104:107], a[144:147], a[208:211], v[104:107]\n"
    "ds_read_b128 a[240:243], v214 offset:4096\n"
    "v_mfma_f32_16x16x32_bf16 v[104:107], a[148:151], a[212:215], v[104:107]\n"
    "ds_read_b128 a[244:247], v214 offset:5120\n"
    "v_mfma_f32_16x16x32_bf16 v[120:123], a[148:151], a[220:223], v[120:123]\n"
    "ds_read_b128 a[252:255], v214 offset:7168\n"
    "v_mfma_f32_16x16x32_bf16 v[120:123], a[144:147], a[216:219], v[120:123]\n"
    "buffer_load_dwordx4 v193, s[64:67], s84 offen offset:1024 lds\n"
    "v_mfma_f32_16x16x32_bf16 v[124:127], a[152:155], a[216:219], v[124:127]\n"
    "ds_read_b128 a[248:251], v214 offset:6144\n"
    "v_mfma_f32_16x16x32_bf16 v[124:127], a[156:159], a[220:223], v[124:127]\n"
    "ds_read_b64_tr_b16 a[168:169], v204 offset:0\n"
    "v_mfma_f32_16x16x32_bf16 v[108:111], a[156:159], a[212:215], v[108:111]\n"
    "ds_read_b64_tr_b16 a[170:171], v204 offset:4096\n"
    "v_mfma_f32_16x16x32_bf16 v[108:111], a[152:155], a[208:211], v[108:111]\n"
    "buffer_load_dwordx4 v194, s[64:67], s84 offen offset:2048 lds\n"
    "v_mfma_f32_16x16x32_bf16 v[92:95], a[152:155], a[200:203], v[92:95]\n"
    "ds_read_b64_tr_b16 a[172:173], v204 offset:8192\n"
    "v_mfma_f32_16x16x32_bf16 v[92:95], a[156:159], a[204:207], v[92:95]\n"
    "ds_read_b64_tr_b16 a[174:175], v204 offset:12288\n"
    "v_mfma_f32_16x16x32_bf16 v[76:79], a[156:159], a[196:199], v[76:79]\n"
    "ds_read_b64_tr_b16 a[176:177], v203 offset:512\n"
    "v_mfma_f32_16x16x32_bf16 v[76:79], a[152:155], a[192:195], v[76:79]\n"
    "buffer_load_dwordx4 v195, s[64:67], s84 offen offset:3072 lds\n"
    "v_mfma_f32_16x16x32_bf16 v[64:67], a[160:163], a[224:227], v[64:67]\n"
    "s_waitcnt vmcnt(16) lgkmcnt(1)\n"
    "ds_read_b64_tr_b16 a[178:179], v203 offset:4608\n"
    "v_mfma_f32_16x16x32_bf16 v[64:67], a[164:167], a[228:231], v[64:67]\n"
    "s_add_u32 m0, s22, 147456\n"
    "s_add_u32 s84, s49, 14336\n"
    "s_barrier\n"
    "ds_read_b64_tr_b16 a[180:181], v203 offset:8704\n"
    "v_mfma_f32_16x16x32_bf16 v[80:83], a[164:167], a[236:239], v[80:83]\n"
    "ds_read_b64_tr_b16 a[182:183], v203 offset:12800\n"
    "v_mfma_f32_16x16x32_bf16 v[80:83], a[160:163], a[232:235], v[80:83]\n"
    "buffer_load_dwordx4 v180, s[76:79], s84 offen offset:0 sc1 lds\n"
    "v_mfma_f32_16x16x32_bf16 v[96:99], a[160:163], a[240:243], v[96:99]\n"
    "ds_read_b64_tr_b16 a[184:185], v204 offset:512\n"
    "v_mfma_f32_16x16x32_bf16 v[96:99], a[164:167], a[244:247], v[96:99]\n"
    "ds_read_b64_tr_b16 a[186:187], v204 offset:4608\n"
    "v_mfma_f32_16x16x32_bf16 v[112:115], a[164:167], a[252:255], v[112:115]\n"
    "ds_read_b64_tr_b16 a[188:189], v204 offset:8704\n"
    "v_mfma_f32_16x16x32_bf16 v[112:115], a[160:163], a[248:251], v[112:115]\n"
    "buffer_load_dwordx4 v180, s[76:79], s84 offen offset:1024 sc1 lds\n"
    "v_mfma_f32_16x16x32_bf16 v[116:119], a[168:171], a[248:251], v[116:119]\n"
    "ds_read_b64_tr_b16 a[190:191], v204 offset:12800\n"
    "v_mfma_f32_16x16x32_bf16 v[116:119], a[172:175], a[252:255], v[116:119]\n"
    "ds_read_b64_tr_b16 a[128:129], v201 offset:0\n"
    "v_mfma_f32_16x16x32_bf16 v[100:103], a[172:175], a[244:247], v[100:103]\n"
    "ds_read_b64_tr_b16 a[130:131], v201 offset:4096\n"
    "v_mfma_f32_16x16x32_bf16 v[100:103], a[168:171], a[240:243], v[100:103]\n"
    "buffer_load_dwordx4 v200, s[76:79], s84 offen offset:2048 sc1 lds\n"
    "v_mfma_f32_16x16x32_bf16 v[84:87], a[168:171], a[232:235], v[84:87]\n"
    "ds_read_b128 a[192:195], v213 offset:0\n"
    "v_mfma_f32_16x16x32_bf16 v[84:87], a[172:175], a[236:239], v[84:87]\n"
    "ds_read_b64_tr_b16 a[132:133], v201 offset:8192\n"
    "v_mfma_f32_16x16x32_bf16 v[68:71], a[172:175], a[228:231], v[68:71]\n"
    "ds_read_b64_tr_b16 a[134:135], v201 offset:12288\n"
    "v_mfma_f32_16x16x32_bf16 v[68:71], a[168:171], a[224:227], v[68:71]\n"
    "s_waitcnt lgkmcnt(2)\n"
    "buffer_load_dwordx4 v200, s[76:79], s84 offen offset:3072 sc1 lds\n"
    "v_mfma_f32_16x16x32_bf16 v[72:75], a[176:179], a[224:227], v[72:75]\n"
    "ds_read_b128 a[196:199], v213 offset:1024\n"
    "v_mfma_f32_16x16x32_bf16 v[72:75], a[180:183], a[228:231], v[72:75]\n"
    "s_add_u32 m0, s22, 131072\n"
    "s_add_u32 s84, s47, 458752\n"
    "ds_read_b128 a[204:207], v213 offset:3072\n"
    "v_mfma_f32_16x16x32_bf16 v[88:91], a[180:183], a[236:239], v[88:91]\n"
    "ds_read_b128 a[200:203], v213 offset:2048\n"
    "v_mfma_f32_16x16x32_bf16 v[88:91], a[176:179], a[232:235], v[88:91]\n"
    "buffer_load_dwordx4 v192, s[64:67], s84 offen offset:0 lds\n"
    "v_mfma_f32_16x16x32_bf16 v[104:107], a[176:179], a[240:243], v[104:107]\n"
    "ds_read_b128 a[208:211], v213 offset:4096\n"
    "v_mfma_f32_16x16x32_bf16 v[104:107], a[180:183], a[244:247], v[104:107]\n"
    "ds_read_b128 a[212:215], v213 offset:5120\n"
    "v_mfma_f32_16x16x32_bf16 v[120:123], a[180:183], a[252:255], v[120:123]\n"
    "ds_read_b128 a[220:223], v213 offset:7168\n"
    "v_mfma_f32_16x16x32_bf16 v[120:123], a[176:179], a[248:251], v[120:123]\n"
    "buffer_load_dwordx4 v193, s[64:67], s84 offen offset:1024 lds\n"
    "v_mfma_f32_16x16x32_bf16 v[124:127], a[184:187], a[248:251], v[124:127]\n"
    "ds_read_b128 a[216:219], v213 offset:6144\n"
    "v_mfma_f32_16x16x32_bf16 v[124:127], a[188:191], a[252:255], v[124:127]\n"
    "ds_read_b64_tr_b16 a[136:137], v202 offset:0\n"
    "v_mfma_f32_16x16x32_bf16 v[108:111], a[188:191], a[244:247], v[108:111]\n"
    "ds_read_b64_tr_b16 a[138:139], v202 offset:4096\n"
    "v_mfma_f32_16x16x32_bf16 v[108:111], a[184:187], a[240:243], v[108:111]\n"
    "buffer_load_dwordx4 v194, s[64:67], s84 offen offset:2048 lds\n"
    "v_mfma_f32_16x16x32_bf16 v[92:95], a[184:187], a[232:235], v[92:95]\n"
    "ds_read_b64_tr_b16 a[140:141], v202 offset:8192\n"
    "v_mfma_f32_16x16x32_bf16 v[92:95], a[188:191], a[236:239], v[92:95]\n"
    "ds_read_b64_tr_b16 a[142:143], v202 offset:12288\n"
    "v_mfma_f32_16x16x32_bf16 v[76:79], a[188:191], a[228:231], v[76:79]\n"
    "ds_read_b64_tr_b16 a[144:145], v201 offset:512\n"
    "v_mfma_f32_16x16x32_bf16 v[76:79], a[184:187], a[224:227], v[76:79]\n"
    "buffer_load_dwordx4 v195, s[64:67], s84 offen offset:3072 lds\n"
    "v_mfma_f32_16x16x32_bf16 v[64:67], a[128:131], a[192:195], v[64:67]\n"
    "s_waitcnt vmcnt(16) lgkmcnt(1)\n"
    "ds_read_b64_tr_b16 a[146:147], v201 offset:4608\n"
    "v_mfma_f32_16x16x32_bf16 v[64:67], a[132:135], a[196:199], v[64:67]\n"
    "s_add_u32 m0, s22, 81920\n"
    "s_add_u32 s84, s49, 16384\n"
    "s_barrier\n"
    "ds_read_b64_tr_b16 a[148:149], v201 offset:8704\n"
    "v_mfma_f32_16x16x32_bf16 v[80:83], a[132:135], a[204:207], v[80:83]\n"
    "ds_read_b64_tr_b16 a[150:151], v201 offset:12800\n"
    "v_mfma_f32_16x16x32_bf16 v[80:83], a[128:131], a[200:203], v[80:83]\n"
    "buffer_load_dwordx4 v180, s[76:79], s84 offen offset:0 sc1 lds\n"
    "v_mfma_f32_16x16x32_bf16 v[96:99], a[128:131], a[208:211], v[96:99]\n"
    "ds_read_b64_tr_b16 a[152:153], v202 offset:512\n"
    "v_mfma_f32_16x16x32_bf16 v[96:99], a[132:135], a[212:215], v[96:99]\n"
    "ds_read_b64_tr_b16 a[154:155], v202 offset:4608\n"
    "v_mfma_f32_16x16x32_bf16 v[112:115], a[132:135], a[220:223], v[112:115]\n"
    "ds_read_b64_tr_b16 a[156:157], v202 offset:8704\n"
    "v_mfma_f32_16x16x32_bf16 v[112:115], a[128:131], a[216:219], v[112:115]\n"
    "buffer_load_dwordx4 v180, s[76:79], s84 offen offset:1024 sc1 lds\n"
    "v_mfma_f32_16x16x32_bf16 v[116:119], a[136:139], a[216:219], v[116:119]\n"
    "ds_read_b64_tr_b16 a[158:159], v202 offset:12800\n"
    "v_mfma_f32_16x16x32_bf16 v[116:119], a[140:143], a[220:223], v[116:119]\n"
    "ds_read_b64_tr_b16 a[160:161], v201 offset:32768\n"
    "v_mfma_f32_16x16x32_bf16 v[100:103], a[140:143], a[212:215], v[100:103]\n"
    "ds_read_b64_tr_b16 a[162:163], v201 offset:36864\n"
    "v_mfma_f32_16x16x32_bf16 v[100:103], a[136:139], a[208:211], v[100:103]\n"
    "buffer_load_dwordx4 v200, s[76:79], s84 offen offset:2048 sc1 lds\n"
    "v_mfma_f32_16x16x32_bf16 v[84:87], a[136:139], a[200:203], v[84:87]\n"
    "ds_read_b128 a[224:227], v213 offset:32768\n"
    "v_mfma_f32_16x16x32_bf16 v[84:87], a[140:143], a[204:207], v[84:87]\n"
    "ds_read_b64_tr_b16 a[164:165], v201 offset:40960\n"
    "v_mfma_f32_16x16x32_bf16 v[68:71], a[140:143], a[196:199], v[68:71]\n"
    "ds_read_b64_tr_b16 a[166:167], v201 offset:45056\n"
    "v_mfma_f32_16x16x32_bf16 v[68:71], a[136:139], a[192:195], v[68:71]\n"
    "s_waitcnt lgkmcnt(2)\n"
    "buffer_load_dwordx4 v200, s[76:79], s84 offen offset:3072 sc1 lds\n"
    "v_mfma_f32_16x16x32_bf16 v[72:75], a[144:147], a[192:195], v[72:75]\n"
    "ds_read_b128 a[228:231], v213 offset:33792\n"
    "v_mfma_f32_16x16x32_bf16 v[72:75], a[148:151], a[196:199], v[72:75]\n"
    "s_add_u32 m0, s22, 65536\n"
    "s_add_u32 s84, s47, 524288\n"
    "ds_read_b128 a[236:239], v213 offset:35840\n"
    "v_mfma_f32_16x16x32_bf16 v[88:91], a[148:151], a[204:207], v[88:91]\n"
    "ds_read_b128 a[232:235], v213 offset:34816\n"
    "v_mfma_f32_16x16x32_bf16 v[88:91], a[144:147], a[200:203], v[88:91]\n"
    "buffer_load_dwordx4 v192, s[64:67], s84 offen offset:0 lds\n"
    "v_mfma_f32_16x16x32_bf16 v[104:107], a[144:147], a[208:211], v[104:107]\n"
    "ds_read_b128 a[240:243], v213 offset:36864\n"
    "v_mfma_f32_16x16x32_bf16 v[104:107], a[148:151], a[212:215], v[104:107]\n"
    "ds_read_b128 a[244:247], v213 offset:37888\n"
    "v_mfma_f32_16x16x32_bf16 v[120:123], a[148:151], a[220:223], v[120:123]\n"
    "ds_read_b128 a[252:255], v213 offset:39936\n"
    "v_mfma_f32_16x16x32_bf16 v[120:123], a[144:147], a[216:219], v[120:123]\n"
    "buffer_load_dwordx4 v193, s[64:67], s84 offen offset:1024 lds\n"
    "v_mfma_f32_16x16x32_bf16 v[124:127], a[152:155], a[216:219], v[124:127]\n"
    "ds_read_b128 a[248:251], v213 offset:38912\n"
    "v_mfma_f32_16x16x32_bf16 v[124:127], a[156:159], a[220:223], v[124:127]\n"
    "ds_read_b64_tr_b16 a[168:169], v202 offset:32768\n"
    "v_mfma_f32_16x16x32_bf16 v[108:111], a[156:159], a[212:215], v[108:111]\n"
    "ds_read_b64_tr_b16 a[170:171], v202 offset:36864\n"
    "v_mfma_f32_16x16x32_bf16 v[108:111], a[152:155], a[208:211], v[108:111]\n"
    "buffer_load_dwordx4 v194, s[64:67], s84 offen offset:2048 lds\n"
    "v_mfma_f32_16x16x32_bf16 v[92:95], a[152:155], a[200:203], v[92:95]\n"
    "ds_read_b64_tr_b16 a[172:173], v202 offset:40960\n"
    "v_mfma_f32_16x16x32_bf16 v[92:95], a[156:159], a[204:207], v[92:95]\n"
    "ds_read_b64_tr_b16 a[174:175], v202 offset:45056\n"
    "v_mfma_f32_16x16x32_bf16 v[76:79], a[156:159], a[196:199], v[76:79]\n"
    "ds_read_b64_tr_b16 a[176:177], v201 offset:33280\n"
    "v_mfma_f32_16x16x32_bf16 v[76:79], a[152:155], a[192:195], v[76:79]\n"
    "buffer_load_dwordx4 v195, s[64:67], s84 offen offset:3072 lds\n"
    "v_mfma_f32_16x16x32_bf16 v[64:67], a[160:163], a[224:227], v[64:67]\n"
    "s_waitcnt vmcnt(16) lgkmcnt(1)\n"
    "ds_read_b64_tr_b16 a[178:179], v201 offset:37376\n"
    "v_mfma_f32_16x16x32_bf16 v[64:67], a[164:167], a[228:231], v[64:67]\n"
    "s_add_u32 m0, s22, 16384\n"
    "s_add_u32 s84, s49, 18432\n"
    "s_barrier\n"
    "ds_read_b64_tr_b16 a[180:181], v201 offset:41472\n"
    "v_mfma_f32_16x16x32_bf16 v[80:83], a[164:167], a[236:239], v[80:83]\n"
    "ds_read_b64_tr_b16 a[182:183], v201 offset:45568\n"
    "v_mfma_f32_16x16x32_bf16 v[80:83], a[160:163], a[232:235], v[80:83]\n"
    "buffer_load_dwordx4 v180, s[76:79], s84 offen offset:0 sc1 lds\n"
    "v_mfma_f32_16x16x32_bf16 v[96:99], a[160:163], a[240:243], v[96:99]\n"
    "ds_read_b64_tr_b16 a[184:185], v202 offset:33280\n"
    "v_mfma_f32_16x16x32_bf16 v[96:99], a[164:167], a[244:247], v[96:99]\n"
    "ds_read_b64_tr_b16 a[186:187], v202 offset:37376\n"
    "v_mfma_f32_16x16x32_bf16 v[112:115], a[164:167], a[252:255], v[112:115]\n"
    "ds_read_b64_tr_b16 a[188:189], v202 offset:41472\n"
    "v_mfma_f32_16x16x32_bf16 v[112:115], a[160:163], a[248:251], v[112:115]\n"
    "buffer_load_dwordx4 v180, s[76:79], s84 offen offset:1024 sc1 lds\n"
    "v_mfma_f32_16x16x32_bf16 v[116:119], a[168:171], a[248:251], v[116:119]\n"
    "ds_read_b64_tr_b16 a[190:191], v202 offset:45568\n"
    "v_mfma_f32_16x16x32_bf16 v[116:119], a[172:175], a[252:255], v[116:119]\n"
    "ds_read_b64_tr_b16 a[128:129], v203 offset:32768\n"
    "v_mfma_f32_16x16x32_bf16 v[100:103], a[172:175], a[244:247], v[100:103]\n"
    "ds_read_b64_tr_b16 a[130:131], v203 offset:36864\n"
    "v_mfma_f32_16x16x32_bf16 v[100:103], a[168:171], a[240:243], v[100:103]\n"
    "buffer_load_dwordx4 v200, s[76:79], s84 offen offset:2048 sc1 lds\n"
    "v_mfma_f32_16x16x32_bf16 v[84:87], a[168:171], a[232:235], v[84:87]\n"
    "ds_read_b128 a[192:195], v214 offset:32768\n"
    "v_mfma_f32_16x16x32_bf16 v[84:87], a[172:175], a[236:239], v[84:87]\n"
    "ds_read_b64_tr_b16 a[132:133], v203 offset:40960\n"
    "v_mfma_f32_16x16x32_bf16 v[68:71], a[172:175], a[228:231], v[68:71]\n"
    "ds_read_b64_tr_b16 a[134:135], v203 offset:45056\n"
    "v_mfma_f32_16x16x32_bf16 v[68:71], a[168:171], a[224:227], v[68:71]\n"
    "s_waitcnt lgkmcnt(2)\n"
    "buffer_load_dwordx4 v200, s[76:79], s84 offen offset:3072 sc1 lds\n"
    "v_mfma_f32_16x16x32_bf16 v[72:75], a[176:179], a[224:227], v[72:75]\n"
    "ds_read_b128 a[196:199], v214 offset:33792\n"
    "v_mfma_f32_16x16x32_bf16 v[72:75], a[180:183], a[228:231], v[72:75]\n"
    "s_add_u32 m0, s22, 0\n"
    "s_add_u32 s84, s47, 589824\n"
    "ds_read_b128 a[204:207], v214 offset:35840\n"
    "v_mfma_f32_16x16x32_bf16 v[88:91], a[180:183], a[236:239], v[88:91]\n"
    "ds_read_b128 a[200:203], v214 offset:34816\n"
    "v_mfma_f32_16x16x32_bf16 v[88:91], a[176:179], a[232:235], v[88:91]\n"
    "buffer_load_dwordx4 v192, s[64:67], s84 offen offset:0 lds\n"
    "v_mfma_f32_16x16x32_bf16 v[104:107], a[176:179], a[240:243], v[104:107]\n"
    "ds_read_b128 a[208:211], v214 offset:36864\n"
    "v_mfma_f32_16x16x32_bf16 v[104:107], a[180:183], a[244:247], v[104:107]\n"
    "ds_read_b128 a[212:215], v214 offset:37888\n"
    "v_mfma_f32_16x16x32_bf16 v[120:123], a[180:183], a[252:255], v[120:123]\n"
    "ds_read_b128 a[220:223], v214 offset:39936\n"
    "v_mfma_f32_16x16x32_bf16 v[120:123], a[176:179], a[248:251], v[120:123]\n"
    "buffer_load_dwordx4 v193, s[64:67], s84 offen offset:1024 lds\n"
    "v_mfma_f32_16x16x32_bf16 v[124:127], a[184:187], a[248:251], v[124:127]\n"
    "ds_read_b128 a[216:219], v214 offset:38912\n"
    "v_mfma_f32_16x16x32_bf16 v[124:127], a[188:191], a[252:255], v[124:127]\n"
    "ds_read_b64_tr_b16 a[136:137], v204 offset:32768\n"
    "v_mfma_f32_16x16x32_bf16 v[108:111], a[188:191], a[244:247], v[108:111]\n"
    "ds_read_b64_tr_b16 a[138:139], v204 offset:36864\n"
    "v_mfma_f32_16x16x32_bf16 v[108:111], a[184:187], a[240:243], v[108:111]\n"
    "buffer_load_dwordx4 v194, s[64:67], s84 offen offset:2048 lds\n"
    "v_mfma_f32_16x16x32_bf16 v[92:95], a[184:187], a[232:235], v[92:95]\n"
    "ds_read_b64_tr_b16 a[140:141], v204 offset:40960\n"
    "v_mfma_f32_16x16x32_bf16 v[92:95], a[188:191], a[236:239], v[92:95]\n"
    "ds_read_b64_tr_b16 a[142:143], v204 offset:45056\n"
    "v_mfma_f32_16x16x32_bf16 v[76:79], a[188:191], a[228:231], v[76:79]\n"
    "ds_read_b64_tr_b16 a[144:145], v203 offset:33280\n"
    "v_mfma_f32_16x16x32_bf16 v[76:79], a[184:187], a[224:227], v[76:79]\n"
    "buffer_load_dwordx4 v195, s[64:67], s84 offen offset:3072 lds\n"
    "v_mfma_f32_16x16x32_bf16 v[64:67], a[128:131], a[192:195], v[64:67]\n"
    "s_waitcnt vmcnt(16) lgkmcnt(1)\n"
    "ds_read_b64_tr_b16 a[146:147], v203 offset:37376\n"
    "v_mfma_f32_16x16x32_bf16 v[64:67], a[132:135], a[196:199], v[64:67]\n"
    "s_add_u32 m0, s22, 49152\n"
    "s_add_u32 s84, s49, 20480\n"
    "s_barrier\n"
    "ds_read_b64_tr_b16 a[148:149], v203 offset:41472\n"
    "v_mfma_f32_16x16x32_bf16 v[80:83], a[132:135], a[204:207], v[80:83]\n"
    "ds_read_b64_tr_b16 a[150:151], v203 offset:45568\n"
    "v_mfma_f32_16x16x32_bf16 v[80:83], a[128:131], a[200:203], v[80:83]\n"
    "buffer_load_dwordx4 v180, s[76:79], s84 offen offset:0 sc1 lds\n"
    "v_mfma_f32_16x16x32_bf16 v[96:99], a[128:131], a[208:211], v[96:99]\n"
    "ds_read_b64_tr_b16 a[152:153], v204 offset:33280\n"
    "v_mfma_f32_16x16x32_bf16 v[96:99], a[132:135], a[212:215], v[96:99]\n"
    "ds_read_b64_tr_b16 a[154:155], v204 offset:37376\n"
    "v_mfma_f32_16x16x32_bf16 v[112:115], a[132:135], a[220:223], v[112:115]\n"
    "ds_read_b64_tr_b16 a[156:157], v204 offset:41472\n"
    "v_mfma_f32_16x16x32_bf16 v[112:115], a[128:131], a[216:219], v[112:115]\n"
    "buffer_load_dwordx4 v180, s[76:79], s84 offen offset:1024 sc1 lds\n"
    "v_mfma_f32_16x16x32_bf16 v[116:119], a[136:139], a[216:219], v[116:119]\n"
    "ds_read_b64_tr_b16 a[158:159], v204 offset:45568\n"
    "v_mfma_f32_16x16x32_bf16 v[116:119], a[140:143], a[220:223], v[116:119]\n"
    "ds_read_b64_tr_b16 a[160:161], v205 offset:0\n"
    "v_mfma_f32_16x16x32_bf16 v[100:103], a[140:143], a[212:215], v[100:103]\n"
    "ds_read_b64_tr_b16 a[162:163], v205 offset:4096\n"
    "v_mfma_f32_16x16x32_bf16 v[100:103], a[136:139], a[208:211], v[100:103]\n"
    "buffer_load_dwordx4 v200, s[76:79], s84 offen offset:2048 sc1 lds\n"
    "v_mfma_f32_16x16x32_bf16 v[84:87], a[136:139], a[200:203], v[84:87]\n"
    "ds_read_b128 a[224:227], v215 offset:0\n"
    "v_mfma_f32_16x16x32_bf16 v[84:87], a[140:143], a[204:207], v[84:87]\n"
    "ds_read_b64_tr_b16 a[164:165], v205 offset:8192\n"
    "v_mfma_f32_16x16x32_bf16 v[68:71], a[140:143], a[196:199], v[68:71]\n"
    "ds_read_b64_tr_b16 a[166:167], v205 offset:12288\n"
    "v_mfma_f32_16x16x32_bf16 v[68:71], a[136:139], a[192:195], v[68:71]\n"
    "s_waitcnt lgkmcnt(2)\n"
    "buffer_load_dwordx4 v200, s[76:79], s84 offen offset:3072 sc1 lds\n"
    "v_mfma_f32_16x16x32_bf16 v[72:75], a[144:147], a[192:195], v[72:75]\n"
    "ds_read_b128 a[228:231], v215 offset:1024\n"
    "v_mfma_f32_16x16x32_bf16 v[72:75], a[148:151], a[196:199], v[72:75]\n"
    "s_add_u32 m0, s22, 32768\n"
    "s_add_u32 s84, s47, 655360\n"
    "ds_read_b128 a[236:239], v215 offset:3072\n"
    "v_mfma_f32_16x16x32_bf16 v[88:91], a[148:151], a[204:207], v[88:91]\n"
    "ds_read_b128 a[232:235], v215 offset:2048\n"
    "v_mfma_f32_16x16x32_bf16 v[88:91], a[144:147], a[200:203], v[88:91]\n"
    "buffer_load_dwordx4 v192, s[64:67], s84 offen offset:0 lds\n"
    "v_mfma_f32_16x16x32_bf16 v[104:107], a[144:147], a[208:211], v[104:107]\n"
    "ds_read_b128 a[240:243], v215 offset:4096\n"
    "v_mfma_f32_16x16x32_bf16 v[104:107], a[148:151], a[212:215], v[104:107]\n"
    "ds_read_b128 a[244:247], v215 offset:5120\n"
    "v_mfma_f32_16x16x32_bf16 v[120:123], a[148:151], a[220:223], v[120:123]\n"
    "ds_read_b128 a[252:255], v215 offset:7168\n"
    "v_mfma_f32_16x16x32_bf16 v[120:123], a[144:147], a[216:219], v[120:123]\n"
    "buffer_load_dwordx4 v193, s[64:67], s84 offen offset:1024 lds\n"
    "v_mfma_f32_16x16x32_bf16 v[124:127], a[152:155], a[216:219], v[124:127]\n"
    "ds_read_b128 a[248:251], v215 offset:6144\n"
    "v_mfma_f32_16x16x32_bf16 v[124:127], a[156:159], a[220:223], v[124:127]\n"
    "ds_read_b64_tr_b16 a[168:169], v206 offset:0\n"
    "v_mfma_f32_16x16x32_bf16 v[108:111], a[156:159], a[212:215], v[108:111]\n"
    "ds_read_b64_tr_b16 a[170:171], v206 offset:4096\n"
    "v_mfma_f32_16x16x32_bf16 v[108:111], a[152:155], a[208:211], v[108:111]\n"
    "buffer_load_dwordx4 v194, s[64:67], s84 offen offset:2048 lds\n"
    "v_mfma_f32_16x16x32_bf16 v[92:95], a[152:155], a[200:203], v[92:95]\n"
    "ds_read_b64_tr_b16 a[172:173], v206 offset:8192\n"
    "v_mfma_f32_16x16x32_bf16 v[92:95], a[156:159], a[204:207], v[92:95]\n"
    "ds_read_b64_tr_b16 a[174:175], v206 offset:12288\n"
    "v_mfma_f32_16x16x32_bf16 v[76:79], a[156:159], a[196:199], v[76:79]\n"
    "ds_read_b64_tr_b16 a[176:177], v205 offset:512\n"
    "v_mfma_f32_16x16x32_bf16 v[76:79], a[152:155], a[192:195], v[76:79]\n"
    "buffer_load_dwordx4 v195, s[64:67], s84 offen offset:3072 lds\n"
    "v_mfma_f32_16x16x32_bf16 v[64:67], a[160:163], a[224:227], v[64:67]\n"
    "s_waitcnt vmcnt(16) lgkmcnt(1)\n"
    "ds_read_b64_tr_b16 a[178:179], v205 offset:4608\n"
    "v_mfma_f32_16x16x32_bf16 v[64:67], a[164:167], a[228:231], v[64:67]\n"
    "s_add_u32 m0, s22, 114688\n"
    "s_add_u32 s84, s49, 22528\n"
    "s_barrier\n"
    "ds_read_b64_tr_b16 a[180:181], v205 offset:8704\n"
    "v_mfma_f32_16x16x32_bf16 v[80:83], a[164:167], a[236:239], v[80:83]\n"
    "ds_read_b64_tr_b16 a[182:183], v205 offset:12800\n"
    "v_mfma_f32_16x16x32_bf16 v[80:83], a[160:163], a[232:235], v[80:83]\n"
    "buffer_load_dwordx4 v180, s[76:79], s84 offen offset:0 sc1 lds\n"
    "v_mfma_f32_16x16x32_bf16 v[96:99], a[160:163], a[240:243], v[96:99]\n"
    "ds_read_b64_tr_b16 a[184:185], v206 offset:512\n"
    "v_mfma_f32_16x16x32_bf16 v[96:99], a[164:167], a[244:247], v[96:99]\n"
    "ds_read_b64_tr_b16 a[186:187], v206 offset:4608\n"
    "v_mfma_f32_16x16x32_bf16 v[112:115], a[164:167], a[252:255], v[112:115]\n"
    "ds_read_b64_tr_b16 a[188:189], v206 offset:8704\n"
    "v_mfma_f32_16x16x32_bf16 v[112:115], a[160:163], a[248:251], v[112:115]\n"
    "buffer_load_dwordx4 v180, s[76:79], s84 offen offset:1024 sc1 lds\n"
    "v_mfma_f32_16x16x32_bf16 v[116:119], a[168:171], a[248:251], v[116:119]\n"
    "ds_read_b64_tr_b16 a[190:191], v206 offset:12800\n"
    "v_mfma_f32_16x16x32_bf16 v[116:119], a[172:175], a[252:255], v[116:119]\n"
    "ds_read_b64_tr_b16 a[128:129], v203 offset:0\n"
    "v_mfma_f32_16x16x32_bf16 v[100:103], a[172:175], a[244:247], v[100:103]\n"
    "ds_read_b64_tr_b16 a[130:131], v203 offset:4096\n"
    "v_mfma_f32_16x16x32_bf16 v[100:103], a[168:171], a[240:243], v[100:103]\n"
    "buffer_load_dwordx4 v200, s[76:79], s84 offen offset:2048 sc1 lds\n"
    "v_mfma_f32_16x16x32_bf16 v[84:87], a[168:171], a[232:235], v[84:87]\n"
    "ds_read_b128 a[192:195], v214 offset:0\n"
    "v_mfma_f32_16x16x32_bf16 v[84:87], a[172:175], a[236:239], v[84:87]\n"
    "ds_read_b64_tr_b16 a[132:133], v203 offset:8192\n"
    "v_mfma_f32_16x16x32_bf16 v[68:71], a[172:175], a[228:231], v[68:71]\n"
    "ds_read_b64_tr_b16 a[134:135], v203 offset:12288\n"
    "v_mfma_f32_16x16x32_bf16 v[68:71], a[168:171], a[224:227], v[68:71]\n"
    "s_waitcnt lgkmcnt(2)\n"
    "buffer_load_dwordx4 v200, s[76:79], s84 offen offset:3072 sc1 lds\n"
    "v_mfma_f32_16x16x32_bf16 v[72:75], a[176:179], a[224:227], v[72:75]\n"
    "ds_read_b128 a[196:199], v214 offset:1024\n"
    "v_mfma_f32_16x16x32_bf16 v[72:75], a[180:183], a[228:231], v[72:75]\n"
    "s_add_u32 m0, s22, 98304\n"
    "s_add_u32 s84, s47, 720896\n"
    "ds_read_b128 a[204:207], v214 offset:3072\n"
    "v_mfma_f32_16x16x32_bf16 v[88:91], a[180:183], a[236:239], v[88:91]\n"
    "ds_read_b128 a[200:203], v214 offset:2048\n"
    "v_mfma_f32_16x16x32_bf16 v[88:91], a[176:179], a[232:235], v[88:91]\n"
    "buffer_load_dwordx4 v192, s[64:67], s84 offen offset:0 lds\n"
    "v_mfma_f32_16x16x32_bf16 v[104:107], a[176:179], a[240:243], v[104:107]\n"
    "ds_read_b128 a[208:211], v214 offset:4096\n"
    "v_mfma_f32_16x16x32_bf16 v[104:107], a[180:183], a[244:247], v[104:107]\n"
    "ds_read_b128 a[212:215], v214 offset:5120\n"
    "v_mfma_f32_16x16x32_bf16 v[120:123], a[180:183], a[252:255], v[120:123]\n"
    "ds_read_b128 a[220:223], v214 offset:7168\n"
    "v_mfma_f32_16x16x32_bf16 v[120:123], a[176:179], a[248:251], v[120:123]\n"
    "buffer_load_dwordx4 v193, s[64:67], s84 offen offset:1024 lds\n"
    "v_mfma_f32_16x16x32_bf16 v[124:127], a[184:187], a[248:251], v[124:127]\n"
    "ds_read_b128 a[216:219], v214 offset:6144\n"
    "v_mfma_f32_16x16x32_bf16 v[124:127], a[188:191], a[252:255], v[124:127]\n"
    "ds_read_b64_tr_b16 a[136:137], v204 offset:0\n"
    "v_mfma_f32_16x16x32_bf16 v[108:111], a[188:191], a[244:247], v[108:111]\n"
    "ds_read_b64_tr_b16 a[138:139], v204 offset:4096\n"
    "v_mfma_f32_16x16x32_bf16 v[108:111], a[184:187], a[240:243], v[108:111]\n"
    "buffer_load_dwordx4 v194, s[64:67], s84 offen offset:2048 lds\n"
    "v_mfma_f32_16x16x32_bf16 v[92:95], a[184:187], a[232:235], v[92:95]\n"
    "ds_read_b64_tr_b16 a[140:141], v204 offset:8192\n"
    "v_mfma_f32_16x16x32_bf16 v[92:95], a[188:191], a[236:239], v[92:95]\n"
    "ds_read_b64_tr_b16 a[142:143], v204 offset:12288\n"
    "v_mfma_f32_16x16x32_bf16 v[76:79], a[188:191], a[228:231], v[76:79]\n"
    "ds_read_b64_tr_b16 a[144:145], v203 offset:512\n"
    "v_mfma_f32_16x16x32_bf16 v[76:79], a[184:187], a[224:227], v[76:79]\n"
    "buffer_load_dwordx4 v195, s[64:67], s84 offen offset:3072 lds\n"
    "v_mfma_f32_16x16x32_bf16 v[64:67], a[128:131], a[192:195], v[64:67]\n"
    "s_waitcnt vmcnt(16) lgkmcnt(1)\n"
    "ds_read_b64_tr_b16 a[146:147], v203 offset:4608\n"
    "v_mfma_f32_16x16x32_bf16 v[64:67], a[132:135], a[196:199], v[64:67]\n"
    "s_add_u32 m0, s22, 147456\n"
    "s_add_u32 s84, s49, 24576\n"
    "s_barrier\n"
    "ds_read_b64_tr_b16 a[148:149], v203 offset:8704\n"
    "v_mfma_f32_16x16x32_bf16 v[80:83], a[132:135], a[204:207], v[80:83]\n"
    "ds_read_b64_tr_b16 a[150:151], v203 offset:12800\n"
    "v_mfma_f32_16x16x32_bf16 v[80:83], a[128:131], a[200:203], v[80:83]\n"
    "buffer_load_dwordx4 v180, s[76:79], s84 offen offset:0 sc1 lds\n"
    "v_mfma_f32_16x16x32_bf16 v[96:99], a[128:131], a[208:211], v[96:99]\n"
    "ds_read_b64_tr_b16 a[152:153], v204 offset:512\n"
    "v_mfma_f32_16x16x32_bf16 v[96:99], a[132:135], a[212:215], v[96:99]\n"
    "ds_read_b64_tr_b16 a[154:155], v204 offset:4608\n"
    "v_mfma_f32_16x16x32_bf16 v[112:115], a[132:135], a[220:223], v[112:115]\n"
    "ds_read_b64_tr_b16 a[156:157], v204 offset:8704\n"
    "v_mfma_f32_16x16x32_bf16 v[112:115], a[128:131], a[216:219], v[112:115]\n"
    "buffer_load_dwordx4 v180, s[76:79], s84 offen offset:1024 sc1 lds\n"
    "v_mfma_f32_16x16x32_bf16 v[116:119], a[136:139], a[216:219], v[116:119]\n"
    "ds_read_b64_tr_b16 a[158:159], v204 offset:12800\n"
    "v_mfma_f32_16x16x32_bf16 v[116:119], a[140:143], a[220:223], v[116:119]\n"
    "ds_read_b64_tr_b16 a[160:161], v201 offset:0\n"
    "v_mfma_f32_16x16x32_bf16 v[100:103], a[140:143], a[212:215], v[100:103]\n"
    "ds_read_b64_tr_b16 a[162:163], v201 offset:4096\n"
    "v_mfma_f32_16x16x32_bf16 v[100:103], a[136:139], a[208:211], v[100:103]\n"
    "buffer_load_dwordx4 v200, s[76:79], s84 offen offset:2048 sc1 lds\n"
    "v_mfma_f32_16x16x32_bf16 v[84:87], a[136:139], a[200:203], v[84:87]\n"
    "ds_read_b128 a[224:227], v213 offset:0\n"
    "v_mfma_f32_16x16x32_bf16 v[84:87], a[140:143], a[204:207], v[84:87]\n"
    "ds_read_b64_tr_b16 a[164:165], v201 offset:8192\n"
    "v_mfma_f32_16x16x32_bf16 v[68:71], a[140:143], a[196:199], v[68:71]\n"
    "ds_read_b64_tr_b16 a[166:167], v201 offset:12288\n"
    "v_mfma_f32_16x16x32_bf16 v[68:71], a[136:139], a[192:195], v[68:71]\n"
    "s_waitcnt lgkmcnt(2)\n"
    "buffer_load_dwordx4 v200, s[76:79], s84 offen offset:3072 sc1 lds\n"
    "v_mfma_f32_16x16x32_bf16 v[72:75], a[144:147], a[192:195], v[72:75]\n"
    "ds_read_b128 a[228:231], v213 offset:1024\n"
    "v_mfma_f32_16x16x32_bf16 v[72:75], a[148:151], a[196:199], v[72:75]\n"
    "s_add_u32 m0, s22, 131072\n"
    "s_add_u32 s84, s47, 786432\n"
    "ds_read_b128 a[236:239], v213 offset:3072\n"
    "v_mfma_f32_16x16x32_bf16 v[88:91], a[148:151], a[204:207], v[88:91]\n"
    "ds_read_b128 a[232:235], v213 offset:2048\n"
    "v_mfma_f32_16x16x32_bf16 v[88:91], a[144:147], a[200:203], v[88:91]\n"
    "buffer_load_dwordx4 v192, s[64:67], s84 offen offset:0 lds\n"
    "v_mfma_f32_16x16x32_bf16 v[104:107], a[144:147], a[208:211], v[104:107]\n"
    "ds_read_b128 a[240:243], v213 offset:4096\n"
    "v_mfma_f32_16x16x32_bf16 v[104:107], a[148:151], a[212:215], v[104:107]\n"
    "ds_read_b128 a[244:247], v213 offset:5120\n"
    "v_mfma_f32_16x16x32_bf16 v[120:123], a[148:151], a[220:223], v[120:123]\n"
    "ds_read_b128 a[252:255], v213 offset:7168\n"
    "v_mfma_f32_16x16x32_bf16 v[120:123], a[144:147], a[216:219], v[120:123]\n"
    "buffer_load_dwordx4 v193, s[64:67], s84 offen offset:1024 lds\n"
    "v_mfma_f32_16x16x32_bf16 v[124:127], a[152:155], a[216:219], v[124:127]\n"
    "ds_read_b128 a[248:251], v213 offset:6144\n"
    "v_mfma_f32_16x16x32_bf16 v[124:127], a[156:159], a[220:223], v[124:127]\n"
    "ds_read_b64_tr_b16 a[168:169], v202 offset:0\n"
    "v_mfma_f32_16x16x32_bf16 v[108:111], a[156:159], a[212:215], v[108:111]\n"
    "ds_read_b64_tr_b16 a[170:171], v202 offset:4096\n"
    "v_mfma_f32_16x16x32_bf16 v[108:111], a[152:155], a[208:211], v[108:111]\n"
    "buffer_load_dwordx4 v194, s[64:67], s84 offen offset:2048 lds\n"
    "v_mfma_f32_16x16x32_bf16 v[92:95], a[152:155], a[200:203], v[92:95]\n"
    "ds_read_b64_tr_b16 a[172:173], v202 offset:8192\n"
    "v_mfma_f32_16x16x32_bf16 v[92:95], a[156:159], a[204:207], v[92:95]\n"
    "ds_read_b64_tr_b16 a[174:175], v202 offset:12288\n"
    "v_mfma_f32_16x16x32_bf16 v[76:79], a[156:159], a[196:199], v[76:79]\n"
    "ds_read_b64_tr_b16 a[176:177], v201 offset:512\n"
    "v_mfma_f32_16x16x32_bf16 v[76:79], a[152:155], a[192:195], v[76:79]\n"
    "buffer_load_dwordx4 v195, s[64:67], s84 offen offset:3072 lds\n"
    "v_mfma_f32_16x16x32_bf16 v[64:67], a[160:163], a[224:227], v[64:67]\n"
    "s_waitcnt vmcnt(16) lgkmcnt(1)\n"
    "ds_read_b64_tr_b16 a[178:179], v201 offset:4608\n"
    "v_mfma_f32_16x16x32_bf16 v[64:67], a[164:167], a[228:231], v[64:67]\n"
    "s_add_u32 m0, s22, 81920\n"
    "s_add_u32 s84, s49, 26624\n"
    "s_barrier\n"
    "ds_read_b64_tr_b16 a[180:181], v201 offset:8704\n"
    "v_mfma_f32_16x16x32_bf16 v[80:83], a[164:167], a[236:239], v[80:83]\n"
    "ds_read_b64_tr_b16 a[182:183], v201 offset:12800\n"
    "v_mfma_f32_16x16x32_bf16 v[80:83], a[160:163], a[232:235], v[80:83]\n"
    "buffer_load_dwordx4 v180, s[76:79], s84 offen offset:0 sc1 lds\n"
    "v_mfma_f32_16x16x32_bf16 v[96:99], a[160:163], a[240:243], v[96:99]\n"
    "ds_read_b64_tr_b16 a[184:185], v202 offset:512\n"
    "v_mfma_f32_16x16x32_bf16 v[96:99], a[164:167], a[244:247], v[96:99]\n"
    "ds_read_b64_tr_b16 a[186:187], v202 offset:4608\n"
    "v_mfma_f32_16x16x32_bf16 v[112:115], a[164:167], a[252:255], v[112:115]\n"
    "ds_read_b64_tr_b16 a[188:189], v202 offset:8704\n"
    "v_mfma_f32_16x16x32_bf16 v[112:115], a[160:163], a[248:251], v[112:115]\n"
    "buffer_load_dwordx4 v180, s[76:79], s84 offen offset:1024 sc1 lds\n"
    "v_mfma_f32_16x16x32_bf16 v[116:119], a[168:171], a[248:251], v[116:119]\n"
    "ds_read_b64_tr_b16 a[190:191], v202 offset:12800\n"
    "v_mfma_f32_16x16x32_bf16 v[116:119], a[172:175], a[252:255], v[116:119]\n"
    "ds_read_b64_tr_b16 a[128:129], v201 offset:32768\n"
    "v_mfma_f32_16x16x32_bf16 v[100:103], a[172:175], a[244:247], v[100:103]\n"
    "ds_read_b64_tr_b16 a[130:131], v201 offset:36864\n"
    "v_mfma_f32_16x16x32_bf16 v[100:103], a[168:171], a[240:243], v[100:103]\n"
    "buffer_load_dwordx4 v200, s[76:79], s84 offen offset:2048 sc1 lds\n"
    "v_mfma_f32_16x16x32_bf16 v[84:87], a[168:171], a[232:235], v[84:87]\n"
    "ds_read_b128 a[192:195], v213 offset:32768\n"
    "v_mfma_f32_16x16x32_bf16 v[84:87], a[172:175], a[236:239], v[84:87]\n"
    "ds_read_b64_tr_b16 a[132:133], v201 offset:40960\n"
    "v_mfma_f32_16x16x32_bf16 v[68:71], a[172:175], a[228:231], v[68:71]\n"
    "ds_read_b64_tr_b16 a[134:135], v201 offset:45056\n"
    "v_mfma_f32_16x16x32_bf16 v[68:71], a[168:171], a[224:227], v[68:71]\n"
    "s_waitcnt lgkmcnt(2)\n"
    "buffer_load_dwordx4 v200, s[76:79], s84 offen offset:3072 sc1 lds\n"
    "v_mfma_f32_16x16x32_bf16 v[72:75], a[176:179], a[224:227], v[72:75]\n"
    "ds_read_b128 a[196:199], v213 offset:33792\n"
    "v_mfma_f32_16x16x32_bf16 v[72:75], a[180:183], a[228:231], v[72:75]\n"
    "s_add_u32 m0, s22, 65536\n"
    "s_add_u32 s84, s47, 851968\n"
    "ds_read_b128 a[204:207], v213 offset:35840\n"
    "v_mfma_f32_16x16x32_bf16 v[88:91], a[180:183], a[236:239], v[88:91]\n"
    "ds_read_b128 a[200:203], v213 offset:34816\n"
    "v_mfma_f32_16x16x32_bf16 v[88:91], a[176:179], a[232:235], v[88:91]\n"
    "buffer_load_dwordx4 v192, s[64:67], s84 offen offset:0 lds\n"
    "v_mfma_f32_16x16x32_bf16 v[104:107], a[176:179], a[240:243], v[104:107]\n"
    "ds_read_b128 a[208:211], v213 offset:36864\n"
    "v_mfma_f32_16x16x32_bf16 v[104:107], a[180:183], a[244:247], v[104:107]\n"
    "ds_read_b128 a[212:215], v213 offset:37888\n"
    "v_mfma_f32_16x16x32_bf16 v[120:123], a[180:183], a[252:255], v[120:123]\n"
    "ds_read_b128 a[220:223], v213 offset:39936\n"
    "v_mfma_f32_16x16x32_bf16 v[120:123], a[176:179], a[248:251], v[120:123]\n"
    "buffer_load_dwordx4 v193, s[64:67], s84 offen offset:1024 lds\n"
    "v_mfma_f32_16x16x32_bf16 v[124:127], a[184:187], a[248:251], v[124:127]\n"
    "ds_read_b128 a[216:219], v213 offset:38912\n"
    "v_mfma_f32_16x16x32_bf16 v[124:127], a[188:191], a[252:255], v[124:127]\n"
    "ds_read_b64_tr_b16 a[136:137], v202 offset:32768\n"
    "v_mfma_f32_16x16x32_bf16 v[108:111], a[188:191], a[244:247], v[108:111]\n"
    "ds_read_b64_tr_b16 a[138:139], v202 offset:36864\n"
    "v_mfma_f32_16x16x32_bf16 v[108:111], a[184:187], a[240:243], v[108:111]\n"
    "buffer_load_dwordx4 v194, s[64:67], s84 offen offset:2048 lds\n"
    "v_mfma_f32_16x16x32_bf16 v[92:95], a[184:187], a[232:235], v[92:95]\n"
    "ds_read_b64_tr_b16 a[140:141], v202 offset:40960\n"
    "v_mfma_f32_16x16x32_bf16 v[92:95], a[188:191], a[236:239], v[92:95]\n"
    "ds_read_b64_tr_b16 a[142:143], v202 offset:45056\n"
    "v_mfma_f32_16x16x32_bf16 v[76:79], a[188:191], a[228:231], v[76:79]\n"
    "ds_read_b64_tr_b16 a[144:145], v201 offset:33280\n"
    "v_mfma_f32_16x16x32_bf16 v[76:79], a[184:187], a[224:227], v[76:79]\n"
    "buffer_load_dwordx4 v195, s[64:67], s84 offen offset:3072 lds\n"
    "v_mfma_f32_16x16x32_bf16 v[64:67], a[128:131], a[192:195], v[64:67]\n"
    "s_waitcnt vmcnt(16) lgkmcnt(1)\n"
    "ds_read_b64_tr_b16 a[146:147], v201 offset:37376\n"
    "v_mfma_f32_16x16x32_bf16 v[64:67], a[132:135], a[196:199], v[64:67]\n"
    "s_add_u32 m0, s22, 16384\n"
    "s_add_u32 s84, s49, 28672\n"
    "s_barrier\n"
    "ds_read_b64_tr_b16 a[148:149], v201 offset:41472\n"
    "v_mfma_f32_16x16x32_bf16 v[80:83], a[132:135], a[204:207], v[80:83]\n"
    "ds_read_b64_tr_b16 a[150:151], v201 offset:45568\n"
    "v_mfma_f32_16x16x32_bf16 v[80:83], a[128:131], a[200:203], v[80:83]\n"
    "buffer_load_dwordx4 v180, s[76:79], s84 offen offset:0 sc1 lds\n"
    "v_mfma_f32_16x16x32_bf16 v[96:99], a[128:131], a[208:211], v[96:99]\n"
    "ds_read_b64_tr_b16 a[152:153], v202 offset:33280\n"
    "v_mfma_f32_16x16x32_bf16 v[96:99], a[132:135], a[212:215], v[96:99]\n"
    "ds_read_b64_tr_b16 a[154:155], v202 offset:37376\n"
    "v_mfma_f32_16x16x32_bf16 v[112:115], a[132:135], a[220:223], v[112:115]\n"
    "ds_read_b64_tr_b16 a[156:157], v202 offset:41472\n"
    "v_mfma_f32_16x16x32_bf16 v[112:115], a[128:131], a[216:219], v[112:115]\n"
    "buffer_load_dwordx4 v180, s[76:79], s84 offen offset:1024 sc1 lds\n"
    "v_mfma_f32_16x16x32_bf16 v[116:119], a[136:139], a[216:219], v[116:119]\n"
    "ds_read_b64_tr_b16 a[158:159], v202 offset:45568\n"
    "v_mfma_f32_16x16x32_bf16 v[116:119], a[140:143], a[220:223], v[116:119]\n"
    "ds_read_b64_tr_b16 a[160:161], v203 offset:32768\n"
    "v_mfma_f32_16x16x32_bf16 v[100:103], a[140:143], a[212:215], v[100:103]\n"
    "ds_read_b64_tr_b16 a[162:163], v203 offset:36864\n"
    "v_mfma_f32_16x16x32_bf16 v[100:103], a[136:139], a[208:211], v[100:103]\n"
    "buffer_load_dwordx4 v200, s[76:79], s84 offen offset:2048 sc1 lds\n"
    "v_mfma_f32_16x16x32_bf16 v[84:87], a[136:139], a[200:203], v[84:87]\n"
    "ds_read_b128 a[224:227], v214 offset:32768\n"
    "v_mfma_f32_16x16x32_bf16 v[84:87], a[140:143], a[204:207], v[84:87]\n"
    "ds_read_b64_tr_b16 a[164:165], v203 offset:40960\n"
    "v_mfma_f32_16x16x32_bf16 v[68:71], a[140:143], a[196:199], v[68:71]\n"
    "ds_read_b64_tr_b16 a[166:167], v203 offset:45056\n"
    "v_mfma_f32_16x16x32_bf16 v[68:71], a[136:139], a[192:195], v[68:71]\n"
    "s_waitcnt lgkmcnt(2)\n"
    "buffer_load_dwordx4 v200, s[76:79], s84 offen offset:3072 sc1 lds\n"
    "v_mfma_f32_16x16x32_bf16 v[72:75], a[144:147], a[192:195], v[72:75]\n"
    "ds_read_b128 a[228:231], v214 offset:33792\n"
    "v_mfma_f32_16x16x32_bf16 v[72:75], a[148:151], a[196:199], v[72:75]\n"
    "s_add_u32 m0, s22, 0\n"
    "s_add_u32 s84, s47, 917504\n"
    "ds_read_b128 a[236:239], v214 offset:35840\n"
    "v_mfma_f32_16x16x32_bf16 v[88:91], a[148:151], a[204:207], v[88:91]\n"
    "ds_read_b128 a[232:235], v214 offset:34816\n"
    "v_mfma_f32_16x16x32_bf16 v[88:91], a[144:147], a[200:203], v[88:91]\n"
    "buffer_load_dwordx4 v192, s[64:67], s84 offen offset:0 lds\n"
    "v_mfma_f32_16x16x32_bf16 v[104:107], a[144:147], a[208:211], v[104:107]\n"
    "ds_read_b128 a[240:243], v214 offset:36864\n"
    "v_mfma_f32_16x16x32_bf16 v[104:107], a[148:151], a[212:215], v[104:107]\n"
    "ds_read_b128 a[244:247], v214 offset:37888\n"
    "v_mfma_f32_16x16x32_bf16 v[120:123], a[148:151], a[220:223], v[120:123]\n"
    "ds_read_b128 a[252:255], v214 offset:39936\n"
    "v_mfma_f32_16x16x32_bf16 v[120:123], a[144:147], a[216:219], v[120:123]\n"
    "buffer_load_dwordx4 v193, s[64:67], s84 offen offset:1024 lds\n"
    "v_mfma_f32_16x16x32_bf16 v[124:127], a[152:155], a[216:219], v[124:127]\n"
    "ds_read_b128 a[248:251], v214 offset:38912\n"
    "v_mfma_f32_16x16x32_bf16 v[124:127], a[156:159], a[220:223], v[124:127]\n"
    "ds_read_b64_tr_b16 a[168:169], v204 offset:32768\n"
    "v_mfma_f32_16x16x32_bf16 v[108:111], a[156:159], a[212:215], v[108:111]\n"
    "ds_read_b64_tr_b16 a[170:171], v204 offset:36864\n"
    "v_mfma_f32_16x16x32_bf16 v[108:111], a[152:155], a[208:211], v[108:111]\n"
    "buffer_load_dwordx4 v194, s[64:67], s84 offen offset:2048 lds\n"
    "v_mfma_f32_16x16x32_bf16 v[92:95], a[152:155], a[200:203], v[92:95]\n"
    "ds_read_b64_tr_b16 a[172:173], v204 offset:40960\n"
    "v_mfma_f32_16x16x32_bf16 v[92:95], a[156:159], a[204:207], v[92:95]\n"
    "ds_read_b64_tr_b16 a[174:175], v204 offset:45056\n"
    "v_mfma_f32_16x16x32_bf16 v[76:79], a[156:159], a[196:199], v[76:79]\n"
    "ds_read_b64_tr_b16 a[176:177], v203 offset:33280\n"
    "v_mfma_f32_16x16x32_bf16 v[76:79], a[152:155], a[192:195], v[76:79]\n"
    "buffer_load_dwordx4 v195, s[64:67], s84 offen offset:3072 lds\n"
    "v_mfma_f32_16x16x32_bf16 v[64:67], a[160:163], a[224:227], v[64:67]\n"
    "s_waitcnt vmcnt(16) lgkmcnt(1)\n"
    "ds_read_b64_tr_b16 a[178:179], v203 offset:37376\n"
    "v_mfma_f32_16x16x32_bf16 v[64:67], a[164:167], a[228:231], v[64:67]\n"
    "s_add_u32 m0, s22, 49152\n"
    "s_add_u32 s84, s49, 30720\n"
    "s_barrier\n"
    "ds_read_b64_tr_b16 a[180:181], v203 offset:41472\n"
    "v_mfma_f32_16x16x32_bf16 v[80:83], a[164:167], a[236:239], v[80:83]\n"
    "ds_read_b64_tr_b16 a[182:183], v203 offset:45568\n"
    "v_mfma_f32_16x16x32_bf16 v[80:83], a[160:163], a[232:235], v[80:83]\n"
    "buffer_load_dwordx4 v180, s[76:79], s84 offen offset:0 sc1 lds\n"
    "v_mfma_f32_16x16x32_bf16 v[96:99], a[160:163], a[240:243], v[96:99]\n"
    "ds_read_b64_tr_b16 a[184:185], v204 offset:33280\n"
    "v_mfma_f32_16x16x32_bf16 v[96:99], a[164:167], a[244:247], v[96:99]\n"
    "ds_read_b64_tr_b16 a[186:187], v204 offset:37376\n"
    "v_mfma_f32_16x16x32_bf16 v[112:115], a[164:167], a[252:255], v[112:115]\n"
    "ds_read_b64_tr_b16 a[188:189], v204 offset:41472\n"
    "v_mfma_f32_16x16x32_bf16 v[112:115], a[160:163], a[248:251], v[112:115]\n"
    "buffer_load_dwordx4 v180, s[76:79], s84 offen offset:1024 sc1 lds\n"
    "v_mfma_f32_16x16x32_bf16 v[116:119], a[168:171], a[248:251], v[116:119]\n"
    "ds_read_b64_tr_b16 a[190:191], v204 offset:45568\n"
    "v_mfma_f32_16x16x32_bf16 v[116:119], a[172:175], a[252:255], v[116:119]\n"
    "ds_read_b64_tr_b16 a[128:129], v205 offset:0\n"
    "v_mfma_f32_16x16x32_bf16 v[100:103], a[172:175], a[244:247], v[100:103]\n"
    "ds_read_b64_tr_b16 a[130:131], v205 offset:4096\n"
    "v_mfma_f32_16x16x32_bf16 v[100:103], a[168:171], a[240:243], v[100:103]\n"
    "buffer_load_dwordx4 v200, s[76:79], s84 offen offset:2048 sc1 lds\n"
    "v_mfma_f32_16x16x32_bf16 v[84:87], a[168:171], a[232:235], v[84:87]\n"
    "ds_read_b128 a[192:195], v215 offset:0\n"
    "v_mfma_f32_16x16x32_bf16 v[84:87], a[172:175], a[236:239], v[84:87]\n"
    "ds_read_b64_tr_b16 a[132:133], v205 offset:8192\n"
    "v_mfma_f32_16x16x32_bf16 v[68:71], a[172:175], a[228:231], v[68:71]\n"
    "ds_read_b64_tr_b16 a[134:135], v205 offset:12288\n"
    "v_mfma_f32_16x16x32_bf16 v[68:71], a[168:171], a[224:227], v[68:71]\n"
    "s_waitcnt lgkmcnt(2)\n"
    "buffer_load_dwordx4 v200, s[76:79], s84 offen offset:3072 sc1 lds\n"
    "v_mfma_f32_16x16x32_bf16 v[72:75], a[176:179], a[224:227], v[72:75]\n"
    "ds_read_b128 a[196:199], v215 offset:1024\n"
    "v_mfma_f32_16x16x32_bf16 v[72:75], a[180:183], a[228:231], v[72:75]\n"
    "s_add_u32 m0, s22, 32768\n"
    "s_add_u32 s84, s47, 983040\n"
    "ds_read_b128 a[204:207], v215 offset:3072\n"
    "v_mfma_f32_16x16x32_bf16 v[88:91], a[180:183], a[236:239], v[88:91]\n"
    "ds_read_b128 a[200:203], v215 offset:2048\n"
    "v_mfma_f32_16x16x32_bf16 v[88:91], a[176:179], a[232:235], v[88:91]\n"
    "buffer_load_dwordx4 v192, s[64:67], s84 offen offset:0 lds\n"
    "v_mfma_f32_16x16x32_bf16 v[104:107], a[176:179], a[240:243], v[104:107]\n"
    "ds_read_b128 a[208:211], v215 offset:4096\n"
    "v_mfma_f32_16x16x32_bf16 v[104:107], a[180:183], a[244:247], v[104:107]\n"
    "ds_read_b128 a[212:215], v215 offset:5120\n"
    "v_mfma_f32_16x16x32_bf16 v[120:123], a[180:183], a[252:255], v[120:123]\n"
    "ds_read_b128 a[220:223], v215 offset:7168\n"
    "v_mfma_f32_16x16x32_bf16 v[120:123], a[176:179], a[248:251], v[120:123]\n"
    "buffer_load_dwordx4 v193, s[64:67], s84 offen offset:1024 lds\n"
    "v_mfma_f32_16x16x32_bf16 v[124:127], a[184:187], a[248:251], v[124:127]\n"
    "ds_read_b128 a[216:219], v215 offset:6144\n"
    "v_mfma_f32_16x16x32_bf16 v[124:127], a[188:191], a[252:255], v[124:127]\n"
    "ds_read_b64_tr_b16 a[136:137], v206 offset:0\n"
    "v_mfma_f32_16x16x32_bf16 v[108:111], a[188:191], a[244:247], v[108:111]\n"
    "ds_read_b64_tr_b16 a[138:139], v206 offset:4096\n"
    "v_mfma_f32_16x16x32_bf16 v[108:111], a[184:187], a[240:243], v[108:111]\n"
    "buffer_load_dwordx4 v194, s[64:67], s84 offen offset:2048 lds\n"
    "v_mfma_f32_16x16x32_bf16 v[92:95], a[184:187], a[232:235], v[92:95]\n"
    "ds_read_b64_tr_b16 a[140:141], v206 offset:8192\n"
    "v_mfma_f32_16x16x32_bf16 v[92:95], a[188:191], a[236:239], v[92:95]\n"
    "ds_read_b64_tr_b16 a[142:143], v206 offset:12288\n"
    "v_mfma_f32_16x16x32_bf16 v[76:79], a[188:191], a[228:231], v[76:79]\n"
    "ds_read_b64_tr_b16 a[144:145], v205 offset:512\n"
    "v_mfma_f32_16x16x32_bf16 v[76:79], a[184:187], a[224:227], v[76:79]\n"
    "buffer_load_dwordx4 v195, s[64:67], s84 offen offset:3072 lds\n"
    "v_mfma_f32_16x16x32_bf16 v[64:67], a[128:131], a[192:195], v[64:67]\n"
    "s_waitcnt vmcnt(16) lgkmcnt(1)\n"
    "ds_read_b64_tr_b16 a[146:147], v205 offset:4608\n"
    "v_mfma_f32_16x16x32_bf16 v[64:67], a[132:135], a[196:199], v[64:67]\n"
    "s_add_u32 m0, s22, 114688\n"
    "s_add_u32 s84, s48, 24576\n"
    "s_barrier\n"
    "ds_read_b64_tr_b16 a[148:149], v205 offset:8704\n"
    "v_mfma_f32_16x16x32_bf16 v[80:83], a[132:135], a[204:207], v[80:83]\n"
    "ds_read_b64_tr_b16 a[150:151], v205 offset:12800\n"
    "v_mfma_f32_16x16x32_bf16 v[80:83], a[128:131], a[200:203], v[80:83]\n"
    "buffer_load_dwordx4 v180, s[72:75], s84 offen offset:0 sc1 lds\n"
    "v_mfma_f32_16x16x32_bf16 v[96:99], a[128:131], a[208:211], v[96:99]\n"
    "ds_read_b64_tr_b16 a[152:153], v206 offset:512\n"
    "v_mfma_f32_16x16x32_bf16 v[96:99], a[132:135], a[212:215], v[96:99]\n"
    "ds_read_b64_tr_b16 a[154:155], v206 offset:4608\n"
    "v_mfma_f32_16x16x32_bf16 v[112:115], a[132:135], a[220:223], v[112:115]\n"
    "ds_read_b64_tr_b16 a[156:157], v206 offset:8704\n"
    "v_mfma_f32_16x16x32_bf16 v[112:115], a[128:131], a[216:219], v[112:115]\n"
    "buffer_load_dwordx4 v180, s[72:75], s84 offen offset:1024 sc1 lds\n"
    "v_mfma_f32_16x16x32_bf16 v[116:119], a[136:139], a[216:219], v[116:119]\n"
    "ds_read_b64_tr_b16 a[158:159], v206 offset:12800\n"
    "v_mfma_f32_16x16x32_bf16 v[116:119], a[140:143], a[220:223], v[116:119]\n"
    "ds_read_b64_tr_b16 a[160:161], v203 offset:0\n"
    "v_mfma_f32_16x16x32_bf16 v[100:103], a[140:143], a[212:215], v[100:103]\n"
    "ds_read_b64_tr_b16 a[162:163], v203 offset:4096\n"
    "v_mfma_f32_16x16x32_bf16 v[100:103], a[136:139], a[208:211], v[100:103]\n"
    "buffer_load_dwordx4 v180, s[72:75], s84 offen offset:2048 sc1 lds\n"
    "v_mfma_f32_16x16x32_bf16 v[84:87], a[136:139], a[200:203], v[84:87]\n"
    "ds_read_b128 a[224:227], v214 offset:0\n"
    "v_mfma_f32_16x16x32_bf16 v[84:87], a[140:143], a[204:207], v[84:87]\n"
    "ds_read_b64_tr_b16 a[164:165], v203 offset:8192\n"
    "v_mfma_f32_16x16x32_bf16 v[68:71], a[140:143], a[196:199], v[68:71]\n"
    "ds_read_b64_tr_b16 a[166:167], v203 offset:12288\n"
    "v_mfma_f32_16x16x32_bf16 v[68:71], a[136:139], a[192:195], v[68:71]\n"
    "s_waitcnt lgkmcnt(2)\n"
    "buffer_load_dwordx4 v180, s[72:75], s84 offen offset:3072 sc1 lds\n"
    "v_mfma_f32_16x16x32_bf16 v[72:75], a[144:147], a[192:195], v[72:75]\n"
    "ds_read_b128 a[228:231], v214 offset:1024\n"
    "v_mfma_f32_16x16x32_bf16 v[72:75], a[148:151], a[196:199], v[72:75]\n"
    "s_add_u32 m0, s22, 98304\n"
    "s_add_u32 s84, s46, 786432\n"
    "ds_read_b128 a[236:239], v214 offset:3072\n"
    "v_mfma_f32_16x16x32_bf16 v[88:91], a[148:151], a[204:207], v[88:91]\n"
    "ds_read_b128 a[232:235], v214 offset:2048\n"
    "v_mfma_f32_16x16x32_bf16 v[88:91], a[144:147], a[200:203], v[88:91]\n"
    "buffer_load_dwordx4 v196, s[60:63], s84 offen offset:0 lds\n"
    "v_mfma_f32_16x16x32_bf16 v[104:107], a[144:147], a[208:211], v[104:107]\n"
    "ds_read_b128 a[240:243], v214 offset:4096\n"
    "v_mfma_f32_16x16x32_bf16 v[104:107], a[148:151], a[212:215], v[104:107]\n"
    "ds_read_b128 a[244:247], v214 offset:5120\n"
    "v_mfma_f32_16x16x32_bf16 v[120:123], a[148:151], a[220:223], v[120:123]\n"
    "ds_read_b128 a[252:255], v214 offset:7168\n"
    "v_mfma_f32_16x16x32_bf16 v[120:123], a[144:147], a[216:219], v[120:123]\n"
    "buffer_load_dwordx4 v197, s[60:63], s84 offen offset:1024 lds\n"
    "v_mfma_f32_16x16x32_bf16 v[124:127], a[152:155], a[216:219], v[124:127]\n"
    "ds_read_b128 a[248:251], v214 offset:6144\n"
    "v_mfma_f32_16x16x32_bf16 v[124:127], a[156:159], a[220:223], v[124:127]\n"
    "ds_read_b64_tr_b16 a[168:169], v204 offset:0\n"
    "v_mfma_f32_16x16x32_bf16 v[108:111], a[156:159], a[212:215], v[108:111]\n"
    "ds_read_b64_tr_b16 a[170:171], v204 offset:4096\n"
    "v_mfma_f32_16x16x32_bf16 v[108:111], a[152:155], a[208:211], v[108:111]\n"
    "buffer_load_dwordx4 v198, s[60:63], s84 offen offset:2048 lds\n"
    "v_mfma_f32_16x16x32_bf16 v[92:95], a[152:155], a[200:203], v[92:95]\n"
    "ds_read_b64_tr_b16 a[172:173], v204 offset:8192\n"
    "v_mfma_f32_16x16x32_bf16 v[92:95], a[156:159], a[204:207], v[92:95]\n"
    "ds_read_b64_tr_b16 a[174:175], v204 offset:12288\n"
    "v_mfma_f32_16x16x32_bf16 v[76:79], a[156:159], a[196:199], v[76:79]\n"
    "ds_read_b64_tr_b16 a[176:177], v203 offset:512\n"
    "v_mfma_f32_16x16x32_bf16 v[76:79], a[152:155], a[192:195], v[76:79]\n"
    "buffer_load_dwordx4 v199, s[60:63], s84 offen offset:3072 lds\n"
    "v_mfma_f32_16x16x32_bf16 v[64:67], a[160:163], a[224:227], v[64:67]\n"
    "s_waitcnt vmcnt(16) lgkmcnt(1)\n"
    "ds_read_b64_tr_b16 a[178:179], v203 offset:4608\n"
    "v_mfma_f32_16x16x32_bf16 v[64:67], a[164:167], a[228:231], v[64:67]\n"
    "s_add_u32 m0, s22, 147456\n"
    "s_add_u32 s84, s48, 28672\n"
    "s_barrier\n"
    "ds_read_b64_tr_b16 a[180:181], v203 offset:8704\n"
    "v_mfma_f32_16x16x32_bf16 v[80:83], a[164:167], a[236:239], v[80:83]\n"
    "ds_read_b64_tr_b16 a[182:183], v203 offset:12800\n"
    "v_mfma_f32_16x16x32_bf16 v[80:83], a[160:163], a[232:235], v[80:83]\n"
    "buffer_load_dwordx4 v180, s[72:75], s84 offen offset:0 sc1 lds\n"
    "v_mfma_f32_16x16x32_bf16 v[96:99], a[160:163], a[240:243], v[96:99]\n"
    "ds_read_b64_tr_b16 a[184:185], v204 offset:512\n"
    "v_mfma_f32_16x16x32_bf16 v[96:99], a[164:167], a[244:247], v[96:99]\n"
    "ds_read_b64_tr_b16 a[186:187], v204 offset:4608\n"
    "v_mfma_f32_16x16x32_bf16 v[112:115], a[164:167], a[252:255], v[112:115]\n"
    "ds_read_b64_tr_b16 a[188:189], v204 offset:8704\n"
    "v_mfma_f32_16x16x32_bf16 v[112:115], a[160:163], a[248:251], v[112:115]\n"
    "buffer_load_dwordx4 v180, s[72:75], s84 offen offset:1024 sc1 lds\n"
    "v_mfma_f32_16x16x32_bf16 v[116:119], a[168:171], a[248:251], v[116:119]\n"
    "ds_read_b64_tr_b16 a[190:191], v204 offset:12800\n"
    "v_mfma_f32_16x16x32_bf16 v[116:119], a[172:175], a[252:255], v[116:119]\n"
    "ds_read_b64_tr_b16 a[128:129], v201 offset:0\n"
    "v_mfma_f32_16x16x32_bf16 v[100:103], a[172:175], a[244:247], v[100:103]\n"
    "ds_read_b64_tr_b16 a[130:131], v201 offset:4096\n"
    "v_mfma_f32_16x16x32_bf16 v[100:103], a[168:171], a[240:243], v[100:103]\n"
    "buffer_load_dwordx4 v180, s[72:75], s84 offen offset:2048 sc1 lds\n"
    "v_mfma_f32_16x16x32_bf16 v[84:87], a[168:171], a[232:235], v[84:87]\n"
    "ds_read_b128 a[192:195], v213 offset:0\n"
    "v_mfma_f32_16x16x32_bf16 v[84:87], a[172:175], a[236:239], v[84:87]\n"
    "ds_read_b64_tr_b16 a[132:133], v201 offset:8192\n"
    "v_mfma_f32_16x16x32_bf16 v[68:71], a[172:175], a[228:231], v[68:71]\n"
    "ds_read_b64_tr_b16 a[134:135], v201 offset:12288\n"
    "v_mfma_f32_16x16x32_bf16 v[68:71], a[168:171], a[224:227], v[68:71]\n"
    "s_waitcnt lgkmcnt(2)\n"
    "buffer_load_dwordx4 v180, s[72:75], s84 offen offset:3072 sc1 lds\n"
    "v_mfma_f32_16x16x32_bf16 v[72:75], a[176:179], a[224:227], v[72:75]\n"
    "ds_read_b128 a[196:199], v213 offset:1024\n"
    "v_mfma_f32_16x16x32_bf16 v[72:75], a[180:183], a[228:231], v[72:75]\n"
    "s_add_u32 m0, s22, 131072\n"
    "s_add_u32 s84, s46, 917504\n"
    "ds_read_b128 a[204:207], v213 offset:3072\n"
    "v_mfma_f32_16x16x32_bf16 v[88:91], a[180:183], a[236:239], v[88:91]\n"
    "ds_read_b128 a[200:203], v213 offset:2048\n"
    "v_mfma_f32_16x16x32_bf16 v[88:91], a[176:179], a[232:235], v[88:91]\n"
    "buffer_load_dwordx4 v196, s[60:63], s84 offen offset:0 lds\n"
    "v_mfma_f32_16x16x32_bf16 v[104:107], a[176:179], a[240:243], v[104:107]\n"
    "ds_read_b128 a[208:211], v213 offset:4096\n"
    "v_mfma_f32_16x16x32_bf16 v[104:107], a[180:183], a[244:247], v[104:107]\n"
    "ds_read_b128 a[212:215], v213 offset:5120\n"
    "v_mfma_f32_16x16x32_bf16 v[120:123], a[180:183], a[252:255], v[120:123]\n"
    "ds_read_b128 a[220:223], v213 offset:7168\n"
    "v_mfma_f32_16x16x32_bf16 v[120:123], a[176:179], a[248:251], v[120:123]\n"
    "buffer_load_dwordx4 v197, s[60:63], s84 offen offset:1024 lds\n"
    "v_mfma_f32_16x16x32_bf16 v[124:127], a[184:187], a[248:251], v[124:127]\n"
    "ds_read_b128 a[216:219], v213 offset:6144\n"
    "v_mfma_f32_16x16x32_bf16 v[124:127], a[188:191], a[252:255], v[124:127]\n"
    "ds_read_b64_tr_b16 a[136:137], v202 offset:0\n"
    "v_mfma_f32_16x16x32_bf16 v[108:111], a[188:191], a[244:247], v[108:111]\n"
    "ds_read_b64_tr_b16 a[138:139], v202 offset:4096\n"
    "v_mfma_f32_16x16x32_bf16 v[108:111], a[184:187], a[240:243], v[108:111]\n"
    "buffer_load_dwordx4 v198, s[60:63], s84 offen offset:2048 lds\n"
    "v_mfma_f32_16x16x32_bf16 v[92:95], a[184:187], a[232:235], v[92:95]\n"
    "ds_read_b64_tr_b16 a[140:141], v202 offset:8192\n"
    "v_mfma_f32_16x16x32_bf16 v[92:95], a[188:191], a[236:239], v[92:95]\n"
    "ds_read_b64_tr_b16 a[142:143], v202 offset:12288\n"
    "v_mfma_f32_16x16x32_bf16 v[76:79], a[188:191], a[228:231], v[76:79]\n"
    "ds_read_b64_tr_b16 a[144:145], v201 offset:512\n"
    "v_mfma_f32_16x16x32_bf16 v[76:79], a[184:187], a[224:227], v[76:79]\n"
    "buffer_load_dwordx4 v199, s[60:63], s84 offen offset:3072 lds\n"
    "v_mfma_f32_16x16x32_bf16 v[64:67], a[128:131], a[192:195], v[64:67]\n"
    "s_waitcnt vmcnt(16) lgkmcnt(1)\n"
    "ds_read_b64_tr_b16 a[146:147], v201 offset:4608\n"
    "v_mfma_f32_16x16x32_bf16 v[64:67], a[132:135], a[196:199], v[64:67]\n"
    "s_add_u32 m0, s22, 81920\n"
    "s_add_u32 s84, s48, 32768\n"
    "s_barrier\n"
    "ds_read_b64_tr_b16 a[148:149], v201 offset:8704\n"
    "v_mfma_f32_16x16x32_bf16 v[80:83], a[132:135], a[204:207], v[80:83]\n"
    "ds_read_b64_tr_b16 a[150:151], v201 offset:12800\n"
    "v_mfma_f32_16x16x32_bf16 v[80:83], a[128:131], a[200:203], v[80:83]\n"
    "buffer_load_dwordx4 v180, s[72:75], s84 offen offset:0 sc1 lds\n"
    "v_mfma_f32_16x16x32_bf16 v[96:99], a[128:131], a[208:211], v[96:99]\n"
    "ds_read_b64_tr_b16 a[152:153], v202 offset:512\n"
    "v_mfma_f32_16x16x32_bf16 v[96:99], a[132:135], a[212:215], v[96:99]\n"
    "ds_read_b64_tr_b16 a[154:155], v202 offset:4608\n"
    "v_mfma_f32_16x16x32_bf16 v[112:115], a[132:135], a[220:223], v[112:115]\n"
    "ds_read_b64_tr_b16 a[156:157], v202 offset:8704\n"
    "v_mfma_f32_16x16x32_bf16 v[112:115], a[128:131], a[216:219], v[112:115]\n"
    "buffer_load_dwordx4 v180, s[72:75], s84 offen offset:1024 sc1 lds\n"
    "v_mfma_f32_16x16x32_bf16 v[116:119], a[136:139], a[216:219], v[116:119]\n"
    "ds_read_b64_tr_b16 a[158:159], v202 offset:12800\n"
    "v_mfma_f32_16x16x32_bf16 v[116:119], a[140:143], a[220:223], v[116:119]\n"
    "ds_read_b64_tr_b16 a[160:161], v201 offset:32768\n"
    "v_mfma_f32_16x16x32_bf16 v[100:103], a[140:143], a[212:215], v[100:103]\n"
    "ds_read_b64_tr_b16 a[162:163], v201 offset:36864\n"
    "v_mfma_f32_16x16x32_bf16 v[100:103], a[136:139], a[208:211], v[100:103]\n"
    "buffer_load_dwordx4 v180, s[72:75], s84 offen offset:2048 sc1 lds\n"
    "v_mfma_f32_16x16x32_bf16 v[84:87], a[136:139], a[200:203], v[84:87]\n"
    "ds_read_b128 a[224:227], v213 offset:32768\n"
    "v_mfma_f32_16x16x32_bf16 v[84:87], a[140:143], a[204:207], v[84:87]\n"
    "ds_read_b64_tr_b16 a[164:165], v201 offset:40960\n"
    "v_mfma_f32_16x16x32_bf16 v[68:71], a[140:143], a[196:199], v[68:71]\n"
    "ds_read_b64_tr_b16 a[166:167], v201 offset:45056\n"
    "v_mfma_f32_16x16x32_bf16 v[68:71], a[136:139], a[192:195], v[68:71]\n"
    "s_waitcnt lgkmcnt(2)\n"
    "buffer_load_dwordx4 v180, s[72:75], s84 offen offset:3072 sc1 lds\n"
    "v_mfma_f32_16x16x32_bf16 v[72:75], a[144:147], a[192:195], v[72:75]\n"
    "ds_read_b128 a[228:231], v213 offset:33792\n"
    "v_mfma_f32_16x16x32_bf16 v[72:75], a[148:151], a[196:199], v[72:75]\n"
    "s_add_u32 m0, s22, 65536\n"
    "s_add_u32 s84, s46, 1048576\n"
    "ds_read_b128 a[236:239], v213 offset:35840\n"
    "v_mfma_f32_16x16x32_bf16 v[88:91], a[148:151], a[204:207], v[88:91]\n"
    "ds_read_b128 a[232:235], v213 offset:34816\n"
    "v_mfma_f32_16x16x32_bf16 v[88:91], a[144:147], a[200:203], v[88:91]\n"
    "buffer_load_dwordx4 v196, s[60:63], s84 offen offset:0 lds\n"
    "v_mfma_f32_16x16x32_bf16 v[104:107], a[144:147], a[208:211], v[104:107]\n"
    "ds_read_b128 a[240:243], v213 offset:36864\n"
    "v_mfma_f32_16x16x32_bf16 v[104:107], a[148:151], a[212:215], v[104:107]\n"
    "ds_read_b128 a[244:247], v213 offset:37888\n"
    "v_mfma_f32_16x16x32_bf16 v[120:123], a[148:151], a[220:223], v[120:123]\n"
    "ds_read_b128 a[252:255], v213 offset:39936\n"
    "v_mfma_f32_16x16x32_bf16 v[120:123], a[144:147], a[216:219], v[120:123]\n"
    "buffer_load_dwordx4 v197, s[60:63], s84 offen offset:1024 lds\n"
    "v_mfma_f32_16x16x32_bf16 v[124:127], a[152:155], a[216:219], v[124:127]\n"
    "ds_read_b128 a[248:251], v213 offset:38912\n"
    "v_mfma_f32_16x16x32_bf16 v[124:127], a[156:159], a[220:223], v[124:127]\n"
    "ds_read_b64_tr_b16 a[168:169], v202 offset:32768\n"
    "v_mfma_f32_16x16x32_bf16 v[108:111], a[156:159], a[212:215], v[108:111]\n"
    "ds_read_b64_tr_b16 a[170:171], v202 offset:36864\n"
    "v_mfma_f32_16x16x32_bf16 v[108:111], a[152:155], a[208:211], v[108:111]\n"
    "buffer_load_dwordx4 v198, s[60:63], s84 offen offset:2048 lds\n"
    "v_mfma_f32_16x16x32_bf16 v[92:95], a[152:155], a[200:203], v[92:95]\n"
    "ds_read_b64_tr_b16 a[172:173], v202 offset:40960\n"
    "v_mfma_f32_16x16x32_bf16 v[92:95], a[156:159], a[204:207], v[92:95]\n"
    "ds_read_b64_tr_b16 a[174:175], v202 offset:45056\n"
    "v_mfma_f32_16x16x32_bf16 v[76:79], a[156:159], a[196:199], v[76:79]\n"
    "ds_read_b64_tr_b16 a[176:177], v201 offset:33280\n"
    "v_mfma_f32_16x16x32_bf16 v[76:79], a[152:155], a[192:195], v[76:79]\n"
    "buffer_load_dwordx4 v199, s[60:63], s84 offen offset:3072 lds\n"
    "v_mfma_f32_16x16x32_bf16 v[64:67], a[160:163], a[224:227], v[64:67]\n"
    "s_waitcnt vmcnt(16) lgkmcnt(1)\n"
    "ds_read_b64_tr_b16 a[178:179], v201 offset:37376\n"
    "v_mfma_f32_16x16x32_bf16 v[64:67], a[164:167], a[228:231], v[64:67]\n"
    "s_add_u32 m0, s22, 16384\n"
    "s_add_u32 s84, s48, 36864\n"
    "s_barrier\n"
    "ds_read_b64_tr_b16 a[180:181], v201 offset:41472\n"
    "v_mfma_f32_16x16x32_bf16 v[80:83], a[164:167], a[236:239], v[80:83]\n"
    "ds_read_b64_tr_b16 a[182:183], v201 offset:45568\n"
    "v_mfma_f32_16x16x32_bf16 v[80:83], a[160:163], a[232:235], v[80:83]\n"
    "buffer_load_dwordx4 v180, s[72:75], s84 offen offset:0 sc1 lds\n"
    "v_mfma_f32_16x16x32_bf16 v[96:99], a[160:163], a[240:243], v[96:99]\n"
    "ds_read_b64_tr_b16 a[184:185], v202 offset:33280\n"
    "v_mfma_f32_16x16x32_bf16 v[96:99], a[164:167], a[244:247], v[96:99]\n"
    "ds_read_b64_tr_b16 a[186:187], v202 offset:37376\n"
    "v_mfma_f32_16x16x32_bf16 v[112:115], a[164:167], a[252:255], v[112:115]\n"
    "ds_read_b64_tr_b16 a[188:189], v202 offset:41472\n"
    "v_mfma_f32_16x16x32_bf16 v[112:115], a[160:163], a[248:251], v[112:115]\n"
    "buffer_load_dwordx4 v180, s[72:75], s84 offen offset:1024 sc1 lds\n"
    "v_mfma_f32_16x16x32_bf16 v[116:119], a[168:171], a[248:251], v[116:119]\n"
    "ds_read_b64_tr_b16 a[190:191], v202 offset:45568\n"
    "v_mfma_f32_16x16x32_bf16 v[116:119], a[172:175], a[252:255], v[116:119]\n"
    "ds_read_b64_tr_b16 a[128:129], v209 offset:32768\n"
    "v_mfma_f32_16x16x32_bf16 v[100:103], a[172:175], a[244:247], v[100:103]\n"
    "ds_read_b64_tr_b16 a[130:131], v209 offset:34816\n"
    "v_mfma_f32_16x16x32_bf16 v[100:103], a[168:171], a[240:243], v[100:103]\n"
    "buffer_load_dwordx4 v180, s[72:75], s84 offen offset:2048 sc1 lds\n"
    "v_mfma_f32_16x16x32_bf16 v[84:87], a[168:171], a[232:235], v[84:87]\n"
    "ds_read_b128 a[192:195], v214 offset:32768\n"
    "v_mfma_f32_16x16x32_bf16 v[84:87], a[172:175], a[236:239], v[84:87]\n"
    "ds_read_b64_tr_b16 a[132:133], v209 offset:36864\n"
    "v_mfma_f32_16x16x32_bf16 v[68:71], a[172:175], a[228:231], v[68:71]\n"
    "ds_read_b64_tr_b16 a[134:135], v209 offset:38912\n"
    "v_mfma_f32_16x16x32_bf16 v[68:71], a[168:171], a[224:227], v[68:71]\n"
    "s_waitcnt lgkmcnt(2)\n"
    "buffer_load_dwordx4 v180, s[72:75], s84 offen offset:3072 sc1 lds\n"
    "v_mfma_f32_16x16x32_bf16 v[72:75], a[176:179], a[224:227], v[72:75]\n"
    "ds_read_b128 a[196:199], v214 offset:33792\n"
    "v_mfma_f32_16x16x32_bf16 v[72:75], a[180:183], a[228:231], v[72:75]\n"
    "s_add_u32 m0, s22, 0\n"
    "s_add_u32 s84, s46, 1179648\n"
    "ds_read_b64_tr_b16 a[136:137], v209 offset:40960\n"
    "v_mfma_f32_16x16x32_bf16 v[88:91], a[180:183], a[236:239], v[88:91]\n"
    "ds_read_b64_tr_b16 a[138:139], v209 offset:43008\n"
    "v_mfma_f32_16x16x32_bf16 v[88:91], a[176:179], a[232:235], v[88:91]\n"
    "buffer_load_dwordx4 v196, s[60:63], s84 offen offset:0 lds\n"
    "v_mfma_f32_16x16x32_bf16 v[104:107], a[176:179], a[240:243], v[104:107]\n"
    "ds_read_b128 a[200:203], v214 offset:34816\n"
    "v_mfma_f32_16x16x32_bf16 v[104:107], a[180:183], a[244:247], v[104:107]\n"
    "ds_read_b64_tr_b16 a[140:141], v209 offset:45056\n"
    "v_mfma_f32_16x16x32_bf16 v[120:123], a[180:183], a[252:255], v[120:123]\n"
    "ds_read_b64_tr_b16 a[142:143], v209 offset:47104\n"
    "v_mfma_f32_16x16x32_bf16 v[120:123], a[176:179], a[248:251], v[120:123]\n"
    "buffer_load_dwordx4 v197, s[60:63], s84 offen offset:1024 lds\n"
    "v_mfma_f32_16x16x32_bf16 v[124:127], a[184:187], a[248:251], v[124:127]\n"
    "ds_read_b128 a[204:207], v214 offset:35840\n"
    "v_mfma_f32_16x16x32_bf16 v[124:127], a[188:191], a[252:255], v[124:127]\n"
    "ds_read_b128 a[220:223], v214 offset:39936\n"
    "v_mfma_f32_16x16x32_bf16 v[108:111], a[188:191], a[244:247], v[108:111]\n"
    "ds_read_b128 a[216:219], v214 offset:38912\n"
    "v_mfma_f32_16x16x32_bf16 v[108:111], a[184:187], a[240:243], v[108:111]\n"
    "buffer_load_dwordx4 v198, s[60:63], s84 offen offset:2048 lds\n"
    "v_mfma_f32_16x16x32_bf16 v[92:95], a[184:187], a[232:235], v[92:95]\n"
    "ds_read_b128 a[212:215], v214 offset:37888\n"
    "v_mfma_f32_16x16x32_bf16 v[92:95], a[188:191], a[236:239], v[92:95]\n"
    "ds_read_b128 a[208:211], v214 offset:36864\n"
    "v_mfma_f32_16x16x32_bf16 v[76:79], a[188:191], a[228:231], v[76:79]\n"
    "ds_read_b64_tr_b16 a[144:145], v210 offset:32768\n"
    "v_mfma_f32_16x16x32_bf16 v[76:79], a[184:187], a[224:227], v[76:79]\n"
    "buffer_load_dwordx4 v199, s[60:63], s84 offen offset:3072 lds\n"
    "v_mfma_f32_16x16x32_bf16 v[0:3], a[128:131], a[192:195], v[0:3]\n"
    "s_waitcnt vmcnt(16) lgkmcnt(1)\n"
    "ds_read_b64_tr_b16 a[146:147], v210 offset:34816\n"
    "ds_read_b64_tr_b16 a[148:149], v210 offset:36864\n"
    "v_mfma_f32_16x16x32_bf16 v[0:3], a[132:135], a[196:199], v[0:3]\n"
    "s_barrier\n"
    "ds_read_b64_tr_b16 a[150:151], v210 offset:38912\n"
    "ds_read_b64_tr_b16 a[152:153], v210 offset:40960\n"
    "v_mfma_f32_16x16x32_bf16 v[0:3], a[136:139], a[200:203], v[0:3]\n"
    "s_add_u32 m0, s22, 49152\n"
    "s_add_u32 s84, s48, 40960\n"
    "ds_read_b64_tr_b16 a[154:155], v210 offset:43008\n"
    "ds_read_b64_tr_b16 a[156:157], v210 offset:45056\n"
    "v_mfma_f32_16x16x32_bf16 v[0:3], a[140:143], a[204:207], v[0:3]\n"
    "v_cvt_pk_bf16_f32 v64, v64, v65\n"
    "v_cvt_pk_bf16_f32 v65, v66, v67\n"
    "v_cvt_pk_bf16_f32 v66, v68, v69\n"
    "v_cvt_pk_bf16_f32 v67, v70, v71\n"
    "v_cvt_pk_bf16_f32 v80, v80, v81\n"
    "v_mfma_f32_16x16x32_bf16 v[8:11], a[140:143], a[220:223], v[8:11]\n"
    "v_cvt_pk_bf16_f32 v81, v82, v83\n"
    "v_cvt_pk_bf16_f32 v82, v84, v85\n"
    "v_cvt_pk_bf16_f32 v83, v86, v87\n"
    "v_cvt_pk_bf16_f32 v72, v72, v73\n"
    "v_cvt_pk_bf16_f32 v73, v74, v75\n"
    "v_mfma_f32_16x16x32_bf16 v[8:11], a[136:139], a[216:219], v[8:11]\n"
    "ds_read_b64_tr_b16 a[158:159], v210 offset:47104\n"
    "v_cvt_pk_bf16_f32 v74, v76, v77\n"
    "v_cvt_pk_bf16_f32 v75, v78, v79\n"
    "v_cvt_pk_bf16_f32 v88, v88, v89\n"
    "v_cvt_pk_bf16_f32 v89, v90, v91\n"
    "v_mfma_f32_16x16x32_bf16 v[8:11], a[132:135], a[212:215], v[8:11]\n"
    "v_cvt_pk_bf16_f32 v90, v92, v93\n"
    "v_cvt_pk_bf16_f32 v91, v94, v95\n"
    "v_cvt_pk_bf16_f32 v96, v96, v97\n"
    "v_cvt_pk_bf16_f32 v97, v98, v99\n"
    "v_cvt_pk_bf16_f32 v98, v100, v101\n"
    "v_mfma_f32_16x16x32_bf16 v[8:11], a[128:131], a[208:211], v[8:11]\n"
    "s_waitcnt lgkmcnt(1)\n"
    "v_cvt_pk_bf16_f32 v99, v102, v103\n"
    "v_cvt_pk_bf16_f32 v112, v112, v113\n"
    "v_cvt_pk_bf16_f32 v113, v114, v115\n"
    "v_cvt_pk_bf16_f32 v114, v116, v117\n"
    "v_cvt_pk_bf16_f32 v115, v118, v119\n"
    "v_mfma_f32_16x16x32_bf16 v[12:15], a[144:147], a[208:211], v[12:15]\n"
    "buffer_load_dwordx4 v180, s[72:75], s84 offen offset:0 sc1 lds\n"
    "buffer_load_dwordx4 v180, s[72:75], s84 offen offset:1024 sc1 lds\n"
    "buffer_load_dwordx4 v180, s[72:75], s84 offen offset:2048 sc1 lds\n"
    "v_cvt_pk_bf16_f32 v104, v104, v105\n"
    "v_cvt_pk_bf16_f32 v105, v106, v107\n"
    "v_mfma_f32_16x16x32_bf16 v[12:15], a[148:151], a[212:215], v[12:15]\n"
    "buffer_load_dwordx4 v180, s[72:75], s84 offen offset:3072 sc1 lds\n"
    "v_cvt_pk_bf16_f32 v106, v108, v109\n"
    "v_cvt_pk_bf16_f32 v107, v110, v111\n"
    "v_cvt_pk_bf16_f32 v120, v120, v121\n"
    "v_cvt_pk_bf16_f32 v121, v122, v123\n"
    "v_mfma_f32_16x16x32_bf16 v[12:15], a[152:155], a[216:219], v[12:15]\n"
    "s_waitcnt lgkmcnt(0)\n"
    "s_add_u32 m0, s22, 32768\n"
    "s_add_u32 s84, s46, 1310720\n"
    "ds_read_b64_tr_b16 a[160:161], v211 offset:0\n"
    "ds_read_b64_tr_b16 a[162:163], v211 offset:2048\n"
    "ds_read_b128 a[224:227], v215 offset:0\n"
    "v_cvt_pk_bf16_f32 v122, v124, v125\n"
    "v_cvt_pk_bf16_f32 v123, v126, v127\n"
    "v_mfma_f32_16x16x32_bf16 v[12:15], a[156:159], a[220:223], v[12:15]\n"
    "ds_read_b64_tr_b16 a[164:165], v211 offset:4096\n"
    "ds_read_b64_tr_b16 a[166:167], v211 offset:6144\n"
    "ds_read_b128 a[228:231], v215 offset:1024\n"
    "v_mfma_f32_16x16x32_bf16 v[4:7], a[156:159], a[204:207], v[4:7]\n"
    "ds_read_b64_tr_b16 a[168:169], v211 offset:8192\n"
    "ds_read_b64_tr_b16 a[170:171], v211 offset:10240\n"
    "ds_read_b128 a[232:235], v215 offset:2048\n"
    "v_mfma_f32_16x16x32_bf16 v[4:7], a[152:155], a[200:203], v[4:7]\n"
    "ds_read_b64_tr_b16 a[172:173], v211 offset:12288\n"
    "ds_read_b64_tr_b16 a[174:175], v211 offset:14336\n"
    "ds_read_b128 a[236:239], v215 offset:3072\n"
    "v_mfma_f32_16x16x32_bf16 v[4:7], a[148:151], a[196:199], v[4:7]\n"
    "buffer_load_dwordx4 v196, s[60:63], s84 offen offset:0 lds\n"
    "buffer_load_dwordx4 v197, s[60:63], s84 offen offset:1024 lds\n"
    "buffer_load_dwordx4 v198, s[60:63], s84 offen offset:2048 lds\n"
    "v_mfma_f32_16x16x32_bf16 v[4:7], a[144:147], a[192:195], v[4:7]\n"
    "s_waitcnt lgkmcnt(3)\n"
    "buffer_load_dwordx4 v199, s[60:63], s84 offen offset:3072 lds\n"
    "buffer_store_dwordx4 v[64:67], v180, s[40:43], s97 offen offset:0 sc1\n"
    "buffer_store_dwordx4 v[80:83], v180, s[40:43], s97 offen offset:1024 sc1\n"
    "v_mfma_f32_16x16x32_bf16 v[0:3], a[160:163], a[224:227], v[0:3]\n"
    "s_waitcnt vmcnt(18) lgkmcnt(12)\n"
    "buffer_store_dwordx4 v[72:75], v180, s[40:43], s97 offen offset:2048 sc1\n"
    "buffer_store_dwordx4 v[88:91], v180, s[40:43], s97 offen offset:3072 sc1\n"
    "buffer_store_dwordx4 v[96:99], v180, s[40:43], s99 offen offset:0 sc1\n"
    "v_mfma_f32_16x16x32_bf16 v[0:3], a[164:167], a[228:231], v[0:3]\n"
    "s_barrier\n"
    "buffer_store_dwordx4 v[112:115], v180, s[40:43], s99 offen offset:1024 sc1\n"
    "buffer_store_dwordx4 v[104:107], v180, s[40:43], s99 offen offset:2048 sc1\n"
    "buffer_store_dwordx4 v[120:123], v180, s[40:43], s99 offen offset:3072 sc1\n"
    "v_mfma_f32_16x16x32_bf16 v[0:3], a[168:171], a[232:235], v[0:3]\n"
    "s_waitcnt lgkmcnt(0)\n"
    "s_add_u32 m0, s22, 114688\n"
    "s_add_u32 s84, s48, 45056\n"
    "ds_read_b128 a[252:255], v215 offset:7168\n"
    "ds_read_b128 a[248:251], v215 offset:6144\n"
    "ds_read_b128 a[244:247], v215 offset:5120\n"
    "v_mfma_f32_16x16x32_bf16 v[0:3], a[172:175], a[236:239], v[0:3]\n"
    "s_waitcnt lgkmcnt(2)\n"
    "ds_read_b128 a[240:243], v215 offset:4096\n"
    "ds_read_b64_tr_b16 a[176:177], v212 offset:0\n"
    "ds_read_b64_tr_b16 a[178:179], v212 offset:2048\n"
    "v_mfma_f32_16x16x32_bf16 v[8:11], a[172:175], a[252:255], v[8:11]\n"
    "s_waitcnt lgkmcnt(4)\n"
    "ds_read_b64_tr_b16 a[180:181], v212 offset:4096\n"
    "ds_read_b64_tr_b16 a[182:183], v212 offset:6144\n"
    "ds_read_b64_tr_b16 a[184:185], v212 offset:8192\n"
    "v_mfma_f32_16x16x32_bf16 v[8:11], a[168:171], a[248:251], v[8:11]\n"
    "s_waitcnt lgkmcnt(6)\n"
    "ds_read_b64_tr_b16 a[186:187], v212 offset:10240\n"
    "ds_read_b64_tr_b16 a[188:189], v212 offset:12288\n"
    "ds_read_b64_tr_b16 a[190:191], v212 offset:14336\n"
    "v_mfma_f32_16x16x32_bf16 v[8:11], a[164:167], a[244:247], v[8:11]\n"
    "s_waitcnt lgkmcnt(6)\n"
    "buffer_load_dwordx4 v180, s[72:75], s84 offen offset:0 sc1 lds\n"
    "buffer_load_dwordx4 v180, s[72:75], s84 offen offset:1024 sc1 lds\n"
    "v_mfma_f32_16x16x32_bf16 v[8:11], a[160:163], a[240:243], v[8:11]\n"
    "buffer_load_dwordx4 v180, s[72:75], s84 offen offset:2048 sc1 lds\n"
    "buffer_load_dwordx4 v180, s[72:75], s84 offen offset:3072 sc1 lds\n"
    "v_mfma_f32_16x16x32_bf16 v[12:15], a[176:179], a[240:243], v[12:15]\n"
    "s_waitcnt lgkmcnt(0)\n"
    "s_add_u32 m0, s22, 98304\n"
    "s_add_u32 s84, s46, 1441792\n"
    "ds_read_b64_tr_b16 a[128:129], v209 offset:0\n"
    "ds_read_b64_tr_b16 a[130:131], v209 offset:2048\n"
    "v_mfma_f32_16x16x32_bf16 v[12:15], a[180:183], a[244:247], v[12:15]\n"
    "ds_read_b128 a[192:195], v214 offset:0\n"
    "ds_read_b64_tr_b16 a[132:133], v209 offset:4096\n"
    "v_mfma_f32_16x16x32_bf16 v[12:15], a[184:187], a[248:251], v[12:15]\n"
    "ds_read_b64_tr_b16 a[134:135], v209 offset:6144\n"
    "ds_read_b128 a[196:199], v214 offset:1024\n"
    "v_mfma_f32_16x16x32_bf16 v[12:15], a[188:191], a[252:255], v[12:15]\n"
    "ds_read_b64_tr_b16 a[136:137], v209 offset:8192\n"
    "ds_read_b64_tr_b16 a[138:139], v209 offset:10240\n"
    "v_mfma_f32_16x16x32_bf16 v[4:7], a[188:191], a[236:239], v[4:7]\n"
    "ds_read_b128 a[200:203], v214 offset:2048\n"
    "ds_read_b64_tr_b16 a[140:141], v209 offset:12288\n"
    "v_mfma_f32_16x16x32_bf16 v[4:7], a[184:187], a[232:235], v[4:7]\n"
    "ds_read_b64_tr_b16 a[142:143], v209 offset:14336\n"
    "ds_read_b128 a[204:207], v214 offset:3072\n"
    "buffer_load_dwordx4 v196, s[60:63], s84 offen offset:0 lds\n"
    "v_mfma_f32_16x16x32_bf16 v[4:7], a[180:183], a[228:231], v[4:7]\n"
    "buffer_load_dwordx4 v197, s[60:63], s84 offen offset:1024 lds\n"
    "buffer_load_dwordx4 v198, s[60:63], s84 offen offset:2048 lds\n"
    "buffer_load_dwordx4 v199, s[60:63], s84 offen offset:3072 lds\n"
    "v_mfma_f32_16x16x32_bf16 v[4:7], a[176:179], a[224:227], v[4:7]\n"
    "s_waitcnt lgkmcnt(2)\n"
    "ds_read_b128 a[220:223], v214 offset:7168\n"
    "ds_read_b128 a[216:219], v214 offset:6144\n"
    "v_mfma_f32_16x16x32_bf16 v[0:3], a[128:131], a[192:195], v[0:3]\n"
    "s_waitcnt vmcnt(8) lgkmcnt(14)\n"
    "ds_read_b128 a[212:215], v214 offset:5120\n"
    "ds_read_b128 a[208:211], v214 offset:4096\n"
    "v_mfma_f32_16x16x32_bf16 v[0:3], a[132:135], a[196:199], v[0:3]\n"
    "s_barrier\n"
    "s_cmp_lg_u32 s25, 0\n"
    "s_cbranch_scc1 Lpb_s%=\n"
    "v_mov_b32 v184, s92\n"
    "v_mov_b32 v186, s80\n"
    "v_mov_b32 v187, s81\n"
    "s_mov_b64 exec, 1\n"
    "global_store_dwordx2 v184, v[186:187], s[82:83] offset:2048 sc1\n"
    "s_mov_b64 exec, -1\n"
    "Lpb_s%=:\n"
    "ds_read_b64_tr_b16 a[144:145], v210 offset:0\n"
    "ds_read_b64_tr_b16 a[146:147], v210 offset:2048\n"
    "v_mfma_f32_16x16x32_bf16 v[0:3], a[136:139], a[200:203], v[0:3]\n"
    "s_waitcnt lgkmcnt(4)\n"
    "s_add_u32 m0, s22, 147456\n"
    "s_add_u32 s84, s48, 49152\n"
    "ds_read_b64_tr_b16 a[148:149], v210 offset:4096\n"
    "ds_read_b64_tr_b16 a[150:151], v210 offset:6144\n"
    "v_mfma_f32_16x16x32_bf16 v[0:3], a[140:143], a[204:207], v[0:3]\n"
    "ds_read_b64_tr_b16 a[152:153], v210 offset:8192\n"
    "ds_read_b64_tr_b16 a[154:155], v210 offset:10240\n"
    "v_mfma_f32_16x16x32_bf16 v[8:11], a[140:143], a[220:223], v[8:11]\n"
    "ds_read_b64_tr_b16 a[156:157], v210 offset:12288\n"
    "ds_read_b64_tr_b16 a[158:159], v210 offset:14336\n"
    "v_mfma_f32_16x16x32_bf16 v[8:11], a[136:139], a[216:219], v[8:11]\n"
    "s_waitcnt lgkmcnt(4)\n"
    "buffer_load_dwordx4 v180, s[72:75], s84 offen offset:0 sc1 lds\n"
    "buffer_load_dwordx4 v180, s[72:75], s84 offen offset:1024 sc1 lds\n"
    "v_mfma_f32_16x16x32_bf16 v[8:11], a[132:135], a[212:215], v[8:11]\n"
    "buffer_load_dwordx4 v180, s[72:75], s84 offen offset:2048 sc1 lds\n"
    "buffer_load_dwordx4 v180, s[72:75], s84 offen offset:3072 sc1 lds\n"
    "v_mfma_f32_16x16x32_bf16 v[8:11], a[128:131], a[208:211], v[8:11]\n"
    "s_add_u32 m0, s22, 131072\n"
    "s_add_u32 s84, s46, 1572864\n"
    "ds_read_b64_tr_b16 a[160:161], v207 offset:0\n"
    "ds_read_b64_tr_b16 a[162:163], v207 offset:2048\n"
    "v_mfma_f32_16x16x32_bf16 v[12:15], a[144:147], a[208:211], v[12:15]\n"
    "ds_read_b128 a[224:227], v213 offset:0\n"
    "ds_read_b64_tr_b16 a[164:165], v207 offset:4096\n"
    "v_mfma_f32_16x16x32_bf16 v[12:15], a[148:151], a[212:215], v[12:15]\n"
    "s_waitcnt lgkmcnt(4)\n"
    "ds_read_b64_tr_b16 a[166:167], v207 offset:6144\n"
    "ds_read_b128 a[228:231], v213 offset:1024\n"
    "v_mfma_f32_16x16x32_bf16 v[12:15], a[152:155], a[216:219], v[12:15]\n"
    "ds_read_b64_tr_b16 a[168:169], v207 offset:8192\n"
    "ds_read_b64_tr_b16 a[170:171], v207 offset:10240\n"
    "v_mfma_f32_16x16x32_bf16 v[12:15], a[156:159], a[220:223], v[12:15]\n"
    "ds_read_b128 a[232:235], v213 offset:2048\n"
    "ds_read_b64_tr_b16 a[172:173], v207 offset:12288\n"
    "v_mfma_f32_16x16x32_bf16 v[4:7], a[156:159], a[204:207], v[4:7]\n"
    "ds_read_b64_tr_b16 a[174:175], v207 offset:14336\n"
    "ds_read_b128 a[236:239], v213 offset:3072\n"
    "v_mfma_f32_16x16x32_bf16 v[4:7], a[152:155], a[200:203], v[4:7]\n"
    "buffer_load_dwordx4 v196, s[60:63], s84 offen offset:0 lds\n"
    "buffer_load_dwordx4 v197, s[60:63], s84 offen offset:1024 lds\n"
    "buffer_load_dwordx4 v198, s[60:63], s84 offen offset:2048 lds\n"
    "v_mfma_f32_16x16x32_bf16 v[4:7], a[148:151], a[196:199], v[4:7]\n"
    "buffer_load_dwordx4 v199, s[60:63], s84 offen offset:3072 lds\n"
    "ds_read_b128 a[252:255], v213 offset:7168\n"
    "v_mfma_f32_16x16x32_bf16 v[4:7], a[144:147], a[192:195], v[4:7]\n"
    "s_waitcnt lgkmcnt(1)\n"
    "ds_read_b128 a[248:251], v213 offset:6144\n"
    "ds_read_b128 a[244:247], v213 offset:5120\n"
    "v_mfma_f32_16x16x32_bf16 v[0:3], a[160:163], a[224:227], v[0:3]\n"
    "s_waitcnt vmcnt(24) lgkmcnt(15)\n"
    "ds_read_b128 a[240:243], v213 offset:4096\n"
    "ds_read_b64_tr_b16 a[176:177], v208 offset:0\n"
    "v_mfma_f32_16x16x32_bf16 v[0:3], a[164:167], a[228:231], v[0:3]\n"
    "s_barrier\n"
    "ds_read_b64_tr_b16 a[178:179], v208 offset:2048\n"
    "ds_read_b64_tr_b16 a[180:181], v208 offset:4096\n"
    "v_mfma_f32_16x16x32_bf16 v[0:3], a[168:171], a[232:235], v[0:3]\n"
    "s_add_u32 m0, s22, 81920\n"
    "s_add_u32 s84, s48, 53248\n"
    "ds_read_b64_tr_b16 a[182:183], v208 offset:6144\n"
    "ds_read_b64_tr_b16 a[184:185], v208 offset:8192\n"
    "v_mfma_f32_16x16x32_bf16 v[0:3], a[172:175], a[236:239], v[0:3]\n"
    "s_waitcnt lgkmcnt(4)\n"
    "ds_read_b64_tr_b16 a[186:187], v208 offset:10240\n"
    "ds_read_b64_tr_b16 a[188:189], v208 offset:12288\n"
    "v_mfma_f32_16x16x32_bf16 v[8:11], a[172:175], a[252:255], v[8:11]\n"
    "ds_read_b64_tr_b16 a[190:191], v208 offset:14336\n"
    "buffer_load_dwordx4 v180, s[72:75], s84 offen offset:0 sc1 lds\n"
    "v_mfma_f32_16x16x32_bf16 v[8:11], a[168:171], a[248:251], v[8:11]\n"
    "buffer_load_dwordx4 v180, s[72:75], s84 offen offset:1024 sc1 lds\n"
    "buffer_load_dwordx4 v180, s[72:75], s84 offen offset:2048 sc1 lds\n"
    "v_mfma_f32_16x16x32_bf16 v[8:11], a[164:167], a[244:247], v[8:11]\n"
    "buffer_load_dwordx4 v180, s[72:75], s84 offen offset:3072 sc1 lds\n"
    "ds_read_b64_tr_b16 a[128:129], v207 offset:32768\n"
    "v_mfma_f32_16x16x32_bf16 v[8:11], a[160:163], a[240:243], v[8:11]\n"
    "s_waitcnt lgkmcnt(1)\n"
    "s_add_u32 m0, s22, 65536\n"
    "s_add_u32 s84, s46, 1703936\n"
    "ds_read_b64_tr_b16 a[130:131], v207 offset:34816\n"
    "ds_read_b128 a[192:195], v213 offset:32768\n"
    "v_mfma_f32_16x16x32_bf16 v[12:15], a[176:179], a[240:243], v[12:15]\n"
    "ds_read_b64_tr_b16 a[132:133], v207 offset:36864\n"
    "ds_read_b64_tr_b16 a[134:135], v207 offset:38912\n"
    "v_mfma_f32_16x16x32_bf16 v[12:15], a[180:183], a[244:247], v[12:15]\n"
    "ds_read_b128 a[196:199], v213 offset:33792\n"
    "ds_read_b64_tr_b16 a[136:137], v207 offset:40960\n"
    "v_mfma_f32_16x16x32_bf16 v[12:15], a[184:187], a[248:251], v[12:15]\n"
    "ds_read_b64_tr_b16 a[138:139], v207 offset:43008\n"
    "ds_read_b128 a[200:203], v213 offset:34816\n"
    "v_mfma_f32_16x16x32_bf16 v[12:15], a[188:191], a[252:255], v[12:15]\n"
    "ds_read_b64_tr_b16 a[140:141], v207 offset:45056\n"
    "ds_read_b64_tr_b16 a[142:143], v207 offset:47104\n"
    "v_mfma_f32_16x16x32_bf16 v[4:7], a[188:191], a[236:239], v[4:7]\n"
    "ds_read_b128 a[204:207], v213 offset:35840\n"
    "buffer_load_dwordx4 v196, s[60:63], s84 offen offset:0 lds\n"
    "v_mfma_f32_16x16x32_bf16 v[4:7], a[184:187], a[232:235], v[4:7]\n"
    "buffer_load_dwordx4 v197, s[60:63], s84 offen offset:1024 lds\n"
    "buffer_load_dwordx4 v198, s[60:63], s84 offen offset:2048 lds\n"
    "buffer_load_dwordx4 v199, s[60:63], s84 offen offset:3072 lds\n"
    "v_mfma_f32_16x16x32_bf16 v[4:7], a[180:183], a[228:231], v[4:7]\n"
    "ds_read_b128 a[220:223], v213 offset:39936\n"
    "ds_read_b128 a[216:219], v213 offset:38912\n"
    "v_mfma_f32_16x16x32_bf16 v[4:7], a[176:179], a[224:227], v[4:7]\n"
    "s_waitcnt lgkmcnt(2)\n"
    "ds_read_b128 a[212:215], v213 offset:37888\n"
    "ds_read_b128 a[208:211], v213 offset:36864\n"
    "v_mfma_f32_16x16x32_bf16 v[0:3], a[128:131], a[192:195], v[0:3]\n"
    "s_waitcnt vmcnt(16) lgkmcnt(15)\n"
    "ds_read_b64_tr_b16 a[144:145], v208 offset:32768\n"
    "ds_read_b64_tr_b16 a[146:147], v208 offset:34816\n"
    "v_mfma_f32_16x16x32_bf16 v[0:3], a[132:135], a[196:199], v[0:3]\n"
    "s_barrier\n"
    "s_cmp_lg_u32 s97, 0x80000000\n"
    "s_cbranch_scc1 Low%=\n"
    "ds_write_b128 v173, v[64:67] offset:0\n"
    "ds_write_b128 v173, v[80:83] offset:1024\n"
    "ds_write_b128 v173, v[72:75] offset:2048\n"
    "ds_write_b128 v173, v[88:91] offset:3072\n"
    "ds_write_b128 v173, v[96:99] offset:4096\n"
    "ds_write_b128 v173, v[112:115] offset:5120\n"
    "ds_write_b128 v173, v[104:107] offset:6144\n"
    "ds_write_b128 v173, v[120:123] offset:7168\n"
    "s_waitcnt lgkmcnt(0)\n"
    "Low%=:\n"
    "ds_read_b64_tr_b16 a[148:149], v208 offset:36864\n"
    "ds_read_b64_tr_b16 a[150:151], v208 offset:38912\n"
    "v_mfma_f32_16x16x32_bf16 v[0:3], a[136:139], a[200:203], v[0:3]\n"
    "ds_read_b64_tr_b16 a[152:153], v208 offset:40960\n"
    "ds_read_b64_tr_b16 a[154:155], v208 offset:43008\n"
    "v_mfma_f32_16x16x32_bf16 v[0:3], a[140:143], a[204:207], v[0:3]\n"
    "s_waitcnt lgkmcnt(4)\n"
    "ds_read_b64_tr_b16 a[156:157], v208 offset:45056\n"
    "ds_read_b64_tr_b16 a[158:159], v208 offset:47104\n"
    "v_mfma_f32_16x16x32_bf16 v[8:11], a[140:143], a[220:223], v[8:11]\n"
    "ds_read_b64_tr_b16 a[160:161], v209 offset:32768\n"
    "ds_read_b64_tr_b16 a[162:163], v209 offset:34816\n"
    "v_mfma_f32_16x16x32_bf16 v[8:11], a[136:139], a[216:219], v[8:11]\n"
    "ds_read_b128 a[224:227], v214 offset:32768\n"
    "ds_read_b64_tr_b16 a[164:165], v209 offset:36864\n"
    "v_mfma_f32_16x16x32_bf16 v[8:11], a[132:135], a[212:215], v[8:11]\n"
    "ds_read_b64_tr_b16 a[166:167], v209 offset:38912\n"
    "ds_read_b128 a[228:231], v214 offset:33792\n"
    "v_mfma_f32_16x16x32_bf16 v[8:11], a[128:131], a[208:211], v[8:11]\n"
    "ds_read_b64_tr_b16 a[168:169], v209 offset:40960\n"
    "ds_read_b64_tr_b16 a[170:171], v209 offset:43008\n"
    "v_mfma_f32_16x16x32_bf16 v[12:15], a[144:147], a[208:211], v[12:15]\n"
    "s_waitcnt lgkmcnt(4)\n"
    "ds_read_b128 a[232:235], v214 offset:34816\n"
    "ds_read_b64_tr_b16 a[172:173], v209 offset:45056\n"
    "v_mfma_f32_16x16x32_bf16 v[12:15], a[148:151], a[212:215], v[12:15]\n"
    "ds_read_b64_tr_b16 a[174:175], v209 offset:47104\n"
    "ds_read_b128 a[236:239], v214 offset:35840\n"
    "v_mfma_f32_16x16x32_bf16 v[12:15], a[152:155], a[216:219], v[12:15]\n"
    "global_load_dwordx2 v[222:223], v219, s[82:83] offset:2048 sc1\n"
    "global_load_dwordx2 v[160:161], v216, s[36:37] offset:0\n"
    "v_mfma_f32_16x16x32_bf16 v[12:15], a[156:159], a[220:223], v[12:15]\n"
    "global_load_dwordx2 v[162:163], v216, s[36:37] offset:32\n"
    "global_load_dwordx2 v[164:165], v217, s[36:37] offset:0\n"
    "v_mfma_f32_16x16x32_bf16 v[4:7], a[156:159], a[204:207], v[4:7]\n"
    "global_load_dwordx2 v[166:167], v217, s[36:37] offset:32\n"
    "ds_read_b128 a[252:255], v214 offset:39936\n"
    "v_mfma_f32_16x16x32_bf16 v[4:7], a[152:155], a[200:203], v[4:7]\n"
    "ds_read_b128 a[248:251], v214 offset:38912\n"
    "ds_read_b128 a[244:247], v214 offset:37888\n"
    "v_mfma_f32_16x16x32_bf16 v[4:7], a[148:151], a[196:199], v[4:7]\n"
    "ds_read_b128 a[240:243], v214 offset:36864\n"
    "ds_read_b64_tr_b16 a[176:177], v210 offset:32768\n"
    "v_mfma_f32_16x16x32_bf16 v[4:7], a[144:147], a[192:195], v[4:7]\n"
    "ds_read_b64_tr_b16 a[178:179], v210 offset:34816\n"
    "ds_read_b64_tr_b16 a[180:181], v210 offset:36864\n"
    "v_mfma_f32_16x16x32_bf16 v[0:3], a[160:163], a[224:227], v[0:3]\n"
    "s_waitcnt vmcnt(13) lgkmcnt(4)\n"
    "ds_read_b64_tr_b16 a[182:183], v210 offset:38912\n"
    "ds_read_b64_tr_b16 a[184:185], v210 offset:40960\n"
    "v_mfma_f32_16x16x32_bf16 v[0:3], a[164:167], a[228:231], v[0:3]\n"
    "s_barrier\n"
    "ds_read_b64_tr_b16 a[186:187], v210 offset:43008\n"
    "ds_read_b64_tr_b16 a[188:189], v210 offset:45056\n"
    "v_mfma_f32_16x16x32_bf16 v[0:3], a[168:171], a[232:235], v[0:3]\n"
    "s_waitcnt vmcnt(4)\n"
    "s_mov_b32 s28, 0x40000\n"
    "Lpk_s%=:\n"
    "v_cmp_ne_u64 vcc, s[80:81], v[222:223]\n"
    "s_cmp_eq_u64 vcc, 0\n"
    "s_cbranch_scc1 Lpkd_s%=\n"
    "global_load_dwordx2 v[222:223], v219, s[82:83] offset:2048 sc1\n"
    "s_waitcnt vmcnt(0)\n"
    "s_sub_u32 s28, s28, 1\n"
    "s_cbranch_scc0 Lpk_s%=\n"
    "Lpkd_s%=:\n"
    "ds_read_b64_tr_b16 a[190:191], v210 offset:47104\n"
    "ds_read_b64_tr_b16 a[128:129], v211 offset:0\n"
    "v_mfma_f32_16x16x32_bf16 v[0:3], a[172:175], a[236:239], v[0:3]\n"
    "ds_read_b64_tr_b16 a[130:131], v211 offset:2048\n"
    "ds_read_b128 a[192:195], v215 offset:0\n"
    "v_mfma_f32_16x16x32_bf16 v[8:11], a[172:175], a[252:255], v[8:11]\n"
    "ds_read_b64_tr_b16 a[132:133], v211 offset:4096\n"
    "ds_read_b64_tr_b16 a[134:135], v211 offset:6144\n"
    "v_mfma_f32_16x16x32_bf16 v[8:11], a[168:171], a[248:251], v[8:11]\n"
    "ds_read_b128 a[196:199], v215 offset:1024\n"
    "ds_read_b64_tr_b16 a[136:137], v211 offset:8192\n"
    "v_mfma_f32_16x16x32_bf16 v[8:11], a[164:167], a[244:247], v[8:11]\n"
    "s_waitcnt lgkmcnt(4)\n"
    "ds_read_b64_tr_b16 a[138:139], v211 offset:10240\n"
    "ds_read_b128 a[200:203], v215 offset:2048\n"
    "v_mfma_f32_16x16x32_bf16 v[8:11], a[160:163], a[240:243], v[8:11]\n"
    "ds_read_b64_tr_b16 a[140:141], v211 offset:12288\n"
    "ds_read_b64_tr_b16 a[142:143], v211 offset:14336\n"
    "v_mfma_f32_16x16x32_bf16 v[12:15], a[176:179], a[240:243], v[12:15]\n"
    "ds_read_b128 a[204:207], v215 offset:3072\n"
    "ds_read_b128 a[220:223], v215 offset:7168\n"
    "v_mfma_f32_16x16x32_bf16 v[12:15], a[180:183], a[244:247], v[12:15]\n"
    "ds_read_b128 a[216:219], v215 offset:6144\n"
    "ds_read_b128 a[212:215], v215 offset:5120\n"
    "v_mfma_f32_16x16x32_bf16 v[12:15], a[184:187], a[248:251], v[12:15]\n"
    "ds_read_b128 a[208:211], v215 offset:4096\n"
    "ds_read_b64_tr_b16 a[144:145], v212 offset:0\n"
    "v_mfma_f32_16x16x32_bf16 v[12:15], a[188:191], a[252:255], v[12:15]\n"
    "ds_read_b64_tr_b16 a[146:147], v212 offset:2048\n"
    "ds_read_b128 v[240:243], v174 offset:0\n"
    "v_mfma_f32_16x16x32_bf16 v[4:7], a[188:191], a[236:239], v[4:7]\n"
    "ds_read_b128 v[244:247], v174 offset:1024\n"
    "buffer_load_dwordx4 v[248:251], v180, s[40:43], s38 offen offset:0 sc1\n"
    "v_mfma_f32_16x16x32_bf16 v[4:7], a[184:187], a[232:235], v[4:7]\n"
    "buffer_load_dwordx4 v[252:255], v180, s[40:43], s38 offen offset:1024 sc1\n"
    "buffer_load_dwordx4 v[48:51], v180, s[40:43], s39 offen offset:0 sc1\n"
    "v_mfma_f32_16x16x32_bf16 v[4:7], a[180:183], a[228:231], v[4:7]\n"
    "buffer_load_dwordx4 v[52:55], v180, s[40:43], s39 offen offset:1024 sc1\n"
    "buffer_load_dwordx4 v[56:59], v180, s[40:43], s85 offen offset:0 sc1\n"
    "v_mfma_f32_16x16x32_bf16 v[4:7], a[176:179], a[224:227], v[4:7]\n"
    "buffer_load_dwordx4 v[60:63], v180, s[40:43], s85 offen offset:1024 sc1\n"
    "ds_read_b64_tr_b16 a[148:149], v212 offset:4096\n"
    "v_mfma_f32_16x16x32_bf16 v[0:3], a[128:131], a[192:195], v[0:3]\n"
    "s_waitcnt vmcnt(11) lgkmcnt(1)\n"
    "ds_read_b64_tr_b16 a[150:151], v212 offset:6144\n"
    "ds_read_b64_tr_b16 a[152:153], v212 offset:8192\n"
    "v_mfma_f32_16x16x32_bf16 v[0:3], a[132:135], a[196:199], v[0:3]\n"
    "s_barrier\n"
    "ds_read_b64_tr_b16 a[154:155], v212 offset:10240\n"
    "ds_read_b64_tr_b16 a[156:157], v212 offset:12288\n"
    "v_mfma_f32_16x16x32_bf16 v[0:3], a[136:139], a[200:203], v[0:3]\n"
    "ds_read_b64_tr_b16 a[158:159], v212 offset:14336\n"
    "ds_read_b64_tr_b16 a[160:161], v209 offset:0\n"
    "v_mfma_f32_16x16x32_bf16 v[0:3], a[140:143], a[204:207], v[0:3]\n"
    "ds_read_b64_tr_b16 a[162:163], v209 offset:2048\n"
    "ds_read_b128 a[224:227], v214 offset:0\n"
    "v_mfma_f32_16x16x32_bf16 v[8:11], a[140:143], a[220:223], v[8:11]\n"
    "ds_read_b64_tr_b16 a[164:165], v209 offset:4096\n"
    "ds_read_b64_tr_b16 a[166:167], v209 offset:6144\n"
    "v_mfma_f32_16x16x32_bf16 v[8:11], a[136:139], a[216:219], v[8:11]\n"
    "ds_read_b128 a[228:231], v214 offset:1024\n"
    "ds_read_b64_tr_b16 a[168:169], v209 offset:8192\n"
    "v_mfma_f32_16x16x32_bf16 v[8:11], a[132:135], a[212:215], v[8:11]\n"
    "ds_read_b64_tr_b16 a[170:171], v209 offset:10240\n"
    "ds_read_b128 a[232:235], v214 offset:2048\n"
    "v_mfma_f32_16x16x32_bf16 v[8:11], a[128:131], a[208:211], v[8:11]\n"
    "ds_read_b64_tr_b16 a[172:173], v209 offset:12288\n"
    "ds_read_b64_tr_b16 a[174:175], v209 offset:14336\n"
    "v_mfma_f32_16x16x32_bf16 v[12:15], a[144:147], a[208:211], v[12:15]\n"
    "s_waitcnt lgkmcnt(4)\n"
    "ds_read_b128 a[236:239], v214 offset:3072\n"
    "ds_read_b128 a[252:255], v214 offset:7168\n"
    "v_mfma_f32_16x16x32_bf16 v[12:15], a[148:151], a[212:215], v[12:15]\n"
    "ds_read_b128 a[248:251], v214 offset:6144\n"
    "ds_read_b128 a[244:247], v214 offset:5120\n"
    "v_mfma_f32_16x16x32_bf16 v[12:15], a[152:155], a[216:219], v[12:15]\n"
    "ds_read_b128 a[240:243], v214 offset:4096\n"
    "ds_read_b64_tr_b16 a[176:177], v210 offset:0\n"
    "v_mfma_f32_16x16x32_bf16 v[12:15], a[156:159], a[220:223], v[12:15]\n"
    "ds_read_b64_tr_b16 a[178:179], v210 offset:2048\n"
    "ds_read_b64_tr_b16 a[180:181], v210 offset:4096\n"
    "v_mfma_f32_16x16x32_bf16 v[4:7], a[156:159], a[204:207], v[4:7]\n"
    "ds_read_b64_tr_b16 a[182:183], v210 offset:6144\n"
    "ds_read_b64_tr_b16 a[184:185], v210 offset:8192\n"
    "v_mfma_f32_16x16x32_bf16 v[4:7], a[152:155], a[200:203], v[4:7]\n"
    "ds_read_b64_tr_b16 a[186:187], v210 offset:10240\n"
    "ds_read_b64_tr_b16 a[188:189], v210 offset:12288\n"
    "v_mfma_f32_16x16x32_bf16 v[4:7], a[148:151], a[196:199], v[4:7]\n"
    "ds_read_b64_tr_b16 a[190:191], v210 offset:14336\n"
    "v_mfma_f32_16x16x32_bf16 v[4:7], a[144:147], a[192:195], v[4:7]\n"
    "v_mfma_f32_16x16x32_bf16 v[0:3], a[160:163], a[224:227], v[0:3]\n"
    "v_mfma_f32_16x16x32_bf16 v[0:3], a[164:167], a[228:231], v[0:3]\n"
    "s_waitcnt lgkmcnt(0)\n"
    "v_mfma_f32_16x16x32_bf16 v[0:3], a[168:171], a[232:235], v[0:3]\n"
    "v_mfma_f32_16x16x32_bf16 v[0:3], a[172:175], a[236:239], v[0:3]\n"
    "v_mfma_f32_16x16x32_bf16 v[8:11], a[172:175], a[252:255], v[8:11]\n"
    "v_mfma_f32_16x16x32_bf16 v[8:11], a[168:171], a[248:251], v[8:11]\n"
    "v_mfma_f32_16x16x32_bf16 v[8:11], a[164:167], a[244:247], v[8:11]\n"
    "v_mfma_f32_16x16x32_bf16 v[8:11], a[160:163], a[240:243], v[8:11]\n"
    "v_mfma_f32_16x16x32_bf16 v[12:15], a[176:179], a[240:243], v[12:15]\n"
    "v_mfma_f32_16x16x32_bf16 v[12:15], a[180:183], a[244:247], v[12:15]\n"
    "v_mfma_f32_16x16x32_bf16 v[12:15], a[184:187], a[248:251], v[12:15]\n"
    "v_mfma_f32_16x16x32_bf16 v[12:15], a[188:191], a[252:255], v[12:15]\n"
    "v_mfma_f32_16x16x32_bf16 v[4:7], a[188:191], a[236:239], v[4:7]\n"
    "v_mfma_f32_16x16x32_bf16 v[4:7], a[184:187], a[232:235], v[4:7]\n"
    "v_mfma_f32_16x16x32_bf16 v[4:7], a[180:183], a[228:231], v[4:7]\n"
    "v_mfma_f32_16x16x32_bf16 v[4:7], a[176:179], a[224:227], v[4:7]\n"
    "s_waitcnt vmcnt(6)\n"
    "v_lshlrev_b32 v32, 16, v160\n"
    "v_and_b32 v33, 0xffff0000, v160\n"
    "v_lshlrev_b32 v34, 16, v161\n"
    "v_and_b32 v35, 0xffff0000, v161\n"
    "v_lshlrev_b32 v36, 16, v162\n"
    "v_and_b32 v37, 0xffff0000, v162\n"
    "v_lshlrev_b32 v38, 16, v163\n"
    "v_and_b32 v39, 0xffff0000, v163\n"
    "v_lshlrev_b32 v40, 16, v164\n"
    "v_and_b32 v41, 0xffff0000, v164\n"
    "v_lshlrev_b32 v42, 16, v165\n"
    "v_and_b32 v43, 0xffff0000, v165\n"
    "v_lshlrev_b32 v44, 16, v166\n"
    "v_and_b32 v45, 0xffff0000, v166\n"
    "v_lshlrev_b32 v46, 16, v167\n"
    "v_and_b32 v47, 0xffff0000, v167\n"
    "v_mul_f32 v16, 0xbfb8aa3b, v0\n"
    "v_mul_f32 v17, 0xbfb8aa3b, v1\n"
    "v_mul_f32 v18, 0xbfb8aa3b, v2\n"
    "v_mul_f32 v19, 0xbfb8aa3b, v3\n"
    "v_mul_f32 v20, 0xbfb8aa3b, v4\n"
    "v_mul_f32 v21, 0xbfb8aa3b, v5\n"
    "v_mul_f32 v22, 0xbfb8aa3b, v6\n"
    "v_mul_f32 v23, 0xbfb8aa3b, v7\n"
    "v_mul_f32 v24, 0xbfb8aa3b, v8\n"
    "v_mul_f32 v25, 0xbfb8aa3b, v9\n"
    "v_mul_f32 v26, 0xbfb8aa3b, v10\n"
    "v_mul_f32 v27, 0xbfb8aa3b, v11\n"
    "v_mul_f32 v28, 0xbfb8aa3b, v12\n"
    "v_mul_f32 v29, 0xbfb8aa3b, v13\n"
    "v_mul_f32 v30, 0xbfb8aa3b, v14\n"
    "v_mul_f32 v31, 0xbfb8aa3b, v15\n"
    "v_exp_f32 v16, v16\n"
    "v_exp_f32 v17, v17\n"
    "v_exp_f32 v18, v18\n"
    "v_exp_f32 v19, v19\n"
    "v_exp_f32 v20, v20\n"
    "v_exp_f32 v21, v21\n"
    "v_exp_f32 v22, v22\n"
    "v_exp_f32 v23, v23\n"
    "v_exp_f32 v24, v24\n"
    "v_exp_f32 v25, v25\n"
    "v_exp_f32 v26, v26\n"
    "v_exp_f32 v27, v27\n"
    "v_exp_f32 v28, v28\n"
    "v_exp_f32 v29, v29\n"
    "v_exp_f32 v30, v30\n"
    "v_exp_f32 v31, v31\n"
    "v_add_f32 v16, 1.0, v16\n"
    "v_add_f32 v17, 1.0, v17\n"
    "v_add_f32 v18, 1.0, v18\n"
    "v_add_f32 v19, 1.0, v19\n"
    "v_add_f32 v20, 1.0, v20\n"
    "v_add_f32 v21, 1.0, v21\n"
    "v_add_f32 v22, 1.0, v22\n"
    "v_add_f32 v23, 1.0, v23\n"
    "v_add_f32 v24, 1.0, v24\n"
    "v_add_f32 v25, 1.0, v25\n"
    "v_add_f32 v26, 1.0, v26\n"
    "v_add_f32 v27, 1.0, v27\n"
    "v_add_f32 v28, 1.0, v28\n"
    "v_add_f32 v29, 1.0, v29\n"
    "v_add_f32 v30, 1.0, v30\n"
    "v_add_f32 v31, 1.0, v31\n"
    "v_rcp_f32 v16, v16\n"
    "v_rcp_f32 v17, v17\n"
    "v_rcp_f32 v18, v18\n"
    "v_rcp_f32 v19, v19\n"
    "v_rcp_f32 v20, v20\n"
    "v_rcp_f32 v21, v21\n"
    "v_rcp_f32 v22, v22\n"
    "v_rcp_f32 v23, v23\n"
    "v_rcp_f32 v24, v24\n"
    "v_rcp_f32 v25, v25\n"
    "v_rcp_f32 v26, v26\n"
    "v_rcp_f32 v27, v27\n"
    "v_rcp_f32 v28, v28\n"
    "v_rcp_f32 v29, v29\n"
    "v_rcp_f32 v30, v30\n"
    "v_rcp_f32 v31, v31\n"
    "v_mul_f32 v0, v0, v16\n"
    "v_mul_f32 v1, v1, v17\n"
    "v_mul_f32 v2, v2, v18\n"
    "v_mul_f32 v3, v3, v19\n"
    "v_mul_f32 v4, v4, v20\n"
    "v_mul_f32 v5, v5, v21\n"
    "v_mul_f32 v6, v6, v22\n"
    "v_mul_f32 v7, v7, v23\n"
    "v_mul_f32 v8, v8, v24\n"
    "v_mul_f32 v9, v9, v25\n"
    "v_mul_f32 v10, v10, v26\n"
    "v_mul_f32 v11, v11, v27\n"
    "v_mul_f32 v12, v12, v28\n"
    "v_mul_f32 v13, v13, v29\n"
    "v_mul_f32 v14, v14, v30\n"
    "v_mul_f32 v15, v15, v31\n"
    "s_waitcnt lgkmcnt(0)\n"
    "v_lshlrev_b32 v128, 16, v240\n"
    "v_and_b32 v129, 0xffff0000, v240\n"
    "v_lshlrev_b32 v130, 16, v241\n"
    "v_and_b32 v131, 0xffff0000, v241\n"
    "v_lshlrev_b32 v132, 16, v242\n"
    "v_and_b32 v133, 0xffff0000, v242\n"
    "v_lshlrev_b32 v134, 16, v243\n"
    "v_and_b32 v135, 0xffff0000, v243\n"
    "v_lshlrev_b32 v136, 16, v244\n"
    "v_and_b32 v137, 0xffff0000, v244\n"
    "v_lshlrev_b32 v138, 16, v245\n"
    "v_and_b32 v139, 0xffff0000, v245\n"
    "v_lshlrev_b32 v140, 16, v246\n"
    "v_and_b32 v141, 0xffff0000, v246\n"
    "v_lshlrev_b32 v142, 16, v247\n"
    "v_and_b32 v143, 0xffff0000, v247\n"
    "s_waitcnt vmcnt(4)\n"
    "v_lshlrev_b32 v144, 16, v248\n"
    "v_and_b32 v145, 0xffff0000, v248\n"
    "v_lshlrev_b32 v146, 16, v249\n"
    "v_and_b32 v147, 0xffff0000, v249\n"
    "v_lshlrev_b32 v148, 16, v250\n"
    "v_and_b32 v149, 0xffff0000, v250\n"
    "v_lshlrev_b32 v150, 16, v251\n"
    "v_and_b32 v151, 0xffff0000, v251\n"
    "v_lshlrev_b32 v152, 16, v252\n"
    "v_and_b32 v153, 0xffff0000, v252\n"
    "v_lshlrev_b32 v154, 16, v253\n"
    "v_and_b32 v155, 0xffff0000, v253\n"
    "v_lshlrev_b32 v156, 16, v254\n"
    "v_and_b32 v157, 0xffff0000, v254\n"
    "v_lshlrev_b32 v158, 16, v255\n"
    "v_and_b32 v159, 0xffff0000, v255\n"
    "v_pk_add_f32 v[128:129], v[128:129], v[144:145]\n"
    "v_pk_add_f32 v[130:131], v[130:131], v[146:147]\n"
    "v_pk_add_f32 v[132:133], v[132:133], v[148:149]\n"
    "v_pk_add_f32 v[134:135], v[134:135], v[150:151]\n"
    "v_pk_add_f32 v[136:137], v[136:137], v[152:153]\n"
    "v_pk_add_f32 v[138:139], v[138:139], v[154:155]\n"
    "v_pk_add_f32 v[140:141], v[140:141], v[156:157]\n"
    "v_pk_add_f32 v[142:143], v[142:143], v[158:159]\n"
    "s_waitcnt vmcnt(2)\n"
    "v_lshlrev_b32 v144, 16, v48\n"
    "v_and_b32 v145, 0xffff0000, v48\n"
    "v_lshlrev_b32 v146, 16, v49\n"
    "v_and_b32 v147, 0xffff0000, v49\n"
    "v_lshlrev_b32 v148, 16, v50\n"
    "v_and_b32 v149, 0xffff0000, v50\n"
    "v_lshlrev_b32 v150, 16, v51\n"
    "v_and_b32 v151, 0xffff0000, v51\n"
    "v_lshlrev_b32 v152, 16, v52\n"
    "v_and_b32 v153, 0xffff0000, v52\n"
    "v_lshlrev_b32 v154, 16, v53\n"
    "v_and_b32 v155, 0xffff0000, v53\n"
    "v_lshlrev_b32 v156, 16, v54\n"
    "v_and_b32 v157, 0xffff0000, v54\n"
    "v_lshlrev_b32 v158, 16, v55\n"
    "v_and_b32 v159, 0xffff0000, v55\n"
    "v_pk_add_f32 v[128:129], v[128:129], v[144:145]\n"
    "v_pk_add_f32 v[130:131], v[130:131], v[146:147]\n"
    "v_pk_add_f32 v[132:133], v[132:133], v[148:149]\n"
    "v_pk_add_f32 v[134:135], v[134:135], v[150:151]\n"
    "v_pk_add_f32 v[136:137], v[136:137], v[152:153]\n"
    "v_pk_add_f32 v[138:139], v[138:139], v[154:155]\n"
    "v_pk_add_f32 v[140:141], v[140:141], v[156:157]\n"
    "v_pk_add_f32 v[142:143], v[142:143], v[158:159]\n"
    "s_waitcnt vmcnt(0)\n"
    "v_lshlrev_b32 v144, 16, v56\n"
    "v_and_b32 v145, 0xffff0000, v56\n"
    "v_lshlrev_b32 v146, 16, v57\n"
    "v_and_b32 v147, 0xffff0000, v57\n"
    "v_lshlrev_b32 v148, 16, v58\n"
    "v_and_b32 v149, 0xffff0000, v58\n"
    "v_lshlrev_b32 v150, 16, v59\n"
    "v_and_b32 v151, 0xffff0000, v59\n"
    "v_lshlrev_b32 v152, 16, v60\n"
    "v_and_b32 v153, 0xffff0000, v60\n"
    "v_lshlrev_b32 v154, 16, v61\n"
    "v_and_b32 v155, 0xffff0000, v61\n"
    "v_lshlrev_b32 v156, 16, v62\n"
    "v_and_b32 v157, 0xffff0000, v62\n"
    "v_lshlrev_b32 v158, 16, v63\n"
    "v_and_b32 v159, 0xffff0000, v63\n"
    "v_pk_add_f32 v[128:129], v[128:129], v[144:145]\n"
    "v_pk_add_f32 v[130:131], v[130:131], v[146:147]\n"
    "v_pk_add_f32 v[132:133], v[132:133], v[148:149]\n"
    "v_pk_add_f32 v[134:135], v[134:135], v[150:151]\n"
    "v_pk_add_f32 v[136:137], v[136:137], v[152:153]\n"
    "v_pk_add_f32 v[138:139], v[138:139], v[154:155]\n"
    "v_pk_add_f32 v[140:141], v[140:141], v[156:157]\n"
    "v_pk_add_f32 v[142:143], v[142:143], v[158:159]\n"
    "v_pk_fma_f32 v[128:129], v[128:129], v[0:1], v[32:33]\n"
    "v_pk_fma_f32 v[130:131], v[130:131], v[2:3], v[34:35]\n"
    "v_cvt_pk_bf16_f32 v160, v128, v129\n"
    "v_cvt_pk_bf16_f32 v161, v130, v131\n"
    "global_store_dwordx2 v216, v[160:161], s[52:53] offset:0\n"
    "v_pk_fma_f32 v[132:133], v[132:133], v[4:5], v[36:37]\n"
    "v_pk_fma_f32 v[134:135], v[134:135], v[6:7], v[38:39]\n"
    "v_cvt_pk_bf16_f32 v162, v132, v133\n"
    "v_cvt_pk_bf16_f32 v163, v134, v135\n"
    "global_store_dwordx2 v216, v[162:163], s[52:53] offset:32\n"
    "v_pk_fma_f32 v[136:137], v[136:137], v[8:9], v[40:41]\n"
    "v_pk_fma_f32 v[138:139], v[138:139], v[10:11], v[42:43]\n"
    "v_cvt_pk_bf16_f32 v164, v136, v137\n"
    "v_cvt_pk_bf16_f32 v165, v138, v139\n"
    "global_store_dwordx2 v217, v[164:165], s[52:53] offset:0\n"
    "v_pk_fma_f32 v[140:141], v[140:141], v[12:13], v[44:45]\n"
    "v_pk_fma_f32 v[142:143], v[142:143], v[14:15], v[46:47]\n"
    "v_cvt_pk_bf16_f32 v166, v140, v141\n"
    "v_cvt_pk_bf16_f32 v167, v142, v143\n"
    "global_store_dwordx2 v217, v[166:167], s[52:53] offset:32\n"
    "s_branch Lend%=\n"
    "Lnoffn%=:\n"
    "s_waitcnt vmcnt(0)\n"
    "s_barrier\n"
    "s_cmp_lg_u32 s25, 0\n"
    "s_cbranch_scc1 Lpb_y%=\n"
    "v_mov_b32 v184, s92\n"
    "v_mov_b32 v186, s80\n"
    "v_mov_b32 v187, s81\n"
    "s_mov_b64 exec, 1\n"
    "global_store_dwordx2 v184, v[186:187], s[82:83] offset:-1024 sc1\n"
    "s_mov_b64 exec, -1\n"
    "Lpb_y%=:\n"
    "ds_read_b128 a[16:19], v164 offset:0\n"
    "ds_read_b128 a[20:23], v175 offset:0\n"
    "ds_read_b128 a[24:27], v164 offset:2048\n"
    "ds_read_b128 a[28:31], v175 offset:2048\n"
    "ds_read_b128 a[48:51], v164 offset:4096\n"
    "ds_read_b128 a[52:55], v175 offset:4096\n"
    "ds_read_b128 a[56:59], v164 offset:6144\n"
    "ds_read_b128 a[60:63], v175 offset:6144\n"
    "ds_read_b128 a[144:147], v164 offset:8192\n"
    "ds_read_b128 a[148:151], v175 offset:8192\n"
    "ds_read_b128 a[152:155], v164 offset:10240\n"
    "ds_read_b128 a[156:159], v175 offset:10240\n"
    "ds_read_b128 a[176:179], v164 offset:12288\n"
    "ds_read_b128 a[180:183], v175 offset:12288\n"
    "ds_read_b128 a[184:187], v164 offset:14336\n"
    "ds_read_b128 a[188:191], v175 offset:14336\n"
    "s_waitcnt lgkmcnt(0)\n"
    "s_add_u32 m0, s19, 0\n"
    "s_add_u32 s84, s91, 512\n"
    "buffer_load_dwordx4 v167, s[68:71], s84 offen offset:0 lds\n"
    "buffer_load_dwordx4 v168, s[68:71], s84 offen offset:1024 lds\n"
    "buffer_load_dwordx4 v169, s[68:71], s84 offen offset:2048 lds\n"
    "buffer_load_dwordx4 v170, s[68:71], s84 offen offset:3072 lds\n"
    "s_add_u32 m0, s19, 4096\n"
    "s_add_u32 s84, s91, 16896\n"
    "buffer_load_dwordx4 v167, s[68:71], s84 offen offset:0 lds\n"
    "buffer_load_dwordx4 v168, s[68:71], s84 offen offset:1024 lds\n"
    "buffer_load_dwordx4 v169, s[68:71], s84 offen offset:2048 lds\n"
    "buffer_load_dwordx4 v170, s[68:71], s84 offen offset:3072 lds\n"
    "s_add_u32 m0, s19, 8192\n"
    "s_add_u32 s84, s91, 33280\n"
    "buffer_load_dwordx4 v167, s[68:71], s84 offen offset:0 lds\n"
    "buffer_load_dwordx4 v168, s[68:71], s84 offen offset:1024 lds\n"
    "buffer_load_dwordx4 v169, s[68:71], s84 offen offset:2048 lds\n"
    "buffer_load_dwordx4 v170, s[68:71], s84 offen offset:3072 lds\n"
    "s_add_u32 m0, s19, 12288\n"
    "s_add_u32 s84, s91, 49664\n"
    "buffer_load_dwordx4 v167, s[68:71], s84 offen offset:0 lds\n"
    "buffer_load_dwordx4 v168, s[68:71], s84 offen offset:1024 lds\n"
    "buffer_load_dwordx4 v169, s[68:71], s84 offen offset:2048 lds\n"
    "buffer_load_dwordx4 v170, s[68:71], s84 offen offset:3072 lds\n"
    "s_waitcnt vmcnt(0)\n"
    "ds_read_b128 a[64:67], v164 offset:0\n"
    "ds_read_b128 a[68:71], v175 offset:0\n"
    "ds_read_b128 a[72:75], v164 offset:2048\n"
    "ds_read_b128 a[76:79], v175 offset:2048\n"
    "ds_read_b128 a[96:99], v164 offset:4096\n"
    "ds_read_b128 a[100:103], v175 offset:4096\n"
    "ds_read_b128 a[104:107], v164 offset:6144\n"
    "ds_read_b128 a[108:111], v175 offset:6144\n"
    "ds_read_b128 a[192:195], v164 offset:8192\n"
    "ds_read_b128 a[196:199], v175 offset:8192\n"
    "ds_read_b128 a[200:203], v164 offset:10240\n"
    "ds_read_b128 a[204:207], v175 offset:10240\n"
    "ds_read_b128 a[224:227], v164 offset:12288\n"
    "ds_read_b128 a[228:231], v175 offset:12288\n"
    "ds_read_b128 a[232:235], v164 offset:14336\n"
    "ds_read_b128 a[236:239], v175 offset:14336\n"
    "s_waitcnt lgkmcnt(0)\n"
    "s_add_u32 m0, s19, 0\n"
    "s_add_u32 s84, s91, 768\n"
    "buffer_load_dwordx4 v167, s[68:71], s84 offen offset:0 lds\n"
    "buffer_load_dwordx4 v168, s[68:71], s84 offen offset:1024 lds\n"
    "buffer_load_dwordx4 v169, s[68:71], s84 offen offset:2048 lds\n"
    "buffer_load_dwordx4 v170, s[68:71], s84 offen offset:3072 lds\n"
    "s_add_u32 m0, s19, 4096\n"
    "s_add_u32 s84, s91, 17152\n"
    "buffer_load_dwordx4 v167, s[68:71], s84 offen offset:0 lds\n"
    "buffer_load_dwordx4 v168, s[68:71], s84 offen offset:1024 lds\n"
    "buffer_load_dwordx4 v169, s[68:71], s84 offen offset:2048 lds\n"
    "buffer_load_dwordx4 v170, s[68:71], s84 offen offset:3072 lds\n"
    "s_add_u32 m0, s19, 8192\n"
    "s_add_u32 s84, s91, 33536\n"
    "buffer_load_dwordx4 v167, s[68:71], s84 offen offset:0 lds\n"
    "buffer_load_dwordx4 v168, s[68:71], s84 offen offset:1024 lds\n"
    "buffer_load_dwordx4 v169, s[68:71], s84 offen offset:2048 lds\n"
    "buffer_load_dwordx4 v170, s[68:71], s84 offen offset:3072 lds\n"
    "s_add_u32 m0, s19, 12288\n"
    "s_add_u32 s84, s91, 49920\n"
    "buffer_load_dwordx4 v167, s[68:71], s84 offen offset:0 lds\n"
    "buffer_load_dwordx4 v168, s[68:71], s84 offen offset:1024 lds\n"
    "buffer_load_dwordx4 v169, s[68:71], s84 offen offset:2048 lds\n"
    "buffer_load_dwordx4 v170, s[68:71], s84 offen offset:3072 lds\n"
    "s_waitcnt vmcnt(0)\n"
    "ds_read_b128 a[80:83], v164 offset:0\n"
    "ds_read_b128 a[84:87], v175 offset:0\n"
    "ds_read_b128 a[88:91], v164 offset:2048\n"
    "ds_read_b128 a[92:95], v175 offset:2048\n"
    "ds_read_b128 a[112:115], v164 offset:4096\n"
    "ds_read_b128 a[116:119], v175 offset:4096\n"
    "ds_read_b128 a[120:123], v164 offset:6144\n"
    "ds_read_b128 a[124:127], v175 offset:6144\n"
    "ds_read_b128 a[208:211], v164 offset:8192\n"
    "ds_read_b128 a[212:215], v175 offset:8192\n"
    "ds_read_b128 a[216:219], v164 offset:10240\n"
    "ds_read_b128 a[220:223], v175 offset:10240\n"
    "ds_read_b128 a[240:243], v164 offset:12288\n"
    "ds_read_b128 a[244:247], v175 offset:12288\n"
    "ds_read_b128 a[248:251], v164 offset:14336\n"
    "ds_read_b128 a[252:255], v175 offset:14336\n"
    "s_waitcnt lgkmcnt(0)\n"
    "s_barrier\n"
    "v_add_u32 v175, 0xffff0000, v176\n"
    "v_lshrrev_b32 v178, 4, v181\n"
    "v_lshlrev_b32 v178, 4, v178\n"
    "v_add_u32 v178, 0x20000, v178\n"
    "s_add_u32 m0, s23, 32768\n"
    "s_add_u32 s84, s85, 2097152\n"
    "buffer_load_dwordx4 v177, s[56:59], s84 offen lds\n"
    "s_add_u32 m0, s23, 36864\n"
    "s_add_u32 s84, s85, 2359296\n"
    "buffer_load_dwordx4 v177, s[56:59], s84 offen lds\n"
    "s_add_u32 m0, s23, 40960\n"
    "s_add_u32 s84, s85, 2621440\n"
    "buffer_load_dwordx4 v177, s[56:59], s84 offen lds\n"
    "s_add_u32 m0, s23, 45056\n"
    "s_add_u32 s84, s85, 2883584\n"
    "buffer_load_dwordx4 v177, s[56:59], s84 offen lds\n"
    "s_add_u32 m0, s23, 49152\n"
    "s_add_u32 s84, s85, 3145728\n"
    "buffer_load_dwordx4 v177, s[56:59], s84 offen lds\n"
    "s_add_u32 m0, s23, 53248\n"
    "s_add_u32 s84, s85, 3407872\n"
    "buffer_load_dwordx4 v177, s[56:59], s84 offen lds\n"
    "s_add_u32 m0, s23, 57344\n"
    "s_add_u32 s84, s85, 3670016\n"
    "buffer_load_dwordx4 v177, s[56:59], s84 offen lds\n"
    "s_add_u32 m0, s23, 61440\n"
    "s_add_u32 s84, s85, 3932160\n"
    "buffer_load_dwordx4 v177, s[56:59], s84 offen lds\n"
    "s_add_u32 m0, s23, 98304\n"
    "s_add_u32 s84, s85, 128\n"
    "buffer_load_dwordx4 v177, s[56:59], s84 offen lds\n"
    "s_add_u32 m0, s23, 102400\n"
    "s_add_u32 s84, s85, 262272\n"
    "buffer_load_dwordx4 v177, s[56:59], s84 offen lds\n"
    "s_add_u32 m0, s23, 106496\n"
    "s_add_u32 s84, s85, 524416\n"
    "buffer_load_dwordx4 v177, s[56:59], s84 offen lds\n"
    "s_add_u32 m0, s23, 110592\n"
    "s_add_u32 s84, s85, 786560\n"
    "buffer_load_dwordx4 v177, s[56:59], s84 offen lds\n"
    "s_add_u32 m0, s23, 114688\n"
    "s_add_u32 s84, s85, 1048704\n"
    "buffer_load_dwordx4 v177, s[56:59], s84 offen lds\n"
    "s_add_u32 m0, s23, 118784\n"
    "s_add_u32 s84, s85, 1310848\n"
    "buffer_load_dwordx4 v177, s[56:59], s84 offen lds\n"
    "s_add_u32 m0, s23, 122880\n"
    "s_add_u32 s84, s85, 1572992\n"
    "buffer_load_dwordx4 v177, s[56:59], s84 offen lds\n"
    "s_add_u32 m0, s23, 126976\n"
    "s_add_u32 s84, s85, 1835136\n"
    "buffer_load_dwordx4 v177, s[56:59], s84 offen lds\n"
    "s_lshl_b32 s28, s27, 13\n"
    "s_lshl_b32 s29, s25, 8\n"
    "s_add_u32 s28, s28, s29\n"
    "v_and_b32 v184, 7, v181\n"
    "v_lshl_add_u32 v184, v184, 5, s28\n"
    "global_load_dwordx2 v[186:187], v184, s[82:83] offset:-1024 sc1\n"
    "s_waitcnt vmcnt(0)\n"
    "s_mov_b32 s28, 0x40000\n"
    "Lpk_y%=:\n"
    "v_cmp_ne_u64 vcc, s[80:81], v[186:187]\n"
    "s_cmp_eq_u64 vcc, 0\n"
    "s_cbranch_scc1 Lpkd_y%=\n"
    "global_load_dwordx2 v[186:187], v184, s[82:83] offset:-1024 sc1\n"
    "s_waitcnt vmcnt(0)\n"
    "s_sub_u32 s28, s28, 1\n"
    "s_cbranch_scc0 Lpk_y%=\n"
    "Lpkd_y%=:\n"
    "v_and_b32 v159, 15, v181\n"
    "v_lshlrev_b32 v159, 3, v159\n"
    "global_load_dwordx2 v[152:153], v159, s[34:35] offset:0 sc1\n"
    "global_load_dwordx2 v[154:155], v159, s[34:35] offset:128 sc1\n"
    "global_load_dwordx2 v[156:157], v159, s[34:35] offset:256 sc1\n"
    "global_load_dwordx2 v[158:159], v159, s[34:35] offset:384 sc1\n"
    "s_waitcnt vmcnt(0)\n"
    "v_mov_b32 v188, v152\n"
    "v_mul_f32 v165, 0xbfb8aa3b, v153\n"
    "v_mul_f32 v153, v153, v153\n"
    "v_rcp_f32 v167, v153\n"
    "v_mov_b32 v189, v154\n"
    "v_mul_f32 v166, 0xbfb8aa3b, v155\n"
    "v_mul_f32 v155, v155, v155\n"
    "v_rcp_f32 v168, v155\n"
    "v_mov_b32 v190, v156\n"
    "v_mul_f32 v171, 0xbfb8aa3b, v157\n"
    "v_mul_f32 v157, v157, v157\n"
    "v_rcp_f32 v169, v157\n"
    "v_mov_b32 v191, v158\n"
    "v_mul_f32 v172, 0xbfb8aa3b, v159\n"
    "v_mul_f32 v159, v159, v159\n"
    "v_rcp_f32 v170, v159\n"
    "s_barrier\n"
    "ds_read_b64_tr_b16 v[192:193], v175 offset:0\n"
    "ds_read_b64_tr_b16 v[194:195], v175 offset:256\n"
    "ds_read_b64_tr_b16 v[196:197], v175 offset:4096\n"
    "ds_read_b64_tr_b16 v[198:199], v175 offset:4352\n"
    "ds_read_b64_tr_b16 v[200:201], v175 offset:8192\n"
    "ds_read_b64_tr_b16 v[202:203], v175 offset:8448\n"
    "ds_read_b64_tr_b16 v[204:205], v175 offset:12288\n"
    "ds_read_b64_tr_b16 v[206:207], v175 offset:12544\n"
    "ds_read_b64_tr_b16 v[208:209], v175 offset:16384\n"
    "ds_read_b64_tr_b16 v[210:211], v175 offset:16640\n"
    "ds_read_b64_tr_b16 v[212:213], v175 offset:20480\n"
    "ds_read_b64_tr_b16 v[214:215], v175 offset:20736\n"
    "ds_read_b64_tr_b16 v[216:217], v175 offset:24576\n"
    "ds_read_b64_tr_b16 v[218:219], v175 offset:24832\n"
    "ds_read_b64_tr_b16 v[220:221], v175 offset:28672\n"
    "ds_read_b64_tr_b16 v[222:223], v175 offset:28928\n"
    "s_add_u32 m0, s23, 65536\n"
    "s_add_u32 s84, s85, 2097280\n"
    "buffer_load_dwordx4 v177, s[56:59], s84 offen lds\n"
    "s_add_u32 m0, s23, 69632\n"
    "s_add_u32 s84, s85, 2359424\n"
    "buffer_load_dwordx4 v177, s[56:59], s84 offen lds\n"
    "s_add_u32 m0, s23, 69632\n"
    "s_add_u32 s84, s85, 2359424\n"
    "s_waitcnt lgkmcnt(0)\n"
    "s_branch Lu4%=\n"
    "Lfallback%=:\n"
    "s_waitcnt vmcnt(0) lgkmcnt(0)\n"
    "s_mov_b64 s[8:9], s[56:57]\n"
    "s_mov_b64 s[10:11], s[58:59]\n"
    "s_mov_b64 s[12:13], s[68:69]\n"
    "s_mov_b64 s[14:15], s[70:71]\n"
    "v_lshrrev_b32 v148, 4, v181\n"
    "v_sub_u32 v148, 0, v148\n"
    "v_xor_b32 v148, v148, v181\n"
    "v_and_b32 v148, 3, v148\n"
    "v_lshlrev_b32 v148, 4, v148\n"
    "v_lshrrev_b32 v149, 2, v181\n"
    "v_lshl_add_u32 v148, v149, 8, v148\n"
    "v_add_u32 v167, 3072, v148\n"
    "v_add_u32 v168, 2112, v148\n"
    "v_add_u32 v169, 1152, v148\n"
    "v_add_u32 v170, 192, v148\n"
    "v_bfe_u32 v148, v181, 2, 3\n"
    "v_lshlrev_b32 v148, 8, v148\n"
    "v_lshrrev_b32 v149, 5, v181\n"
    "v_lshl_add_u32 v148, v149, 6, v148\n"
    "v_lshrrev_b32 v149, 1, v181\n"
    "v_lshrrev_b32 v150, 4, v181\n"
    "v_xor_b32 v149, v149, v150\n"
    "v_and_b32 v149, 1, v149\n"
    "v_lshl_add_u32 v148, v149, 5, v148\n"
    "v_and_b32 v149, 1, v181\n"
    "v_lshl_add_u32 v148, v149, 4, v148\n"
    "v_add_u32 v171, 1024, v148\n"
    "v_add_u32 v172, 128, v148\n"
    "s_mov_b32 s21, 1\n"
    "s_mov_b32 s16, s22\n"
    "s_mov_b32 m0, s22\n"
    "s_mov_b32 s20, 31\n"
    "v_and_b32 v164, 0x7fff, v164\n"
    "v_and_b32 v165, 0x7fff, v165\n"
    "v_and_b32 v166, 0x7fff, v166\n"
    "v_mov_b32 v248, 0xff800000\n"
    "v_mov_b32 v249, 0xff800000\n"
    "v_mov_b32 v250, 0xff800000\n"
    "v_mov_b32 v251, 0xff800000\n"
    "Lfbl%=:\n"
    "s_barrier\n"
    "buffer_load_dwordx4 v167, s[8:11], s16 offen offset:0 lds\n"
    "buffer_load_dwordx4 v168, s[8:11], s16 offen offset:1024 lds\n"
    "buffer_load_dwordx4 v169, s[8:11], s16 offen offset:2048 lds\n"
    "buffer_load_dwordx4 v170, s[8:11], s16 offen offset:3072 lds\n"
    "s_add_u32 s16, s16, 16384\n"
    "s_waitcnt vmcnt(0)\n"
    "s_barrier\n"
    "ds_read_b128 a[128:131], v164 offset:0\n"
    "ds_read_b128 a[132:135], v164 offset:1024\n"
    "ds_read_b128 a[136:139], v164 offset:2048\n"
    "ds_read_b128 a[140:143], v164 offset:3072\n"
    "ds_read_b128 a[144:147], v164 offset:4096\n"
    "ds_read_b128 a[148:151], v164 offset:5120\n"
    "ds_read_b128 a[152:155], v164 offset:6144\n"
    "ds_read_b128 a[156:159], v164 offset:7168\n"
    "ds_read_b128 a[160:163], v164 offset:8192\n"
    "ds_read_b128 a[164:167], v164 offset:9216\n"
    "ds_read_b128 a[168:171], v164 offset:10240\n"
    "ds_read_b128 a[172:175], v164 offset:11264\n"
    "ds_read_b128 a[176:179], v164 offset:12288\n"
    "ds_read_b128 a[180:183], v164 offset:13312\n"
    "ds_read_b128 a[184:187], v164 offset:14336\n"
    "ds_read_b128 a[188:191], v164 offset:15360\n"
    "s_waitcnt lgkmcnt(0)\n"
    "v_mfma_f32_16x16x32_bf16 v[0:3], a[128:131], v[64:67], 0\n"
    "v_mfma_f32_16x16x32_bf16 v[0:3], a[132:135], v[68:71], v[0:3]\n"
    "v_mfma_f32_16x16x32_bf16 v[0:3], a[136:139], v[72:75], v[0:3]\n"
    "v_mfma_f32_16x16x32_bf16 v[0:3], a[140:143], v[76:79], v[0:3]\n"
    "v_mfma_f32_16x16x32_bf16 v[16:19], a[128:131], v[80:83], 0\n"
    "v_mfma_f32_16x16x32_bf16 v[16:19], a[132:135], v[84:87], v[16:19]\n"
    "v_mfma_f32_16x16x32_bf16 v[16:19], a[136:139], v[88:91], v[16:19]\n"
    "v_mfma_f32_16x16x32_bf16 v[16:19], a[140:143], v[92:95], v[16:19]\n"
    "v_mfma_f32_16x16x32_bf16 v[32:35], a[128:131], v[96:99], 0\n"
    "v_mfma_f32_16x16x32_bf16 v[32:35], a[132:135], v[100:103], v[32:35]\n"
    "v_mfma_f32_16x16x32_bf16 v[32:35], a[136:139], v[104:107], v[32:35]\n"
    "v_mfma_f32_16x16x32_bf16 v[32:35], a[140:143], v[108:111], v[32:35]\n"
    "v_mfma_f32_16x16x32_bf16 v[48:51], a[128:131], v[112:115], 0\n"
    "v_mfma_f32_16x16x32_bf16 v[48:51], a[132:135], v[116:119], v[48:51]\n"
    "v_mfma_f32_16x16x32_bf16 v[48:51], a[136:139], v[120:123], v[48:51]\n"
    "v_mfma_f32_16x16x32_bf16 v[48:51], a[140:143], v[124:127], v[48:51]\n"
    "v_mfma_f32_16x16x32_bf16 v[4:7], a[144:147], v[64:67], 0\n"
    "v_mfma_f32_16x16x32_bf16 v[4:7], a[148:151], v[68:71], v[4:7]\n"
    "v_mfma_f32_16x16x32_bf16 v[4:7], a[152:155], v[72:75], v[4:7]\n"
    "v_mfma_f32_16x16x32_bf16 v[4:7], a[156:159], v[76:79], v[4:7]\n"
    "v_mfma_f32_16x16x32_bf16 v[20:23], a[144:147], v[80:83], 0\n"
    "v_mfma_f32_16x16x32_bf16 v[20:23], a[148:151], v[84:87], v[20:23]\n"
    "v_mfma_f32_16x16x32_bf16 v[20:23], a[152:155], v[88:91], v[20:23]\n"
    "v_mfma_f32_16x16x32_bf16 v[20:23], a[156:159], v[92:95], v[20:23]\n"
    "v_mfma_f32_16x16x32_bf16 v[36:39], a[144:147], v[96:99], 0\n"
    "v_mfma_f32_16x16x32_bf16 v[36:39], a[148:151], v[100:103], v[36:39]\n"
    "v_mfma_f32_16x16x32_bf16 v[36:39], a[152:155], v[104:107], v[36:39]\n"
    "v_mfma_f32_16x16x32_bf16 v[36:39], a[156:159], v[108:111], v[36:39]\n"
    "v_mfma_f32_16x16x32_bf16 v[52:55], a[144:147], v[112:115], 0\n"
    "v_mfma_f32_16x16x32_bf16 v[52:55], a[148:151], v[116:119], v[52:55]\n"
    "v_mfma_f32_16x16x32_bf16 v[52:55], a[152:155], v[120:123], v[52:55]\n"
    "v_mfma_f32_16x16x32_bf16 v[52:55], a[156:159], v[124:127], v[52:55]\n"
    "v_mfma_f32_16x16x32_bf16 v[8:11], a[160:163], v[64:67], 0\n"
    "v_mfma_f32_16x16x32_bf16 v[8:11], a[164:167], v[68:71], v[8:11]\n"
    "v_mfma_f32_16x16x32_bf16 v[8:11], a[168:171], v[72:75], v[8:11]\n"
    "v_mfma_f32_16x16x32_bf16 v[8:11], a[172:175], v[76:79], v[8:11]\n"
    "v_mfma_f32_16x16x32_bf16 v[24:27], a[160:163], v[80:83], 0\n"
    "v_mfma_f32_16x16x32_bf16 v[24:27], a[164:167], v[84:87], v[24:27]\n"
    "v_mfma_f32_16x16x32_bf16 v[24:27], a[168:171], v[88:91], v[24:27]\n"
    "v_mfma_f32_16x16x32_bf16 v[24:27], a[172:175], v[92:95], v[24:27]\n"
    "v_mfma_f32_16x16x32_bf16 v[40:43], a[160:163], v[96:99], 0\n"
    "v_mfma_f32_16x16x32_bf16 v[40:43], a[164:167], v[100:103], v[40:43]\n"
    "v_mfma_f32_16x16x32_bf16 v[40:43], a[168:171], v[104:107], v[40:43]\n"
    "v_mfma_f32_16x16x32_bf16 v[40:43], a[172:175], v[108:111], v[40:43]\n"
    "v_mfma_f32_16x16x32_bf16 v[56:59], a[160:163], v[112:115], 0\n"
    "v_mfma_f32_16x16x32_bf16 v[56:59], a[164:167], v[116:119], v[56:59]\n"
    "v_mfma_f32_16x16x32_bf16 v[56:59], a[168:171], v[120:123], v[56:59]\n"
    "v_mfma_f32_16x16x32_bf16 v[56:59], a[172:175], v[124:127], v[56:59]\n"
    "v_mfma_f32_16x16x32_bf16 v[12:15], a[176:179], v[64:67], 0\n"
    "v_mfma_f32_16x16x32_bf16 v[12:15], a[180:183], v[68:71], v[12:15]\n"
    "v_mfma_f32_16x16x32_bf16 v[12:15], a[184:187], v[72:75], v[12:15]\n"
    "v_mfma_f32_16x16x32_bf16 v[12:15], a[188:191], v[76:79], v[12:15]\n"
    "v_mfma_f32_16x16x32_bf16 v[28:31], a[176:179], v[80:83], 0\n"
    "v_mfma_f32_16x16x32_bf16 v[28:31], a[180:183], v[84:87], v[28:31]\n"
    "v_mfma_f32_16x16x32_bf16 v[28:31], a[184:187], v[88:91], v[28:31]\n"
    "v_mfma_f32_16x16x32_bf16 v[28:31], a[188:191], v[92:95], v[28:31]\n"
    "v_mfma_f32_16x16x32_bf16 v[44:47], a[176:179], v[96:99], 0\n"
    "v_mfma_f32_16x16x32_bf16 v[44:47], a[180:183], v[100:103], v[44:47]\n"
    "v_mfma_f32_16x16x32_bf16 v[44:47], a[184:187], v[104:107], v[44:47]\n"
    "v_mfma_f32_16x16x32_bf16 v[44:47], a[188:191], v[108:111], v[44:47]\n"
    "v_mfma_f32_16x16x32_bf16 v[60:63], a[176:179], v[112:115], 0\n"
    "v_mfma_f32_16x16x32_bf16 v[60:63], a[180:183], v[116:119], v[60:63]\n"
    "v_mfma_f32_16x16x32_bf16 v[60:63], a[184:187], v[120:123], v[60:63]\n"
    "v_mfma_f32_16x16x32_bf16 v[60:63], a[188:191], v[124:127], v[60:63]\n"
    "s_nop 7\n"
    "v_max3_f32 v248, v248, v0, v1\n"
    "v_max3_f32 v248, v248, v2, v3\n"
    "v_max3_f32 v248, v248, v4, v5\n"
    "v_max3_f32 v248, v248, v6, v7\n"
    "v_max3_f32 v248, v248, v8, v9\n"
    "v_max3_f32 v248, v248, v10, v11\n"
    "v_max3_f32 v248, v248, v12, v13\n"
    "v_max3_f32 v248, v248, v14, v15\n"
    "v_max3_f32 v249, v249, v16, v17\n"
    "v_max3_f32 v249, v249, v18, v19\n"
    "v_max3_f32 v249, v249, v20, v21\n"
    "v_max3_f32 v249, v249, v22, v23\n"
    "v_max3_f32 v249, v249, v24, v25\n"
    "v_max3_f32 v249, v249, v26, v27\n"
    "v_max3_f32 v249, v249, v28, v29\n"
    "v_max3_f32 v249, v249, v30, v31\n"
    "v_max3_f32 v250, v250, v32, v33\n"
    "v_max3_f32 v250, v250, v34, v35\n"
    "v_max3_f32 v250, v250, v36, v37\n"
    "v_max3_f32 v250, v250, v38, v39\n"
    "v_max3_f32 v250, v250, v40, v41\n"
    "v_max3_f32 v250, v250, v42, v43\n"
    "v_max3_f32 v250, v250, v44, v45\n"
    "v_max3_f32 v250, v250, v46, v47\n"
    "v_max3_f32 v251, v251, v48, v49\n"
    "v_max3_f32 v251, v251, v50, v51\n"
    "v_max3_f32 v251, v251, v52, v53\n"
    "v_max3_f32 v251, v251, v54, v55\n"
    "v_max3_f32 v251, v251, v56, v57\n"
    "v_max3_f32 v251, v251, v58, v59\n"
    "v_max3_f32 v251, v251, v60, v61\n"
    "v_max3_f32 v251, v251, v62, v63\n"
    "s_sub_u32 s20, s20, 1\n"
    "s_cbranch_scc0 Lfbl%=\n"
    "v_mov_b32 v0, v248\n"
    "v_mov_b32 v1, v248\n"
    "v_mov_b32 v2, v248\n"
    "v_mov_b32 v3, v248\n"
    "v_mov_b32 v16, v249\n"
    "v_mov_b32 v17, v249\n"
    "v_mov_b32 v18, v249\n"
    "v_mov_b32 v19, v249\n"
    "v_mov_b32 v32, v250\n"
    "v_mov_b32 v33, v250\n"
    "v_mov_b32 v34, v250\n"
    "v_mov_b32 v35, v250\n"
    "v_mov_b32 v48, v251\n"
    "v_mov_b32 v49, v251\n"
    "v_mov_b32 v50, v251\n"
    "v_mov_b32 v51, v251\n"
    "v_max3_f32 v184, v0, v1, v2\n"
    "v_max_f32 v184, v184, v3\n"
    "v_mov_b32 v185, v184\n"
    "v_max3_f32 v186, v16, v17, v18\n"
    "v_max_f32 v186, v186, v19\n"
    "v_mov_b32 v187, v186\n"
    "v_max3_f32 v188, v32, v33, v34\n"
    "v_max_f32 v188, v188, v35\n"
    "v_mov_b32 v189, v188\n"
    "v_max3_f32 v190, v48, v49, v50\n"
    "v_max_f32 v190, v190, v51\n"
    "v_mov_b32 v191, v190\n"
    "v_permlane16_swap_b32 v184, v185\n"
    "v_max_f32 v184, v184, v185\n"
    "v_mov_b32 v185, v184\n"
    "v_permlane16_swap_b32 v186, v187\n"
    "v_max_f32 v186, v186, v187\n"
    "v_mov_b32 v187, v186\n"
    "v_permlane16_swap_b32 v188, v189\n"
    "v_max_f32 v188, v188, v189\n"
    "v_mov_b32 v189, v188\n"
    "v_permlane16_swap_b32 v190, v191\n"
    "v_max_f32 v190, v190, v191\n"
    "v_mov_b32 v191, v190\n"
    "v_permlane32_swap_b32 v184, v185\n"
    "v_min_f32 v128, -v184, -v185\n"
    "v_min_f32 v129, -v184, -v185\n"
    "v_min_f32 v130, -v184, -v185\n"
    "v_min_f32 v131, -v184, -v185\n"
    "v_permlane32_swap_b32 v186, v187\n"
    "v_min_f32 v132, -v186, -v187\n"
    "v_min_f32 v133, -v186, -v187\n"
    "v_min_f32 v134, -v186, -v187\n"
    "v_min_f32 v135, -v186, -v187\n"
    "v_permlane32_swap_b32 v188, v189\n"
    "v_min_f32 v136, -v188, -v189\n"
    "v_min_f32 v137, -v188, -v189\n"
    "v_min_f32 v138, -v188, -v189\n"
    "v_min_f32 v139, -v188, -v189\n"
    "v_permlane32_swap_b32 v190, v191\n"
    "v_min_f32 v140, -v190, -v191\n"
    "v_min_f32 v141, -v190, -v191\n"
    "v_min_f32 v142, -v190, -v191\n"
    "v_min_f32 v143, -v190, -v191\n"
    "s_barrier\n"
    "s_add_u32 m0, s22, 0\n"
    "s_add_u32 s16, s22, 0\n"
    "buffer_load_dwordx4 v167, s[8:11], s16 offen offset:0 lds\n"
    "buffer_load_dwordx4 v168, s[8:11], s16 offen offset:1024 lds\n"
    "buffer_load_dwordx4 v169, s[8:11], s16 offen offset:2048 lds\n"
    "buffer_load_dwordx4 v170, s[8:11], s16 offen offset:3072 lds\n"
    "s_add_u32 m0, s22, 16384\n"
    "s_nop 0\n"
    "buffer_load_dwordx4 v171, s[12:15], s16 offen offset:0 sc1 lds\n"
    "buffer_load_dwordx4 v172, s[12:15], s16 offen offset:1024 sc1 lds\n"
    "buffer_load_dwordx4 v171, s[12:15], s16 offen offset:2048 sc1 lds\n"
    "buffer_load_dwordx4 v172, s[12:15], s16 offen offset:3072 sc1 lds\n"
    "s_add_u32 m0, s22, 32768\n"
    "s_add_u32 s16, s22, 16384\n"
    "buffer_load_dwordx4 v167, s[8:11], s16 offen offset:0 lds\n"
    "buffer_load_dwordx4 v168, s[8:11], s16 offen offset:1024 lds\n"
    "buffer_load_dwordx4 v169, s[8:11], s16 offen offset:2048 lds\n"
    "buffer_load_dwordx4 v170, s[8:11], s16 offen offset:3072 lds\n"
    "s_add_u32 m0, s22, 49152\n"
    "s_nop 0\n"
    "buffer_load_dwordx4 v171, s[12:15], s16 offen offset:0 sc1 lds\n"
    "buffer_load_dwordx4 v172, s[12:15], s16 offen offset:1024 sc1 lds\n"
    "buffer_load_dwordx4 v171, s[12:15], s16 offen offset:2048 sc1 lds\n"
    "buffer_load_dwordx4 v172, s[12:15], s16 offen offset:3072 sc1 lds\n"
    "s_add_u32 m0, s22, 98304\n"
    "s_add_u32 s16, s22, 32768\n"
    "buffer_load_dwordx4 v167, s[8:11], s16 offen offset:0 lds\n"
    "buffer_load_dwordx4 v168, s[8:11], s16 offen offset:1024 lds\n"
    "buffer_load_dwordx4 v169, s[8:11], s16 offen offset:2048 lds\n"
    "buffer_load_dwordx4 v170, s[8:11], s16 offen offset:3072 lds\n"
    "s_add_u32 m0, s22, 114688\n"
    "s_nop 0\n"
    "buffer_load_dwordx4 v171, s[12:15], s16 offen offset:0 sc1 lds\n"
    "buffer_load_dwordx4 v172, s[12:15], s16 offen offset:1024 sc1 lds\n"
    "buffer_load_dwordx4 v171, s[12:15], s16 offen offset:2048 sc1 lds\n"
    "buffer_load_dwordx4 v172, s[12:15], s16 offen offset:3072 sc1 lds\n"
    "v_accvgpr_write_b32 a0, 0\n"
    "v_accvgpr_write_b32 a1, 0\n"
    "v_accvgpr_write_b32 a2, 0\n"
    "v_accvgpr_write_b32 a3, 0\n"
    "v_accvgpr_write_b32 a4, 0\n"
    "v_accvgpr_write_b32 a5, 0\n"
    "v_accvgpr_write_b32 a6, 0\n"
    "v_accvgpr_write_b32 a7, 0\n"
    "v_accvgpr_write_b32 a8, 0\n"
    "v_accvgpr_write_b32 a9, 0\n"
    "v_accvgpr_write_b32 a10, 0\n"
    "v_accvgpr_write_b32 a11, 0\n"
    "v_accvgpr_write_b32 a12, 0\n"
    "v_accvgpr_write_b32 a13, 0\n"
    "v_accvgpr_write_b32 a14, 0\n"
    "v_accvgpr_write_b32 a15, 0\n"
    "v_accvgpr_write_b32 a16, 0\n"
    "v_accvgpr_write_b32 a17, 0\n"
    "v_accvgpr_write_b32 a18, 0\n"
    "v_accvgpr_write_b32 a19, 0\n"
    "v_accvgpr_write_b32 a20, 0\n"
    "v_accvgpr_write_b32 a21, 0\n"
    "v_accvgpr_write_b32 a22, 0\n"
    "v_accvgpr_write_b32 a23, 0\n"
    "v_accvgpr_write_b32 a24, 0\n"
    "v_accvgpr_write_b32 a25, 0\n"
    "v_accvgpr_write_b32 a26, 0\n"
    "v_accvgpr_write_b32 a27, 0\n"
    "v_accvgpr_write_b32 a28, 0\n"
    "v_accvgpr_write_b32 a29, 0\n"
    "v_accvgpr_write_b32 a30, 0\n"
    "v_accvgpr_write_b32 a31, 0\n"
    "v_accvgpr_write_b32 a32, 0\n"
    "v_accvgpr_write_b32 a33, 0\n"
    "v_accvgpr_write_b32 a34, 0\n"
    "v_accvgpr_write_b32 a35, 0\n"
    "v_accvgpr_write_b32 a36, 0\n"
    "v_accvgpr_write_b32 a37, 0\n"
    "v_accvgpr_write_b32 a38, 0\n"
    "v_accvgpr_write_b32 a39, 0\n"
    "v_accvgpr_write_b32 a40, 0\n"
    "v_accvgpr_write_b32 a41, 0\n"
    "v_accvgpr_write_b32 a42, 0\n"
    "v_accvgpr_write_b32 a43, 0\n"
    "v_accvgpr_write_b32 a44, 0\n"
    "v_accvgpr_write_b32 a45, 0\n"
    "v_accvgpr_write_b32 a46, 0\n"
    "v_accvgpr_write_b32 a47, 0\n"
    "v_accvgpr_write_b32 a48, 0\n"
    "v_accvgpr_write_b32 a49, 0\n"
    "v_accvgpr_write_b32 a50, 0\n"
    "v_accvgpr_write_b32 a51, 0\n"
    "v_accvgpr_write_b32 a52, 0\n"
    "v_accvgpr_write_b32 a53, 0\n"
    "v_accvgpr_write_b32 a54, 0\n"
    "v_accvgpr_write_b32 a55, 0\n"
    "v_accvgpr_write_b32 a56, 0\n"
    "v_accvgpr_write_b32 a57, 0\n"
    "v_accvgpr_write_b32 a58, 0\n"
    "v_accvgpr_write_b32 a59, 0\n"
    "v_accvgpr_write_b32 a60, 0\n"
    "v_accvgpr_write_b32 a61, 0\n"
    "v_accvgpr_write_b32 a62, 0\n"
    "v_accvgpr_write_b32 a63, 0\n"
    "v_accvgpr_write_b32 a64, 0\n"
    "v_accvgpr_write_b32 a65, 0\n"
    "v_accvgpr_write_b32 a66, 0\n"
    "v_accvgpr_write_b32 a67, 0\n"
    "v_accvgpr_write_b32 a68, 0\n"
    "v_accvgpr_write_b32 a69, 0\n"
    "v_accvgpr_write_b32 a70, 0\n"
    "v_accvgpr_write_b32 a71, 0\n"
    "v_accvgpr_write_b32 a72, 0\n"
    "v_accvgpr_write_b32 a73, 0\n"
    "v_accvgpr_write_b32 a74, 0\n"
    "v_accvgpr_write_b32 a75, 0\n"
    "v_accvgpr_write_b32 a76, 0\n"
    "v_accvgpr_write_b32 a77, 0\n"
    "v_accvgpr_write_b32 a78, 0\n"
    "v_accvgpr_write_b32 a79, 0\n"
    "v_accvgpr_write_b32 a80, 0\n"
    "v_accvgpr_write_b32 a81, 0\n"
    "v_accvgpr_write_b32 a82, 0\n"
    "v_accvgpr_write_b32 a83, 0\n"
    "v_accvgpr_write_b32 a84, 0\n"
    "v_accvgpr_write_b32 a85, 0\n"
    "v_accvgpr_write_b32 a86, 0\n"
    "v_accvgpr_write_b32 a87, 0\n"
    "v_accvgpr_write_b32 a88, 0\n"
    "v_accvgpr_write_b32 a89, 0\n"
    "v_accvgpr_write_b32 a90, 0\n"
    "v_accvgpr_write_b32 a91, 0\n"
    "v_accvgpr_write_b32 a92, 0\n"
    "v_accvgpr_write_b32 a93, 0\n"
    "v_accvgpr_write_b32 a94, 0\n"
    "v_accvgpr_write_b32 a95, 0\n"
    "v_accvgpr_write_b32 a96, 0\n"
    "v_accvgpr_write_b32 a97, 0\n"
    "v_accvgpr_write_b32 a98, 0\n"
    "v_accvgpr_write_b32 a99, 0\n"
    "v_accvgpr_write_b32 a100, 0\n"
    "v_accvgpr_write_b32 a101, 0\n"
    "v_accvgpr_write_b32 a102, 0\n"
    "v_accvgpr_write_b32 a103, 0\n"
    "v_accvgpr_write_b32 a104, 0\n"
    "v_accvgpr_write_b32 a105, 0\n"
    "v_accvgpr_write_b32 a106, 0\n"
    "v_accvgpr_write_b32 a107, 0\n"
    "v_accvgpr_write_b32 a108, 0\n"
    "v_accvgpr_write_b32 a109, 0\n"
    "v_accvgpr_write_b32 a110, 0\n"
    "v_accvgpr_write_b32 a111, 0\n"
    "v_accvgpr_write_b32 a112, 0\n"
    "v_accvgpr_write_b32 a113, 0\n"
    "v_accvgpr_write_b32 a114, 0\n"
    "v_accvgpr_write_b32 a115, 0\n"
    "v_accvgpr_write_b32 a116, 0\n"
    "v_accvgpr_write_b32 a117, 0\n"
    "v_accvgpr_write_b32 a118, 0\n"
    "v_accvgpr_write_b32 a119, 0\n"
    "v_accvgpr_write_b32 a120, 0\n"
    "v_accvgpr_write_b32 a121, 0\n"
    "v_accvgpr_write_b32 a122, 0\n"
    "v_accvgpr_write_b32 a123, 0\n"
    "v_accvgpr_write_b32 a124, 0\n"
    "v_accvgpr_write_b32 a125, 0\n"
    "v_accvgpr_write_b32 a126, 0\n"
    "v_accvgpr_write_b32 a127, 0\n"
    "v_mov_b32 v144, 0\n"
    "v_mov_b32 v145, 0\n"
    "v_mov_b32 v146, 0\n"
    "v_mov_b32 v147, 0\n"
    "v_mov_b32 v148, 0\n"
    "v_mov_b32 v149, 0\n"
    "v_mov_b32 v150, 0\n"
    "v_mov_b32 v151, 0\n"
    "v_mov_b32 v152, 0\n"
    "v_mov_b32 v153, 0\n"
    "v_mov_b32 v154, 0\n"
    "v_mov_b32 v155, 0\n"
    "v_mov_b32 v156, 0\n"
    "v_mov_b32 v157, 0\n"
    "v_mov_b32 v158, 0\n"
    "v_mov_b32 v159, 0\n"
    "s_mov_b32 s18, 32768\n"
    "s_mov_b32 s19, 32768\n"
    "s_mov_b32 s20, 30\n"
    "s_add_u32 s17, s22, 98304\n"
    "s_add_u32 s16, s22, 32768\n"
    "s_waitcnt vmcnt(0)\n"
    "s_barrier\n"
    "ds_read_b128 a[128:131], v164 offset:0\n"
    "ds_read_b128 a[132:135], v164 offset:1024\n"
    "ds_read_b128 a[136:139], v164 offset:2048\n"
    "ds_read_b128 a[140:143], v164 offset:3072\n"
    "ds_read_b128 a[144:147], v164 offset:4096\n"
    "ds_read_b128 a[148:151], v164 offset:5120\n"
    "ds_read_b128 a[152:155], v164 offset:6144\n"
    "ds_read_b128 a[156:159], v164 offset:7168\n"
    "ds_read_b128 a[160:163], v164 offset:8192\n"
    "ds_read_b128 a[164:167], v164 offset:9216\n"
    "ds_read_b128 a[168:171], v164 offset:10240\n"
    "ds_read_b128 a[172:175], v164 offset:11264\n"
    "ds_read_b128 a[176:179], v164 offset:12288\n"
    "ds_read_b128 a[180:183], v164 offset:13312\n"
    "ds_read_b128 a[184:187], v164 offset:14336\n"
    "ds_read_b128 a[188:191], v164 offset:15360\n"
    "s_waitcnt lgkmcnt(0)\n"
    "v_mfma_f32_16x16x32_bf16 v[0:3], a[128:131], v[64:67], v[128:131]\n"
    "v_mfma_f32_16x16x32_bf16 v[0:3], a[132:135], v[68:71], v[0:3]\n"
    "v_mfma_f32_16x16x32_bf16 v[0:3], a[136:139], v[72:75], v[0:3]\n"
    "v_mfma_f32_16x16x32_bf16 v[0:3], a[140:143], v[76:79], v[0:3]\n"
    "v_xor_b32 v164, s18, v164\n"
    "v_mfma_f32_16x16x32_bf16 v[16:19], a[140:143], v[92:95], v[132:135]\n"
    "v_mfma_f32_16x16x32_bf16 v[16:19], a[136:139], v[88:91], v[16:19]\n"
    "v_mfma_f32_16x16x32_bf16 v[16:19], a[132:135], v[84:87], v[16:19]\n"
    "v_mfma_f32_16x16x32_bf16 v[16:19], a[128:131], v[80:83], v[16:19]\n"
    "v_mfma_f32_16x16x32_bf16 v[20:23], a[144:147], v[80:83], v[132:135]\n"
    "v_mfma_f32_16x16x32_bf16 v[20:23], a[148:151], v[84:87], v[20:23]\n"
    "s_mov_b32 m0, s17\n"
    "v_mfma_f32_16x16x32_bf16 v[20:23], a[152:155], v[88:91], v[20:23]\n"
    "buffer_load_dwordx4 v167, s[8:11], s16 offen offset:0 lds\n"
    "v_mfma_f32_16x16x32_bf16 v[20:23], a[156:159], v[92:95], v[20:23]\n"
    "v_mfma_f32_16x16x32_bf16 v[4:7], a[156:159], v[76:79], v[128:131]\n"
    "v_exp_f32 v0, v0\n"
    "v_mfma_f32_16x16x32_bf16 v[4:7], a[152:155], v[72:75], v[4:7]\n"
    "buffer_load_dwordx4 v168, s[8:11], s16 offen offset:1024 lds\n"
    "v_mfma_f32_16x16x32_bf16 v[4:7], a[148:151], v[68:71], v[4:7]\n"
    "v_exp_f32 v1, v1\n"
    "v_mfma_f32_16x16x32_bf16 v[4:7], a[144:147], v[64:67], v[4:7]\n"
    "v_cvt_pk_bf16_f32 v0, v0, v1\n"
    "v_mfma_f32_16x16x32_bf16 v[8:11], a[160:163], v[64:67], v[128:131]\n"
    "s_waitcnt lgkmcnt(0)\n"
    "v_exp_f32 v2, v2\n"
    "v_mfma_f32_16x16x32_bf16 v[8:11], a[164:167], v[68:71], v[8:11]\n"
    "buffer_load_dwordx4 v169, s[8:11], s16 offen offset:2048 lds\n"
    "v_mfma_f32_16x16x32_bf16 v[8:11], a[168:171], v[72:75], v[8:11]\n"
    "v_exp_f32 v3, v3\n"
    "v_mfma_f32_16x16x32_bf16 v[8:11], a[172:175], v[76:79], v[8:11]\n"
    "v_cvt_pk_bf16_f32 v1, v2, v3\n"
    "v_mfma_f32_16x16x32_bf16 v[24:27], a[172:175], v[92:95], v[132:135]\n"
    "v_exp_f32 v16, v16\n"
    "v_mfma_f32_16x16x32_bf16 v[24:27], a[168:171], v[88:91], v[24:27]\n"
    "buffer_load_dwordx4 v170, s[8:11], s16 offen offset:3072 lds\n"
    "v_mfma_f32_16x16x32_bf16 v[24:27], a[164:167], v[84:87], v[24:27]\n"
    "v_exp_f32 v17, v17\n"
    "v_mfma_f32_16x16x32_bf16 v[24:27], a[160:163], v[80:83], v[24:27]\n"
    "v_cvt_pk_bf16_f32 v16, v16, v17\n"
    "v_mfma_f32_16x16x32_bf16 v[28:31], a[176:179], v[80:83], v[132:135]\n"
    "s_add_u32 m0, s17, 16384\n"
    "v_exp_f32 v18, v18\n"
    "v_mfma_f32_16x16x32_bf16 v[28:31], a[180:183], v[84:87], v[28:31]\n"
    "buffer_load_dwordx4 v171, s[12:15], s16 offen offset:0 sc1 lds\n"
    "v_mfma_f32_16x16x32_bf16 v[28:31], a[184:187], v[88:91], v[28:31]\n"
    "v_exp_f32 v19, v19\n"
    "v_mfma_f32_16x16x32_bf16 v[28:31], a[188:191], v[92:95], v[28:31]\n"
    "v_cvt_pk_bf16_f32 v17, v18, v19\n"
    "v_mfma_f32_16x16x32_bf16 v[12:15], a[188:191], v[76:79], v[128:131]\n"
    "v_exp_f32 v20, v20\n"
    "v_mfma_f32_16x16x32_bf16 v[12:15], a[184:187], v[72:75], v[12:15]\n"
    "buffer_load_dwordx4 v172, s[12:15], s16 offen offset:1024 sc1 lds\n"
    "v_mfma_f32_16x16x32_bf16 v[12:15], a[180:183], v[68:71], v[12:15]\n"
    "v_exp_f32 v21, v21\n"
    "v_mfma_f32_16x16x32_bf16 v[12:15], a[176:179], v[64:67], v[12:15]\n"
    "v_cvt_pk_bf16_f32 v18, v20, v21\n"
    "v_exp_f32 v22, v22\n"
    "buffer_load_dwordx4 v171, s[12:15], s16 offen offset:2048 sc1 lds\n"
    "v_exp_f32 v23, v23\n"
    "buffer_load_dwordx4 v172, s[12:15], s16 offen offset:3072 sc1 lds\n"
    "v_cvt_pk_bf16_f32 v19, v22, v23\n"
    "s_xor_b32 s17, s17, s18\n"
    "v_exp_f32 v4, v4\n"
    "s_add_u32 s16, s16, 16384\n"
    "s_xor_b32 s18, s18, 98304\n"
    "v_exp_f32 v5, v5\n"
    "ds_read_b64_tr_b16 a[192:193], v165 offset:16384\n"
    "v_cvt_pk_bf16_f32 v2, v4, v5\n"
    "v_exp_f32 v6, v6\n"
    "ds_read_b64_tr_b16 a[194:195], v165 offset:20480\n"
    "v_exp_f32 v7, v7\n"
    "ds_read_b64_tr_b16 a[196:197], v165 offset:24576\n"
    "v_cvt_pk_bf16_f32 v3, v6, v7\n"
    "v_exp_f32 v8, v8\n"
    "ds_read_b64_tr_b16 a[198:199], v165 offset:28672\n"
    "v_exp_f32 v9, v9\n"
    "ds_read_b64_tr_b16 a[200:201], v166 offset:16384\n"
    "v_cvt_pk_bf16_f32 v4, v8, v9\n"
    "v_exp_f32 v10, v10\n"
    "ds_read_b64_tr_b16 a[202:203], v166 offset:20480\n"
    "v_exp_f32 v11, v11\n"
    "ds_read_b64_tr_b16 a[204:205], v166 offset:24576\n"
    "v_cvt_pk_bf16_f32 v5, v10, v11\n"
    "v_exp_f32 v24, v24\n"
    "ds_read_b64_tr_b16 a[206:207], v166 offset:28672\n"
    "v_exp_f32 v25, v25\n"
    "ds_read_b64_tr_b16 a[208:209], v165 offset:16896\n"
    "v_cvt_pk_bf16_f32 v20, v24, v25\n"
    "v_exp_f32 v26, v26\n"
    "ds_read_b64_tr_b16 a[210:211], v165 offset:20992\n"
    "v_exp_f32 v27, v27\n"
    "ds_read_b64_tr_b16 a[212:213], v165 offset:25088\n"
    "v_cvt_pk_bf16_f32 v21, v26, v27\n"
    "v_exp_f32 v28, v28\n"
    "ds_read_b64_tr_b16 a[214:215], v165 offset:29184\n"
    "v_exp_f32 v29, v29\n"
    "ds_read_b64_tr_b16 a[216:217], v166 offset:16896\n"
    "v_cvt_pk_bf16_f32 v22, v28, v29\n"
    "v_exp_f32 v30, v30\n"
    "s_waitcnt lgkmcnt(3)\n"
    "ds_read_b64_tr_b16 a[218:219], v166 offset:20992\n"
    "s_waitcnt lgkmcnt(0)\n"
    "s_branch Lent%=\n"
    "Lwarm%=:\n"
    "s_cmp_lt_u32 s20, 2\n"
    "s_cbranch_scc1 Lsw%=\n"
    "s_lshr_b32 s30, s24, 3\n"
    "s_lshl_b32 s30, s30, 2\n"
    "s_add_u32 s30, s30, s25\n"
    "s_lshl_b32 s31, s30, 13\n"
    "s_lshr_b32 s29, s26, 3\n"
    "s_lshl_b32 s28, s29, 20\n"
    "s_add_u32 s31, s31, s28\n"
    "s_add_u32 s34, s44, s31\n"
    "s_addc_u32 s35, s45, 0\n"
    "s_min_u32 s30, s30, 111\n"
    "s_lshl_b32 s30, s30, 14\n"
    "s_and_b32 s29, s29, 1\n"
    "s_lshl_b32 s29, s29, 7\n"
    "s_add_u32 s30, s30, s29\n"
    "s_add_u32 s30, s50, s30\n"
    "s_addc_u32 s31, s51, 0\n"
    "v_lshlrev_b32 v184, 7, v181\n"
    "v_lshrrev_b32 v187, 2, v181\n"
    "v_lshlrev_b32 v187, 10, v187\n"
    "v_and_b32 v188, 3, v181\n"
    "v_lshl_add_u32 v187, v188, 8, v187\n"
    "global_load_dword v185, v184, s[34:35]\n"
    "global_load_dword v186, v187, s[30:31]\n"
    "v_bfe_u32 v189, v181, 2, 3\n"
    "v_mul_u32_u24 v189, 0x300, v189\n"
    "v_add_u32 v189, v189, v171\n"
    "v_add_u32 v192, 0, v189\n"
    "v_add_u32 v193, -896, v189\n"
    "v_add_u32 v194, 6144, v189\n"
    "v_add_u32 v195, 5248, v189\n"
    "v_add_u32 v196, -1024, v189\n"
    "v_add_u32 v197, 6144, v189\n"
    "v_add_u32 v198, 13312, v189\n"
    "v_add_u32 v199, 20480, v189\n"
    "v_add_u32 v200, 129024, v180\n"
    "v_and_b32 v189, 0x7fff, v165\n"
    "s_and_b32 s28, s25, 1\n"
    "s_lshl_b32 s28, s28, 10\n"
    "v_add_u32 v201, s28, v189\n"
    "v_xor_b32 v202, 32, v201\n"
    "s_lshr_b32 s28, s28, 1\n"
    "v_lshrrev_b32 v190, 5, v181\n"
    "v_lshlrev_b32 v190, 10, v190\n"
    "v_sub_u32 v189, v189, v190\n"
    "v_add_u32 v207, s28, v189\n"
    "v_xor_b32 v208, 32, v207\n"
    "s_lshr_b32 s28, s25, 1\n"
    "s_lshl_b32 s28, s28, 13\n"
    "s_add_u32 s28, s28, 16384\n"
    "v_add_u32 v213, s28, v180\n"
    "v_add_u32 v203, 65536, v201\n"
    "v_add_u32 v204, 65536, v202\n"
    "v_add_u32 v209, 65536, v207\n"
    "v_add_u32 v210, 65536, v208\n"
    "v_add_u32 v214, 65536, v213\n"
    "v_add_u32 v205, 131072, v201\n"
    "v_add_u32 v206, 131072, v202\n"
    "v_add_u32 v211, 131072, v207\n"
    "v_add_u32 v212, 131072, v208\n"
    "v_add_u32 v215, 131072, v213\n"
    "v_and_b32 v189, 15, v181\n"
    "v_lshlrev_b32 v189, 10, v189\n"
    "v_lshrrev_b32 v190, 4, v181\n"
    "v_lshl_add_u32 v216, v190, 3, v189\n"
    "v_add_u32 v217, 16384, v216\n"
    "s_lshl_b32 s28, s27, 13\n"
    "v_and_b32 v189, 31, v181\n"
    "v_lshl_add_u32 v220, v189, 5, s28\n"
    "s_and_b32 s28, s92, 0xffffff00\n"
    "v_and_b32 v189, 7, v181\n"
    "v_lshl_add_u32 v218, v189, 5, s28\n"
    "s_and_b32 s28, s92, 0xfffffcff\n"
    "v_and_b32 v189, 3, v181\n"
    "v_lshl_add_u32 v219, v189, 8, s28\n"
    "s_lshr_b32 s29, s26, 3\n"
    "s_cmp_eq_u32 s25, s29\n"
    "s_cselect_b32 s97, 0x80000000, s97\n"
    "s_cselect_b32 s99, 0x80000000, s99\n"
    "s_add_u32 s28, s29, 1\n"
    "s_and_b32 s28, s28, 3\n"
    "s_lshl_b32 s28, s28, 13\n"
    "s_add_u32 s38, s98, s28\n"
    "s_add_u32 s28, s29, 2\n"
    "s_and_b32 s28, s28, 3\n"
    "s_lshl_b32 s28, s28, 13\n"
    "s_add_u32 s39, s98, s28\n"
    "s_add_u32 s28, s29, 3\n"
    "s_and_b32 s28, s28, 3\n"
    "s_lshl_b32 s28, s28, 13\n"
    "s_add_u32 s85, s98, s28\n"
    "v_add_u32 v173, 0, v180\n"
    "s_lshl_b32 s28, s25, 11\n"
    "v_add_u32 v174, s28, v173\n"
    "global_load_dwordx2 v[222:223], v220, s[82:83] offset:0 sc1\n"
    "s_branch Lwarmd%=\n"
    "Lsw%=:\n"
    "s_cmp_eq_u32 s20, 0\n"
    "s_cbranch_scc1 Lsw2%=\n"
    "s_mov_b32 s28, 0x40000\n"
    "Lpk_h%=:\n"
    "v_cmp_ne_u64 vcc, s[80:81], v[222:223]\n"
    "s_cmp_eq_u64 vcc, 0\n"
    "s_cbranch_scc1 Lpkd_h%=\n"
    "global_load_dwordx2 v[222:223], v220, s[82:83] offset:0 sc1\n"
    "s_waitcnt vmcnt(0)\n"
    "s_sub_u32 s28, s28, 1\n"
    "s_cbranch_scc0 Lpk_h%=\n"
    "Lpkd_h%=:\n"
    "s_or_b32 s28, s26, s25\n"
    "s_cbranch_scc1 Lnoeps%=\n"
    "v_mov_b32 v184, s92\n"
    "v_mov_b32 v186, s80\n"
    "v_mov_b32 v187, s81\n"
    "s_mov_b64 exec, 1\n"
    "global_store_dwordx2 v184, v[186:187], s[82:83] offset:-2048 sc1\n"
    "s_mov_b64 exec, -1\n"
    "Lnoeps%=:\n"
    "s_mov_b64 s[56:57], s[8:9]\n"
    "s_mov_b64 s[58:59], s[10:11]\n"
    "s_mov_b64 s[68:69], s[12:13]\n"
    "s_mov_b64 s[70:71], s[14:15]\n"
    "s_mov_b64 s[8:9], s[60:61]\n"
    "s_mov_b64 s[10:11], s[62:63]\n"
    "s_add_u32 s16, s46, 0\n"
    "s_add_u32 s28, s48, 0\n"
    "s_add_u32 s12, s72, s28\n"
    "s_addc_u32 s13, s73, 0\n"
    "s_sub_u32 s12, s12, s16\n"
    "s_subb_u32 s13, s13, 0\n"
    "s_add_u32 s14, s74, s16\n"
    "s_sub_u32 s14, s14, s28\n"
    "v_mov_b32 v167, v196\n"
    "v_mov_b32 v168, v197\n"
    "v_mov_b32 v169, v198\n"
    "v_mov_b32 v170, v199\n"
    "v_mov_b32 v171, v180\n"
    "v_mov_b32 v172, v180\n"
    "s_branch Lwarmd%=\n"
    "Lsw2%=:\n"
    "s_add_u32 s16, s46, 131072\n"
    "s_sub_u32 s12, s12, 126976\n"
    "s_subb_u32 s13, s13, 0\n"
    "s_add_u32 s14, s14, 126976\n"
    "s_branch Lwarmd%=\n"
    "Lend%=:\n"
    :: "s"(__builtin_amdgcn_kernarg_segment_ptr()),"s"(blockIdx.x),"s"(__builtin_amdgcn_readfirstlane(threadIdx.x>>6)),"s"((unsigned)(size_t)lds) : "v0","v1","v2","v3","v4","v5","v6","v7","v8","v9","v10","v11","v12","v13","v14","v15","v16","v17","v18","v19","v20","v21","v22","v23","v24","v25","v26","v27","v28","v29","v30","v31","v32","v33","v34","v35","v36","v37","v38","v39","v40","v41","v42","v43","v44","v45","v46","v47","v48","v49","v50","v51","v52","v53","v54","v55","v56","v57","v58","v59","v60","v61","v62","v63","v64","v65","v66","v67","v68","v69","v70","v71","v72","v73","v74","v75","v76","v77","v78","v79","v80","v81","v82","v83","v84","v85","v86","v87","v88","v89","v90","v91","v92","v93","v94","v95","v96","v97","v98","v99","v100","v101","v102","v103","v104","v105","v106","v107","v108","v109","v110","v111","v112","v113","v114","v115","v116","v117","v118","v119","v120","v121","v122","v123","v124","v125","v126","v127","v128","v129","v130","v131","v132","v133","v134","v135","v136","v137","v138","v139","v140","v141","v142","v143","v144","v145","v146","v147","v148","v149","v150","v151","v152","v153","v154","v155","v156","v157","v158","v159","v160","v161","v162","v163","v164","v165","v166","v167","v168","v169","v170","v171","v172","v173","v174","v175","v176","v177","v178","v179","v180","v181","v182","v183","v184","v185","v186","v187","v188","v189","v190","v191","v192","v193","v194","v195","v196","v197","v198","v199","v200","v201","v202","v203","v204","v205","v206","v207","v208","v209","v210","v211","v212","v213","v214","v215","v216","v217","v218","v219","v220","v221","v222","v223","v224","v225","v226","v227","v228","v229","v230","v231","v232","v233","v234","v235","v236","v237","v238","v239","v240","v241","v242","v243","v244","v245","v246","v247","v248","v249","v250","v251","v252","v253","v254","v255","a0","a1","a2","a3","a4","a5","a6","a7","a8","a9","a10","a11","a12","a13","a14","a15","a16","a17","a18","a19","a20","a21","a22","a23","a24","a25","a26","a27","a28","a29","a30","a31","a32","a33","a34","a35","a36","a37","a38","a39","a40","a41","a42","a43","a44","a45","a46","a47","a48","a49","a50","a51","a52","a53","a54","a55","a56","a57","a58","a59","a60","a61","a62","a63","a64","a65","a66","a67","a68","a69","a70","a71","a72","a73","a74","a75","a76","a77","a78","a79","a80","a81","a82","a83","a84","a85","a86","a87","a88","a89","a90","a91","a92","a93","a94","a95","a96","a97","a98","a99","a100","a101","a102","a103","a104","a105","a106","a107","a108","a109","a110","a111","a112","a113","a114","a115","a116","a117","a118","a119","a120","a121","a122","a123","a124","a125","a126","a127","a128","a129","a130","a131","a132","a133","a134","a135","a136","a137","a138","a139","a140","a141","a142","a143","a144","a145","a146","a147","a148","a149","a150","a151","a152","a153","a154","a155","a156","a157","a158","a159","a160","a161","a162","a163","a164","a165","a166","a167","a168","a169","a170","a171","a172","a173","a174","a175","a176","a177","a178","a179","a180","a181","a182","a183","a184","a185","a186","a187","a188","a189","a190","a191","a192","a193","a194","a195","a196","a197","a198","a199","a200","a201","a202","a203","a204","a205","a206","a207","a208","a209","a210","a211","a212","a213","a214","a215","a216","a217","a218","a219","a220","a221","a222","a223","a224","a225","a226","a227","a228","a229","a230","a231","a232","a233","a234","a235","a236","a237","a238","a239","a240","a241","a242","a243","a244","a245","a246","a247","a248","a249","a250","a251","a252","a253","a254","a255","s8","s9","s10","s11","s12","s13","s14","s15","s16","s17","s18","s19","s20","s21","s22","s23","s24","s25","s26","s27","s28","s29","s30","s31","s33","s34","s35","s36","s37","s38","s39","s40","s41","s42","s43","s44","s45","s46","s47","s48","s49","s50","s51","s52","s53","s54","s55","s56","s57","s58","s59","s60","s61","s62","s63","s64","s65","s66","s67","s68","s69","s70","s71","s72","s73","s74","s75","s76","s77","s78","s79","s80","s81","s82","s83","s84","s85","s86","s87","s88","s89","s90","s91","s92","s93","s94","s95","s96","s97","s98","s99","vcc","scc","memory");
}
extern "C" void kernel_launch(void* const* d_in, const int* in_sizes, int n_in, void* d_out, int out_size, void* d_ws, size_t ws_size, hipStream_t stream) {
  hipLaunchKernelGGL(fused_block, dim3(256), dim3(256), 0, stream, d_in[0], d_in[1], d_in[2], d_in[3], d_in[4], d_in[6], d_in[7], d_in[8], d_out, d_ws);
}
```
